```python
import torch

def _softmax(x, axis=-1):
    return torch.softmax(x, dim=axis)
def _gelu(x):
    return torch.nn.functional.gelu(x, approximate="tanh")
def _silu(x):
    return torch.nn.functional.silu(x)
def _sigmoid(x):
    return torch.sigmoid(x)
def _layernorm(x, axis=-1, eps=1e-5):
    m = x.mean(dim=axis, keepdim=True)
    v = x.var(dim=axis, keepdim=True, unbiased=False)
    return (x - m) / torch.sqrt(v + eps)
def _scatter_add(tgt, idx, src, axis=0):
    return tgt.clone().index_add_(axis, idx, src)
def _segment_sum(src, seg, n):
    out = torch.zeros((n,) + src.shape[1:], dtype=src.dtype)
    return out.index_add_(0, seg, src)
def _pad(x, pw):
    flat = tuple(v for pair in reversed(pw) for v in pair)
    return torch.nn.functional.pad(x, flat)

kernel_name = 'concat_heads__gqa_block_cached__gqa_block_cached__near_ridge'

def setup_inputs():

    _w = (lambda a: (lambda t: t.to(torch.bfloat16) if t.is_floating_point() else t)(torch.as_tensor(a)))
    def _A_setup():
        B, Sq, D = 16, 112, 576
        Hq, Hkv, Sk, Dh = 32, 16, 512, 128
        assert Hq % Hkv == 0, (Hq, Hkv)
        return {
            "x": _w((lambda *s: torch.randn(*s, dtype=torch.bfloat16) * 0.8)(B, Sq, D)),
            "k_cache": _w((lambda *s: torch.randn(*s, dtype=torch.bfloat16) * 0.8)(B, Hkv, Sk, Dh)),
            "v_cache": _w((lambda *s: torch.randn(*s, dtype=torch.bfloat16) * 0.05)(B, Hkv, Sk, Dh)),
            "wq": _w((lambda *s: torch.randn(*s, dtype=torch.bfloat16) * 0.8)(D, Hq * Dh) * (D ** -0.5)),
            "wo": _w((lambda *s: torch.randn(*s, dtype=torch.bfloat16) * 0.05)(Hq * Dh, D) * ((Hq * Dh) ** -0.5)),
        }
    _A_in = _A_setup()

    def _B_setup():
        B, Sq, D = 16, 112, 576
        Hq, Hkv, Sk, Dh = 32, 16, 512, 128
        assert Hq % Hkv == 0, (Hq, Hkv)
        return {
            "x": _w((lambda *s: torch.randn(*s, dtype=torch.bfloat16) * 0.8)(B, Sq, D)),
            "k_cache": _w((lambda *s: torch.randn(*s, dtype=torch.bfloat16) * 0.8)(B, Hkv, Sk, Dh)),
            "v_cache": _w((lambda *s: torch.randn(*s, dtype=torch.bfloat16) * 0.05)(B, Hkv, Sk, Dh)),
            "wq": _w((lambda *s: torch.randn(*s, dtype=torch.bfloat16) * 0.8)(D, Hq * Dh) * (D ** -0.5)),
            "wo": _w((lambda *s: torch.randn(*s, dtype=torch.bfloat16) * 0.05)(Hq * Dh, D) * ((Hq * Dh) ** -0.5)),
        }
    _B_in = _B_setup()

    return {
        "A_x": _A_in["x"],
        "A_k_cache": _A_in["k_cache"],
        "A_v_cache": _A_in["v_cache"],
        "A_wq": _A_in["wq"],
        "A_wo": _A_in["wo"],
        "B_x": _B_in["x"],
        "B_k_cache": _B_in["k_cache"],
        "B_v_cache": _B_in["v_cache"],
        "B_wq": _B_in["wq"],
        "B_wo": _B_in["wo"],
    }

def reference(A_x, A_k_cache, A_v_cache, A_wq, A_wo, B_x, B_k_cache, B_v_cache, B_wq, B_wo):
    def _A_ref(x, k_cache, v_cache, wq, wo):
        B, Sq, D = x.shape
        Hq, Dh = 32, 128
        Hkv = k_cache.shape[1]
        rep = Hq // Hkv
        q = (lambda x, *d: x.permute(*d))((x @ wq).reshape(B, Sq, Hq, Dh), 0, 2, 1, 3)
        k = (lambda x, n, axis: torch.repeat_interleave(x, n, dim=axis))(k_cache, rep, axis=1)
        v = (lambda x, n, axis: torch.repeat_interleave(x, n, dim=axis))(v_cache, rep, axis=1)
        scores = torch.einsum("bhqd,bhkd->bhqk", q, k) * (Dh ** -0.5)
        p = _softmax(scores, axis=-1)
        o = torch.einsum("bhqk,bhkd->bhqd", p, v)
        return (lambda x, *d: x.permute(*d))(o, 0, 2, 1, 3).reshape(B, Sq, Hq * Dh) @ wo

    def _B_ref(x, k_cache, v_cache, wq, wo):
        B, Sq, D = x.shape
        Hq, Dh = 32, 128
        Hkv = k_cache.shape[1]
        rep = Hq // Hkv
        q = (lambda x, *d: x.permute(*d))((x @ wq).reshape(B, Sq, Hq, Dh), 0, 2, 1, 3)
        k = (lambda x, n, axis: torch.repeat_interleave(x, n, dim=axis))(k_cache, rep, axis=1)
        v = (lambda x, n, axis: torch.repeat_interleave(x, n, dim=axis))(v_cache, rep, axis=1)
        scores = torch.einsum("bhqd,bhkd->bhqk", q, k) * (Dh ** -0.5)
        p = _softmax(scores, axis=-1)
        o = torch.einsum("bhqk,bhkd->bhqd", p, v)
        return (lambda x, *d: x.permute(*d))(o, 0, 2, 1, 3).reshape(B, Sq, Hq * Dh) @ wo

    a = _A_ref(A_x, A_k_cache, A_v_cache, A_wq, A_wo)
    b = _B_ref(B_x, B_k_cache, B_v_cache, B_wq, B_wo)
    return torch.concatenate([a, b], axis=-1)
```

```cpp
#include <hip/hip_runtime.h>
#include <hip/hip_bf16.h>

typedef __attribute__((ext_vector_type(8))) __bf16 bf16x8;
typedef __attribute__((ext_vector_type(4))) __bf16 bf16x4;
typedef __attribute__((ext_vector_type(2))) __bf16 bf16x2;
typedef __attribute__((ext_vector_type(4))) float f32x4;
typedef __attribute__((ext_vector_type(2))) float f32x2;
typedef __attribute__((ext_vector_type(4))) unsigned u32x4;
typedef __attribute__((address_space(3))) bf16x4 lds_bf16x4;
typedef __attribute__((address_space(3))) void lds_void;
typedef __attribute__((address_space(1))) void glb_void;

constexpr int NB = 16, SQ = 112, DM = 576, HQ = 32, HKV = 16, SK = 512, DH = 128;

__device__ __forceinline__ unsigned cvtpk(float lo, float hi) {
  f32x2 v = {lo, hi}; bf16x2 b = __builtin_convertvector(v, bf16x2); return __builtin_bit_cast(unsigned, b);
}
__device__ __forceinline__ void glds16(const void* g, void* l) {
  __builtin_amdgcn_global_load_lds((const glb_void*)g, (lds_void*)l, 16, 0, 0);
}
__device__ __forceinline__ bf16x4 trread(const char* p) {
  return __builtin_amdgcn_ds_read_tr16_b64_v4bf16((lds_bf16x4*)p);
}
__device__ __forceinline__ bf16x8 cat(bf16x4 a, bf16x4 b) { return __builtin_shufflevector(a, b, 0, 1, 2, 3, 4, 5, 6, 7); }

struct Ptrs { const unsigned short* x[2]; const unsigned short* k[2]; const unsigned short* v[2]; const unsigned short* wq[2]; const unsigned short* wo[2]; };

__global__ __launch_bounds__(256, 1) void attn_kernel(Ptrs P, unsigned short* __restrict__ Of) {
  extern __shared__ __attribute__((aligned(16))) char lds[];
  const int tid = threadIdx.x, lane = tid & 63, w = __builtin_amdgcn_readfirstlane(tid >> 6);
  const int bid = blockIdx.x, xcd = bid & 7, idx = bid >> 3;
  const int p = xcd >> 2, g = (xcd & 3) * 4 + (idx & 3), bp = idx >> 2;
  const int bl = w >> 1, hl = w & 1;
  const int i16 = lane & 15, gq = lane >> 4, q4 = (lane >> 2) & 3, p4 = lane & 3;
  const unsigned short* X = P.x[p] + (size_t)(2 * bp) * SQ * DM;
  const unsigned short* WQ = P.wq[p] + g * 256;
  char* xs = lds;
  char* ws = lds + 28672;
  f32x4 acc[8][7];
#pragma unroll
  for (int a = 0; a < 8; ++a)
#pragma unroll
    for (int r = 0; r < 7; ++r) acc[a][r] = f32x4{0, 0, 0, 0};
  for (int kc = 0; kc < 9; ++kc) {
    __syncthreads();
#pragma unroll
    for (int i = 0; i < 7; ++i) { int pc = w * 7 + i; int row = pc * 8 + (lane >> 3); int ch = (lane & 7) ^ ((row >> 1) & 7);
      glds16(X + (size_t)row * DM + kc * 64 + ch * 8, xs + pc * 1024); }
#pragma unroll
    for (int i = 0; i < 8; ++i) { int kh = w >> 1, hd = w & 1; int k = i * 4 + (lane >> 4); int ch = (lane & 15) ^ ((k & 3) << 2);
      glds16(WQ + (size_t)(kc * 64 + kh * 32 + k) * 4096 + hd * 128 + ch * 8, ws + (kh * 2 + hd) * 8192 + i * 1024); }
    __syncthreads();
#pragma unroll
    for (int kh = 0; kh < 2; ++kh) {
      bf16x8 xb[7];
#pragma unroll
      for (int r = 0; r < 7; ++r) xb[r] = *(const bf16x8*)(xs + (bl * 112 + r * 16 + i16) * 128 + 16 * ((4 * kh + gq) ^ (i16 >> 1)));
#pragma unroll
      for (int a = 0; a < 8; ++a) { int t = a >> 1, u = a & 1;
        const char* wb = ws + (kh * 2 + hl) * 8192 + (8 * gq + q4) * 256 + 64 * (t ^ q4) + 16 * p4 + 8 * u;
        bf16x8 wa = cat(trread(wb), trread(wb + 1024));
#pragma unroll
        for (int r = 0; r < 7; ++r) acc[a][r] = __builtin_amdgcn_mfma_f32_16x16x32_bf16(wa, xb[r], acc[a][r], 0, 0, 0);
      }
    }
  }
  bf16x8 qf[7][4];
#pragma unroll
  for (int r = 0; r < 7; ++r)
#pragma unroll
    for (int t = 0; t < 4; ++t) {
      u32x4 u = {cvtpk(acc[2 * t][r][0], acc[2 * t][r][1]), cvtpk(acc[2 * t][r][2], acc[2 * t][r][3]),
                 cvtpk(acc[2 * t + 1][r][0], acc[2 * t + 1][r][1]), cvtpk(acc[2 * t + 1][r][2], acc[2 * t + 1][r][3])};
      qf[r][t] = __builtin_bit_cast(bf16x8, u);
    }
#pragma unroll
  for (int a = 0; a < 8; ++a)
#pragma unroll
    for (int r = 0; r < 7; ++r) acc[a][r] = f32x4{0, 0, 0, 0};
  float m[7], l[7];
#pragma unroll
  for (int r = 0; r < 7; ++r) { m[r] = -1e30f; l[r] = 0.f; }
  const float C = 0.08838834764831845f * 1.4426950408889634f;
  const unsigned short* src = (hl ? P.v[p] : P.k[p]) + ((size_t)(2 * bp + bl) * HKV + g) * SK * DH;
  char* ks = lds + bl * 16384, *vs = ks + 8192;
  for (int T = 0; T < 16; ++T) {
    __syncthreads();
#pragma unroll
    for (int i = 0; i < 8; ++i) { int key = i * 4 + (lane >> 4); int ch = hl ? ((lane & 15) ^ ((key & 7) << 1)) : ((lane & 15) ^ (key & 15));
      glds16(src + (size_t)(T * 32 + key) * DH + ch * 8, ks + hl * 8192 + i * 1024); }
    __syncthreads();
    bf16x8 kf[2][4], vf[8];
#pragma unroll
    for (int kb = 0; kb < 2; ++kb)
#pragma unroll
      for (int t = 0; t < 4; ++t) kf[kb][t] = *(const bf16x8*)(ks + (16 * kb + i16) * 256 + 16 * ((4 * t + gq) ^ i16));
#pragma unroll
    for (int c = 0; c < 8; ++c) { int key = 4 * gq + q4; const char* vb = vs + key * 256 + 32 * (c ^ (key & 7)) + 8 * p4;
      vf[c] = cat(trread(vb), trread(vb + 4096)); }
#pragma unroll
    for (int r = 0; r < 7; ++r) {
      f32x4 s0 = {0, 0, 0, 0}, s1 = {0, 0, 0, 0};
#pragma unroll
      for (int t = 0; t < 4; ++t) { s0 = __builtin_amdgcn_mfma_f32_16x16x32_bf16(kf[0][t], qf[r][t], s0, 0, 0, 0);
                                    s1 = __builtin_amdgcn_mfma_f32_16x16x32_bf16(kf[1][t], qf[r][t], s1, 0, 0, 0); }
      if (T == 0) {
      float mx = fmaxf(fmaxf(fmaxf(s0[0], s0[1]), fmaxf(s0[2], s0[3])), fmaxf(fmaxf(s1[0], s1[1]), fmaxf(s1[2], s1[3])));
      mx = fmaxf(mx, __shfl_xor(mx, 16)); mx = fmaxf(mx, __shfl_xor(mx, 32)); m[r] = mx * C; }
      float mn = m[r];
      float e[8];
#pragma unroll
      for (int j = 0; j < 4; ++j) { e[j] = __builtin_amdgcn_exp2f(s0[j] * C - mn); e[4 + j] = __builtin_amdgcn_exp2f(s1[j] * C - mn); }
      l[r] = l[r] + ((e[0] + e[1]) + (e[2] + e[3])) + ((e[4] + e[5]) + (e[6] + e[7]));
      u32x4 u = {cvtpk(e[0], e[1]), cvtpk(e[2], e[3]), cvtpk(e[4], e[5]), cvtpk(e[6], e[7])};
      bf16x8 pf = __builtin_bit_cast(bf16x8, u);
#pragma unroll
      for (int c = 0; c < 8; ++c) { acc[c][r] = __builtin_amdgcn_mfma_f32_16x16x32_bf16(vf[c], pf, acc[c][r], 0, 0, 0); }
    }
  }
  const int b = 2 * bp + bl, h = 2 * g + hl;
#pragma unroll
  for (int r = 0; r < 7; ++r) {
    float ls = l[r]; ls += __shfl_xor(ls, 16); ls += __shfl_xor(ls, 32);
    float inv = 1.0f / ls;
#pragma unroll
    for (int t = 0; t < 4; ++t) {
      f32x4 a = acc[2 * t][r] * inv, c2 = acc[2 * t + 1][r] * inv;
      u32x4 u = {cvtpk(a[0], a[1]), cvtpk(a[2], a[3]), cvtpk(c2[0], c2[1]), cvtpk(c2[2], c2[3])};
      *(u32x4*)(Of + ((((size_t)p * 112 + b * 7 + r) * 128 + h * 4 + t) * 64 + lane) * 8) = u;
    }
  }
}

__global__ __launch_bounds__(256, 1) void oproj_kernel(Ptrs P, const unsigned short* __restrict__ Of, unsigned short* __restrict__ out) {
  extern __shared__ __attribute__((aligned(16))) char lds[];
  const int tid = threadIdx.x, lane = tid & 63, w = __builtin_amdgcn_readfirstlane(tid >> 6);
  const int bid = blockIdx.x; const int p = bid / 126, tl = bid % 126, tm = tl / 9, tn = tl % 9;
  const int i16 = lane & 15, gq = lane >> 4, q4 = (lane >> 2) & 3, p4 = lane & 3;
  const unsigned short* WO = P.wo[p] + tn * 64;
  const unsigned short* Ob = Of + (((size_t)p * 112 + tm * 8 + 2 * w) * 128 * 64 + lane) * 8;
  f32x4 acc[4][2];
#pragma unroll
  for (int a = 0; a < 4; ++a) { acc[a][0] = f32x4{0, 0, 0, 0}; acc[a][1] = f32x4{0, 0, 0, 0}; }
  for (int kc = 0; kc < 32; ++kc) {
    __syncthreads();
#pragma unroll
    for (int i = 0; i < 4; ++i) { int pc = w * 4 + i; int k = pc * 8 + (lane >> 3); int ch = (lane & 7) ^ (((k >> 1) & 3) << 1);
      glds16(WO + (size_t)(kc * 128 + k) * DM + ch * 8, lds + pc * 1024); }
    bf16x8 of[4][2];
#pragma unroll
    for (int t = 0; t < 4; ++t)
#pragma unroll
      for (int r = 0; r < 2; ++r) of[t][r] = *(const bf16x8*)(Ob + ((size_t)r * 128 + kc * 4 + t) * 512);
    __syncthreads();
#pragma unroll
    for (int t = 0; t < 4; ++t)
#pragma unroll
      for (int c = 0; c < 4; ++c) { int k = 32 * t + 4 * gq + q4;
        const char* wb = lds + k * 128 + 32 * (c ^ ((k >> 1) & 3)) + 8 * p4;
        bf16x8 wa = cat(trread(wb), trread(wb + 2048));
        acc[c][0] = __builtin_amdgcn_mfma_f32_16x16x32_bf16(wa, of[t][0], acc[c][0], 0, 0, 0);
        acc[c][1] = __builtin_amdgcn_mfma_f32_16x16x32_bf16(wa, of[t][1], acc[c][1], 0, 0, 0);
      }
  }
#pragma unroll
  for (int r = 0; r < 2; ++r) { int row = tm * 128 + (2 * w + r) * 16 + i16;
#pragma unroll
    for (int c = 0; c < 4; ++c) { uint2 u = {cvtpk(acc[c][r][0], acc[c][r][1]), cvtpk(acc[c][r][2], acc[c][r][3])};
      *(uint2*)(out + (size_t)row * 1152 + p * 576 + tn * 64 + c * 16 + 4 * gq) = u; } }
}

extern "C" void kernel_launch(void* const* d_in, const int* in_sizes, int n_in, void* d_out, int out_size, void* d_ws, size_t ws_size, hipStream_t stream) {
  Ptrs P;
  for (int p = 0; p < 2; ++p) {
    P.x[p] = (const unsigned short*)d_in[5 * p + 0]; P.k[p] = (const unsigned short*)d_in[5 * p + 1]; P.v[p] = (const unsigned short*)d_in[5 * p + 2];
    P.wq[p] = (const unsigned short*)d_in[5 * p + 3]; P.wo[p] = (const unsigned short*)d_in[5 * p + 4];
  }
  unsigned short* Of = (unsigned short*)d_ws;
  hipFuncSetAttribute((const void*)attn_kernel, hipFuncAttributeMaxDynamicSharedMemorySize, 65536);
  attn_kernel<<<256, 256, 61440, stream>>>(P, Of);
  oproj_kernel<<<252, 256, 16384, stream>>>(P, Of, (unsigned short*)d_out);
}
```

```cpp
#include <hip/hip_runtime.h>
#include <hip/hip_bf16.h>

typedef __attribute__((ext_vector_type(2))) unsigned u32x2;
typedef __attribute__((ext_vector_type(4))) unsigned u32x4;
typedef __attribute__((ext_vector_type(8))) unsigned u32x8;
typedef __attribute__((ext_vector_type(16))) unsigned u32x16;

__device__ __forceinline__ unsigned rfl(unsigned x) { return __builtin_amdgcn_readfirstlane(x); }

#define NROT 8u
#define ROTF ((b + (pair >> 1) + ((NROT + 1u) / 2u) * (pair & 1u)) % NROT)
struct Args { const void* a[2][8]; };
constexpr unsigned GSTRIDE = 4096u + 9u * 131072u;
constexpr unsigned OOR = 0x40000000u;

__global__ __launch_bounds__(256, 1) void fused_kernel(Args A) {
  extern __shared__ __attribute__((aligned(16))) char lds[];
  unsigned tid = threadIdx.x, bid = blockIdx.x;
  u32x4 pW, pX; u32x2 pKV, pK, pWO, pPB, pOUT, pD;
  asm volatile("s_lshl_b32 s36, %9, 4\n s_and_b32 s36, s36, 64\n v_readfirstlane_b32 s37, %8\n"
               "s_load_dwordx2 s[44:45], %10, s36 offset:0\n s_load_dwordx2 s[40:41], %10, s36 offset:8\n s_load_dwordx2 %3, %10, s36 offset:16\n"
               "s_load_dwordx2 %4, %10, s36 offset:32\n s_load_dwordx2 %5, %10, s36 offset:40\n s_load_dwordx2 %6, %10, s36 offset:48\n"
               "s_load_dwordx2 %7, %10, s36 offset:56\n"
               "s_lshr_b32 s37, s37, 3\n s_and_b32 s37, s37, 8\n s_add_u32 s36, s36, s37\n s_load_dwordx2 %2, %10, s36 offset:16\n"
               "v_readfirstlane_b32 s68, %8\n"
               "v_and_b32 v40, 63, %8\n"
               "v_bfe_u32 v41, %8, 4, 2\n"
               "v_lshrrev_b32 v42, 3, v40\n"
               "v_mul_u32_u24 v42, 1152, v42\n"
               "v_and_b32 v43, 7, v40\n"
               "v_xor_b32 v43, v43, v41\n"
               "v_lshl_add_u32 v43, v43, 4, v42\n"
               "v_xor_b32 v44, 64, v43\n"
               "v_and_b32 v45, 15, v40\n"
               "v_lshlrev_b32 v42, 2, v41\n"
               "v_xor_b32 v45, v45, v42\n"
               "v_lshlrev_b32 v45, 4, v45\n"
               "v_lshl_add_u32 v45, v41, 13, v45\n"
               "v_xor_b32 v46, 0, v45\n"
               "s_lshr_b32 s68, s68, 6\n"
               "s_lshr_b32 s69, %9, 4\n"
               "s_mul_i32 s47, s69, 129024\n"
               "s_mov_b32 s46, 129024\n"
               "s_and_b32 s69, %9, 3\n"
               "s_lshl_b32 s69, s69, 3\n"
               "s_lshr_b32 s70, %9, 1\n"
               "s_and_b32 s70, s70, 4\n"
               "s_or_b32 s69, s69, s70\n"
               "s_add_u32 s69, s69, s68\n"
               "s_lshl_b32 s43, s69, 8\n"
               "s_sub_u32 s42, 4718592, s43\n"
               "s_lshr_b32 s69, s68, 1\n"
               "s_mul_i32 s69, s69, 40960\n"
               "s_and_b32 s70, s68, 1\n"
               "s_lshl_b32 s70, s70, 12\n"
               "s_add_u32 s69, s69, s70\n"
               "s_add_u32 s69, s69, %11\n"
               "s_add_u32 m0, s69, 32768\n"
               "s_mul_i32 s71, s68, 36864\n"
               "s_mul_i32 s70, s68, 40960\n"
               "s_add_u32 s70, s70, %11\n"
               "s_waitcnt lgkmcnt(0)\n"
               "s_add_u32 s44, s44, s47\n"
               "s_addc_u32 s45, s45, 0\n"
               "s_and_b32 s45, s45, 65535\n"
               "s_mov_b32 s47, 131072\n"
               "buffer_load_dwordx4 v43, s[44:47], s71 offen offset:0 lds\n"
               "s_add_u32 s71, s71, 8192\n"
               "buffer_load_dwordx4 v44, s[44:47], s71 offen offset:1024 lds\n"
               "s_add_u32 s71, s71, 8192\n"
               "buffer_load_dwordx4 v43, s[44:47], s71 offen offset:2048 lds\n"
               "s_add_u32 s71, s71, 8192\n"
               "buffer_load_dwordx4 v44, s[44:47], s71 offen offset:3072 lds\n"
               "s_add_u32 s71, s71, 8192\n"
               "s_add_u32 s40, s40, s43\n"
               "s_addc_u32 s41, s41, 0\n"
               "s_and_b32 s41, s41, 65535\n"
               "s_mov_b32 s43, 131072\n"
               "s_mov_b32 s71, 0\n"
               "s_add_u32 m0, s70, 16384\n"
               "s_nop 0\n"
               "buffer_load_dwordx4 v45, s[40:43], s71 offen offset:0 lds\n"
               "s_add_u32 s71, s71, 31744\n"
               "buffer_load_dwordx4 v45, s[40:43], s71 offen offset:1024 lds\n"
               "s_add_u32 s71, s71, 31744\n"
               "buffer_load_dwordx4 v46, s[40:43], s71 offen offset:2048 lds\n"
               "s_add_u32 s71, s71, 31744\n"
               "buffer_load_dwordx4 v46, s[40:43], s71 offen offset:3072 lds\n"
               "s_add_u32 s71, s71, 35840\n"
               "s_add_u32 m0, s70, 20480\n"
               "s_nop 0\n"
               "buffer_load_dwordx4 v45, s[40:43], s71 offen offset:0 lds\n"
               "s_add_u32 s71, s71, 31744\n"
               "buffer_load_dwordx4 v45, s[40:43], s71 offen offset:1024 lds\n"
               "s_add_u32 s71, s71, 31744\n"
               "buffer_load_dwordx4 v46, s[40:43], s71 offen offset:2048 lds\n"
               "s_add_u32 s71, s71, 31744\n"
               "buffer_load_dwordx4 v46, s[40:43], s71 offen offset:3072 lds\n"
               "s_add_u32 s71, s71, 35840\n"
               : "=&{s[40:43]}"(pW), "=&{s[44:47]}"(pX), "=&{s[48:49]}"(pKV), "=&{s[56:57]}"(pK), "=&{s[8:9]}"(pWO), "=&{s[12:13]}"(pPB), "=&{s[16:17]}"(pOUT), "=&{s[66:67]}"(pD)
               : "v"(tid), "s"(bid), "s"(__builtin_amdgcn_kernarg_segment_ptr()), "s"((unsigned)(size_t)lds)
               : "s36", "s37", "s68", "s69", "s70", "s71", "s72", "s73", "s74", "s75", "s76", "a200", "v40", "v41", "v42", "v43", "v44", "v45", "v46", "scc", "memory");
  asm volatile("" : "+v"(tid));
  asm volatile("" : "+s"(bid));
  const unsigned lane = tid & 63, w = rfl(tid >> 6);
  const unsigned xcd = bid & 7, idx = bid >> 3;
  const unsigned p = xcd >> 2, pair = (xcd & 3) * 2 + (idx & 1), b = idx >> 1;
  const unsigned kvl = w >> 1, isv = w & 1, g = 2 * pair + kvl;
  const unsigned i16 = lane & 15, gq = lane >> 4, q4 = (lane >> 2) & 3, p4 = lane & 3;
  const unsigned ldsb = (unsigned)(size_t)lds;
  const unsigned RBW = ldsb + w * 40960u;
  u32x16 va, vb, pk;
  const unsigned sg = gq;
  va[0] = lane * 16;
  va[1] = gq * 8192u + 16u * (i16 ^ (4u * gq));
  va[2] = va[1];
  va[3] = (lane >> 3) * 1152u + 16u * ((lane & 7) ^ gq);
  va[4] = va[3] ^ 64u;
#pragma unroll
  for (unsigned j = 0; j < 4; ++j) va[5 + j] = gq * 256u + 16u * (i16 ^ (isv ? (8u * (j & 1) + 2u * gq) : (4u * j + gq)));
  va[9] = RBW + lane * 16u;
  va[10] = ldsb + 32768u + i16 * 128u + 16u * (gq ^ (i16 >> 1));
  va[11] = va[10] ^ 64u;
#pragma unroll
  for (unsigned c = 0; c < 8; ++c) {
    const unsigned a = RBW + (8u * gq + q4) * 256u + 64u * ((c & 3u) ^ q4) + 16u * p4;
    if (c < 4) va[12 + c] = a; else vb[c - 4] = a;
  }
#pragma unroll
  for (unsigned t = 0; t < 4; ++t) vb[4 + t] = ldsb + kvl * 81920u + i16 * 256u + 16u * ((4u * t + sg) ^ i16);
#pragma unroll
  for (unsigned c = 0; c < 8; ++c) vb[8 + c] = ldsb + kvl * 81920u + 40960u + (4u * gq + q4) * 256u + 32u * (c ^ ((4u * gq + q4) & 7u)) + 8u * p4;
  pk[0] = (lane >> 3) * 1152u + 16u * ((lane & 7) ^ (gq << 1));
  pk[1] = ldsb + 8192u + (4u * gq + q4) * 128u + 32u * (w ^ (((4u * gq + q4) >> 1) & 3u)) + 8u * p4;
  pk[2] = ldsb + lane * 16u;
  pk[3] = i16 * 2304u + (gq & 1u) * 32u + (gq >> 1) * 16u;
  const unsigned one = (q4 == gq) ? (0x3f80u << (16u * (p4 & 1u))) : 0u;
  pk[4] = (p4 < 2) ? one : 0u;
  pk[5] = (p4 < 2) ? 0u : one;
  pk[6] = (w == 0 ? 8u : ((pair + w) & 7u)) * 128u; pk[7] = 0;
  pk[8] = 4096u + (pair * 4u + w) * 4096u;
  pk[9] = 4096u + pair * 131072u + w * 4096u;
  pk[10] = 4096u + 8u * 131072u + w * 4096u + (pair < 7u ? pair : 6u) * 512u;
  pk[12] = pair * 128u + w * 73728u;
  pk[11] = w < 3u ? pk[12] + 36864u : pair * 36864u + 1024u;
  pk[13] = ((idx * 4u + w) & 31u) * 1024u;
  pk[14] = (b * 4u + w) * 9216u; pk[15] = w * 2048u;
  const u32x2 qKV = {131072u, (b * 16u + g) * 131072u}, qK = qKV;
  const u32x2 qWO = {589824u, pair * 589824u}, qPB = {GSTRIDE, (p * 16u + b) * GSTRIDE}, qOUT = {258048u - p * 1152u, b * 258048u + p * 1152u};
  u32x4 lw, sj, sm; u32x2 sn;
  lw[0] = ((pair + 2u + isv) & 7u) * 128u + kvl * 73728u;
  lw[1] = ((pair + 4u + isv) & 7u) * 128u + kvl * 73728u;
  lw[2] = ((pair + 6u + isv) & 7u) * 128u + kvl * 73728u;
  lw[3] = pair * 128u + isv * 73728u + kvl * 147456u;
  sj[0] = pair; sj[1] = (isv ? ((pair + 1u) & 7u) : 8u) * 128u + kvl * 147456u; sj[2] = w == 0 ? pair * 8u : OOR; sj[3] = (w == 0 && pair == 0) ? 0u : OOR;
  sm[0] = RBW; sm[1] = ldsb + 32768u + kvl * 40960u + isv * 4096u; sm[2] = w * 4u * 9216u;
  sm[3] = g * 8192u;
  sn[0] = (240u << 20) + (bid * 4u + w) * 64u; sn[1] = ((idx * 4u + w) & 63u) | (ROTF << 8);
  asm volatile(
    "s_memrealtime s[38:39]\n"
    "s_lshr_b32 s78, s65, 8\n"
    "s_and_b32 s82, s65, 63\n"
    "s_lshl_b32 s76, s78, 19\n"
    "s_lshl_b32 s78, s78, 7\n"
    "s_mov_b32 s70, 0\n"
    "s_mov_b32 s77, 0\n"
    "s_getpc_b64 s[84:85]\nLBEG_%=:\n"
    "s_and_b32 s85, s85, 65535\n"
    "s_mov_b32 s86, LEND_%=-LBEG_%=\n"
    "s_mov_b32 s87, 131072\n"
    "v_lshlrev_b32 v228, 3, v0\n"
    "v_min_u32 v228, 896, v228\n"
    "s_lshl_b32 s83, s82, 10\n"
    "buffer_load_dword a200, v228, s[84:87], s83 offen offset:0\n"
    "s_mov_b32 s70, 262144\n"
    "s_add_u32 s77, s77, 128\n"
    "s_add_u32 s77, s77, s78\n"
    "s_add_u32 s48, s48, s51\n"
    "s_addc_u32 s49, s49, 0\n"
    "s_and_b32 s49, s49, 65535\n"
    "s_mov_b32 s51, 131072\n"
    "s_add_u32 s56, s56, s59\n"
    "s_addc_u32 s57, s57, 0\n"
    "s_and_b32 s57, s57, 65535\n"
    "s_mov_b32 s59, 131072\n"
    "s_add_u32 s8, s8, s11\n"
    "s_addc_u32 s9, s9, 0\n"
    "s_and_b32 s9, s9, 65535\n"
    "s_mov_b32 s11, 131072\n"
    "s_add_u32 s12, s12, s15\n"
    "s_addc_u32 s13, s13, 0\n"
    "s_and_b32 s13, s13, 65535\n"
    "s_mov_b32 s15, 131072\n"
    "s_add_u32 s16, s16, s19\n"
    "s_addc_u32 s17, s17, 0\n"
    "s_and_b32 s17, s17, 65535\n"
    "s_mov_b32 s19, 131072\n"
    "s_add_u32 m0, s60, 24576\n"
    "s_nop 0\n"
    "buffer_load_dwordx4 v1, s[40:43], s70 offen offset:0 lds\n"
    "s_add_u32 s70, s70, 31744\n"
    "buffer_load_dwordx4 v1, s[40:43], s70 offen offset:1024 lds\n"
    "s_add_u32 s70, s70, 31744\n"
    "buffer_load_dwordx4 v2, s[40:43], s70 offen offset:2048 lds\n"
    "s_add_u32 s70, s70, 31744\n"
    "buffer_load_dwordx4 v2, s[40:43], s70 offen offset:3072 lds\n"
    "s_add_u32 s70, s70, 35840\n"
    "s_add_u32 m0, s60, 28672\n"
    "s_nop 0\n"
    "buffer_load_dwordx4 v1, s[40:43], s70 offen offset:0 lds\n"
    "s_add_u32 s70, s70, 31744\n"
    "buffer_load_dwordx4 v1, s[40:43], s70 offen offset:1024 lds\n"
    "s_add_u32 s70, s70, 31744\n"
    "buffer_load_dwordx4 v2, s[40:43], s70 offen offset:2048 lds\n"
    "s_add_u32 s70, s70, 31744\n"
    "buffer_load_dwordx4 v2, s[40:43], s70 offen offset:3072 lds\n"
    "s_add_u32 s70, s70, 35840\n"
    "s_add_u32 s70, s70, s76\n"
    "s_add_u32 s80, s62, s77\n"
    "s_add_u32 m0, s61, 81920\n"
    "s_nop 0\n"
    "buffer_load_dwordx4 v3, s[44:47], s80 offen offset:0 lds\n"
    "s_add_u32 s80, s80, 8192\n"
    "buffer_load_dwordx4 v4, s[44:47], s80 offen offset:1024 lds\n"
    "s_add_u32 s80, s80, 8192\n"
    "buffer_load_dwordx4 v3, s[44:47], s80 offen offset:2048 lds\n"
    "s_add_u32 s80, s80, 8192\n"
    "buffer_load_dwordx4 v4, s[44:47], s80 offen offset:3072 lds\n"
    "s_add_u32 s77, s77, 128\n"
    "s_cmp_ge_u32 s77, 1152\n"
    "s_cselect_b32 s74, 1024, 0\n"
    "s_sub_u32 s77, s77, s74\n"
    "s_add_u32 m0, s60, 8192\n"
    "s_nop 0\n"
    "buffer_load_dwordx4 v1, s[40:43], s70 offen offset:0 lds\n"
    "s_add_u32 s70, s70, 31744\n"
    "buffer_load_dwordx4 v1, s[40:43], s70 offen offset:1024 lds\n"
    "s_add_u32 s70, s70, 31744\n"
    "buffer_load_dwordx4 v2, s[40:43], s70 offen offset:2048 lds\n"
    "s_add_u32 s70, s70, 31744\n"
    "buffer_load_dwordx4 v2, s[40:43], s70 offen offset:3072 lds\n"
    "s_add_u32 s70, s70, 35840\n"
    "s_add_u32 m0, s60, 12288\n"
    "s_nop 0\n"
    "buffer_load_dwordx4 v1, s[40:43], s70 offen offset:0 lds\n"
    "s_add_u32 s70, s70, 31744\n"
    "buffer_load_dwordx4 v1, s[40:43], s70 offen offset:1024 lds\n"
    "s_add_u32 s70, s70, 31744\n"
    "buffer_load_dwordx4 v2, s[40:43], s70 offen offset:2048 lds\n"
    "s_add_u32 s70, s70, 31744\n"
    "buffer_load_dwordx4 v2, s[40:43], s70 offen offset:3072 lds\n"
    "s_add_u32 s70, s70, 35840\n"
    "s_mov_b32 s81, 0\n"
    "s_mov_b32 s75, 1040356334\n"
    "s_add_u32 s72, s63, 16384\n"
    "s_and_b32 s72, s72, 131071\n"
    "s_mov_b32 s73, 16384\n"
    "s_waitcnt vmcnt(21)\n"
    "s_barrier\n"
    "ds_read_b64_tr_b16 a[0:1], v12 offset:16384\n"
    "ds_read_b64_tr_b16 a[2:3], v12 offset:17408\n"
    "ds_read_b128 a[64:67], v10 offset:0\n"
    "ds_read_b128 a[68:71], v10 offset:2048\n"
    "ds_read_b128 a[72:75], v10 offset:4096\n"
    "ds_read_b128 a[76:79], v10 offset:6144\n"
    "ds_read_b128 a[80:83], v10 offset:40960\n"
    "ds_read_b128 a[84:87], v10 offset:43008\n"
    "ds_read_b128 a[88:91], v10 offset:45056\n"
    "ds_read_b64_tr_b16 a[4:5], v12 offset:16392\n"
    "ds_read_b64_tr_b16 a[6:7], v12 offset:17416\n"
    "ds_read_b64_tr_b16 a[8:9], v13 offset:16384\n"
    "ds_read_b64_tr_b16 a[10:11], v13 offset:17408\n"
    "ds_read_b64_tr_b16 a[12:13], v13 offset:16392\n"
    "ds_read_b64_tr_b16 a[14:15], v13 offset:17416\n"
    "ds_read_b64_tr_b16 a[16:17], v14 offset:16384\n"
    "ds_read_b64_tr_b16 a[18:19], v14 offset:17408\n"
    "ds_read_b64_tr_b16 a[20:21], v14 offset:16392\n"
    "ds_read_b64_tr_b16 a[22:23], v14 offset:17416\n"
    "ds_read_b64_tr_b16 a[24:25], v15 offset:16384\n"
    "ds_read_b64_tr_b16 a[26:27], v15 offset:17408\n"
    "ds_read_b64_tr_b16 a[28:29], v15 offset:16392\n"
    "ds_read_b64_tr_b16 a[30:31], v15 offset:17416\n"
    "s_add_u32 m0, s60, 0\n"
    "s_waitcnt lgkmcnt(15)\n"
    "v_mfma_f32_16x16x32_bf16 v[32:35], a[0:3], a[64:67], 0\n"
    "s_waitcnt lgkmcnt(15)\n"
    "s_waitcnt vmcnt(12)\n"
    "buffer_load_dwordx4 v1, s[40:43], s70 offen offset:0 lds\n"
    "v_mfma_f32_16x16x32_bf16 v[64:67], a[0:3], a[68:71], 0\n"
    "s_waitcnt lgkmcnt(15)\n"
    "s_add_u32 s70, s70, 31744\n"
    "ds_read_b64_tr_b16 a[32:33], v12 offset:24576\n"
    "v_mfma_f32_16x16x32_bf16 v[96:99], a[0:3], a[72:75], 0\n"
    "s_waitcnt lgkmcnt(15)\n"
    "ds_read_b64_tr_b16 a[34:35], v12 offset:25600\n"
    "v_mfma_f32_16x16x32_bf16 v[128:131], a[0:3], a[76:79], 0\n"
    "s_waitcnt lgkmcnt(15)\n"
    "ds_read_b64_tr_b16 a[36:37], v12 offset:24584\n"
    "v_mfma_f32_16x16x32_bf16 v[160:163], a[0:3], a[80:83], 0\n"
    "s_waitcnt lgkmcnt(15)\n"
    "buffer_load_dwordx4 v1, s[40:43], s70 offen offset:1024 lds\n"
    "v_mfma_f32_16x16x32_bf16 v[192:195], a[0:3], a[84:87], 0\n"
    "s_waitcnt lgkmcnt(4)\n"
    "ds_read_b64_tr_b16 a[38:39], v12 offset:25608\n"
    "v_mfma_f32_16x16x32_bf16 v[224:227], a[0:3], a[88:91], 0\n"
    "s_add_u32 s70, s70, 31744\n"
    "ds_read_b64_tr_b16 a[40:41], v13 offset:24576\n"
    "v_mfma_f32_16x16x32_bf16 v[36:39], a[4:7], a[64:67], 0\n"
    "ds_read_b64_tr_b16 a[42:43], v13 offset:25600\n"
    "v_mfma_f32_16x16x32_bf16 v[68:71], a[4:7], a[68:71], 0\n"
    "ds_read_b64_tr_b16 a[44:45], v13 offset:24584\n"
    "v_mfma_f32_16x16x32_bf16 v[100:103], a[4:7], a[72:75], 0\n"
    "buffer_load_dwordx4 v2, s[40:43], s70 offen offset:2048 lds\n"
    "v_mfma_f32_16x16x32_bf16 v[132:135], a[4:7], a[76:79], 0\n"
    "s_add_u32 s70, s70, 31744\n"
    "ds_read_b64_tr_b16 a[46:47], v13 offset:25608\n"
    "v_mfma_f32_16x16x32_bf16 v[164:167], a[4:7], a[80:83], 0\n"
    "ds_read_b64_tr_b16 a[48:49], v14 offset:24576\n"
    "v_mfma_f32_16x16x32_bf16 v[196:199], a[4:7], a[84:87], 0\n"
    "ds_read_b64_tr_b16 a[50:51], v14 offset:25600\n"
    "v_mfma_f32_16x16x32_bf16 v[228:231], a[4:7], a[88:91], 0\n"
    "ds_read_b64_tr_b16 a[52:53], v14 offset:24584\n"
    "v_mfma_f32_16x16x32_bf16 v[40:43], a[8:11], a[64:67], 0\n"
    "buffer_load_dwordx4 v2, s[40:43], s70 offen offset:3072 lds\n"
    "v_mfma_f32_16x16x32_bf16 v[72:75], a[8:11], a[68:71], 0\n"
    "s_add_u32 s70, s70, 35840\n"
    "ds_read_b64_tr_b16 a[54:55], v14 offset:25608\n"
    "v_mfma_f32_16x16x32_bf16 v[104:107], a[8:11], a[72:75], 0\n"
    "s_add_u32 m0, s60, 4096\n"
    "ds_read_b64_tr_b16 a[56:57], v15 offset:24576\n"
    "v_mfma_f32_16x16x32_bf16 v[136:139], a[8:11], a[76:79], 0\n"
    "ds_read_b64_tr_b16 a[58:59], v15 offset:25600\n"
    "v_mfma_f32_16x16x32_bf16 v[168:171], a[8:11], a[80:83], 0\n"
    "ds_read_b64_tr_b16 a[60:61], v15 offset:24584\n"
    "v_mfma_f32_16x16x32_bf16 v[200:203], a[8:11], a[84:87], 0\n"
    "buffer_load_dwordx4 v1, s[40:43], s70 offen offset:0 lds\n"
    "v_mfma_f32_16x16x32_bf16 v[232:235], a[8:11], a[88:91], 0\n"
    "s_add_u32 s70, s70, 31744\n"
    "ds_read_b64_tr_b16 a[62:63], v15 offset:25608\n"
    "v_mfma_f32_16x16x32_bf16 v[44:47], a[12:15], a[64:67], 0\n"
    "ds_read_b128 a[92:95], v11 offset:0\n"
    "v_mfma_f32_16x16x32_bf16 v[76:79], a[12:15], a[68:71], 0\n"
    "ds_read_b128 a[96:99], v11 offset:2048\n"
    "v_mfma_f32_16x16x32_bf16 v[108:111], a[12:15], a[72:75], 0\n"
    "s_waitcnt lgkmcnt(2)\n"
    "ds_read_b128 a[100:103], v11 offset:4096\n"
    "v_mfma_f32_16x16x32_bf16 v[140:143], a[12:15], a[76:79], 0\n"
    "buffer_load_dwordx4 v1, s[40:43], s70 offen offset:1024 lds\n"
    "v_mfma_f32_16x16x32_bf16 v[172:175], a[12:15], a[80:83], 0\n"
    "s_add_u32 s70, s70, 31744\n"
    "ds_read_b128 a[104:107], v11 offset:6144\n"
    "v_mfma_f32_16x16x32_bf16 v[204:207], a[12:15], a[84:87], 0\n"
    "ds_read_b128 a[108:111], v11 offset:40960\n"
    "v_mfma_f32_16x16x32_bf16 v[236:239], a[12:15], a[88:91], 0\n"
    "ds_read_b128 a[112:115], v11 offset:43008\n"
    "v_mfma_f32_16x16x32_bf16 v[48:51], a[16:19], a[64:67], 0\n"
    "ds_read_b128 a[116:119], v11 offset:45056\n"
    "v_mfma_f32_16x16x32_bf16 v[80:83], a[16:19], a[68:71], 0\n"
    "buffer_load_dwordx4 v2, s[40:43], s70 offen offset:2048 lds\n"
    "v_mfma_f32_16x16x32_bf16 v[112:115], a[16:19], a[72:75], 0\n"
    "s_add_u32 s70, s70, 31744\n"
    "v_mfma_f32_16x16x32_bf16 v[144:147], a[16:19], a[76:79], 0\n"
    "v_mfma_f32_16x16x32_bf16 v[176:179], a[16:19], a[80:83], 0\n"
    "v_mfma_f32_16x16x32_bf16 v[208:211], a[16:19], a[84:87], 0\n"
    "buffer_load_dwordx4 v2, s[40:43], s70 offen offset:3072 lds\n"
    "v_mfma_f32_16x16x32_bf16 v[240:243], a[16:19], a[88:91], 0\n"
    "s_waitcnt lgkmcnt(0)\n"
    "v_mfma_f32_16x16x32_bf16 v[52:55], a[20:23], a[64:67], 0\n"
    "s_barrier\n"
    "v_mfma_f32_16x16x32_bf16 v[84:87], a[20:23], a[68:71], 0\n"
    "s_add_u32 s70, s70, 35840\n"
    "v_mfma_f32_16x16x32_bf16 v[116:119], a[20:23], a[72:75], 0\n"
    "s_cmp_ge_u32 s70, 4718592\n"
    "s_cselect_b32 s74, 4194304, 0\n"
    "s_sub_u32 s70, s70, s74\n"
    "s_add_u32 s80, s62, s77\n"
    "s_add_u32 m0, s61, 0\n"
    "v_mfma_f32_16x16x32_bf16 v[148:151], a[20:23], a[76:79], 0\n"
    "buffer_load_dwordx4 v3, s[44:47], s80 offen offset:0 lds\n"
    "v_mfma_f32_16x16x32_bf16 v[180:183], a[20:23], a[80:83], 0\n"
    "s_add_u32 s80, s80, 8192\n"
    "v_mfma_f32_16x16x32_bf16 v[212:215], a[20:23], a[84:87], 0\n"
    "v_add_u32 v10, 81920, v10\n"
    "v_add_u32 v11, 81920, v11\n"
    "v_mfma_f32_16x16x32_bf16 v[244:247], a[20:23], a[88:91], 0\n"
    "v_mfma_f32_16x16x32_bf16 v[56:59], a[24:27], a[64:67], 0\n"
    "v_mfma_f32_16x16x32_bf16 v[88:91], a[24:27], a[68:71], 0\n"
    "buffer_load_dwordx4 v4, s[44:47], s80 offen offset:1024 lds\n"
    "v_mfma_f32_16x16x32_bf16 v[120:123], a[24:27], a[72:75], 0\n"
    "s_add_u32 s80, s80, 8192\n"
    "v_mfma_f32_16x16x32_bf16 v[152:155], a[24:27], a[76:79], 0\n"
    "v_mfma_f32_16x16x32_bf16 v[184:187], a[24:27], a[80:83], 0\n"
    "v_mfma_f32_16x16x32_bf16 v[216:219], a[24:27], a[84:87], 0\n"
    "v_mfma_f32_16x16x32_bf16 v[248:251], a[24:27], a[88:91], 0\n"
    "buffer_load_dwordx4 v3, s[44:47], s80 offen offset:2048 lds\n"
    "v_mfma_f32_16x16x32_bf16 v[60:63], a[28:31], a[64:67], 0\n"
    "s_add_u32 s80, s80, 8192\n"
    "v_mfma_f32_16x16x32_bf16 v[92:95], a[28:31], a[68:71], 0\n"
    "v_mfma_f32_16x16x32_bf16 v[124:127], a[28:31], a[72:75], 0\n"
    "v_mfma_f32_16x16x32_bf16 v[156:159], a[28:31], a[76:79], 0\n"
    "v_mfma_f32_16x16x32_bf16 v[188:191], a[28:31], a[80:83], 0\n"
    "buffer_load_dwordx4 v4, s[44:47], s80 offen offset:3072 lds\n"
    "v_mfma_f32_16x16x32_bf16 v[220:223], a[28:31], a[84:87], 0\n"
    "s_add_u32 s77, s77, 128\n"
    "v_mfma_f32_16x16x32_bf16 v[252:255], a[28:31], a[88:91], 0\n"
    "s_cmp_ge_u32 s77, 1152\n"
    "v_mfma_f32_16x16x32_bf16 v[32:35], a[32:35], a[92:95], v[32:35]\n"
    "s_waitcnt vmcnt(12)\n"
    "ds_read_b64_tr_b16 a[0:1], v12 offset:8192\n"
    "v_mfma_f32_16x16x32_bf16 v[64:67], a[32:35], a[96:99], v[64:67]\n"
    "s_cselect_b32 s74, 1024, 0\n"
    "s_sub_u32 s77, s77, s74\n"
    "s_add_u32 m0, s60, 16384\n"
    "v_mfma_f32_16x16x32_bf16 v[96:99], a[32:35], a[100:103], v[96:99]\n"
    "s_barrier\n"
    "buffer_load_dwordx4 v1, s[40:43], s70 offen offset:0 lds\n"
    "v_mfma_f32_16x16x32_bf16 v[128:131], a[32:35], a[104:107], v[128:131]\n"
    "s_add_u32 s70, s70, 31744\n"
    "ds_read_b64_tr_b16 a[2:3], v12 offset:9216\n"
    "v_mfma_f32_16x16x32_bf16 v[160:163], a[32:35], a[108:111], v[160:163]\n"
    "ds_read_b64_tr_b16 a[4:5], v12 offset:8200\n"
    "v_mfma_f32_16x16x32_bf16 v[192:195], a[32:35], a[112:115], v[192:195]\n"
    "ds_read_b64_tr_b16 a[6:7], v12 offset:9224\n"
    "v_mfma_f32_16x16x32_bf16 v[224:227], a[32:35], a[116:119], v[224:227]\n"
    "ds_read_b64_tr_b16 a[8:9], v13 offset:8192\n"
    "v_mfma_f32_16x16x32_bf16 v[36:39], a[36:39], a[92:95], v[36:39]\n"
    "buffer_load_dwordx4 v1, s[40:43], s70 offen offset:1024 lds\n"
    "v_mfma_f32_16x16x32_bf16 v[68:71], a[36:39], a[96:99], v[68:71]\n"
    "s_add_u32 s70, s70, 31744\n"
    "ds_read_b64_tr_b16 a[10:11], v13 offset:9216\n"
    "v_mfma_f32_16x16x32_bf16 v[100:103], a[36:39], a[100:103], v[100:103]\n"
    "ds_read_b64_tr_b16 a[12:13], v13 offset:8200\n"
    "v_mfma_f32_16x16x32_bf16 v[132:135], a[36:39], a[104:107], v[132:135]\n"
    "ds_read_b64_tr_b16 a[14:15], v13 offset:9224\n"
    "v_mfma_f32_16x16x32_bf16 v[164:167], a[36:39], a[108:111], v[164:167]\n"
    "buffer_load_dwordx4 v2, s[40:43], s70 offen offset:2048 lds\n"
    "v_mfma_f32_16x16x32_bf16 v[196:199], a[36:39], a[112:115], v[196:199]\n"
    "s_add_u32 s70, s70, 31744\n"
    "ds_read_b64_tr_b16 a[16:17], v14 offset:8192\n"
    "v_mfma_f32_16x16x32_bf16 v[228:231], a[36:39], a[116:119], v[228:231]\n"
    "ds_read_b64_tr_b16 a[18:19], v14 offset:9216\n"
    "v_mfma_f32_16x16x32_bf16 v[40:43], a[40:43], a[92:95], v[40:43]\n"
    "ds_read_b64_tr_b16 a[20:21], v14 offset:8200\n"
    "v_mfma_f32_16x16x32_bf16 v[72:75], a[40:43], a[96:99], v[72:75]\n"
    "ds_read_b64_tr_b16 a[22:23], v14 offset:9224\n"
    "v_mfma_f32_16x16x32_bf16 v[104:107], a[40:43], a[100:103], v[104:107]\n"
    "buffer_load_dwordx4 v2, s[40:43], s70 offen offset:3072 lds\n"
    "v_mfma_f32_16x16x32_bf16 v[136:139], a[40:43], a[104:107], v[136:139]\n"
    "s_add_u32 s70, s70, 35840\n"
    "ds_read_b64_tr_b16 a[24:25], v15 offset:8192\n"
    "v_mfma_f32_16x16x32_bf16 v[168:171], a[40:43], a[108:111], v[168:171]\n"
    "s_add_u32 m0, s60, 20480\n"
    "ds_read_b64_tr_b16 a[26:27], v15 offset:9216\n"
    "v_mfma_f32_16x16x32_bf16 v[200:203], a[40:43], a[112:115], v[200:203]\n"
    "ds_read_b64_tr_b16 a[28:29], v15 offset:8200\n"
    "v_mfma_f32_16x16x32_bf16 v[232:235], a[40:43], a[116:119], v[232:235]\n"
    "ds_read_b64_tr_b16 a[30:31], v15 offset:9224\n"
    "v_mfma_f32_16x16x32_bf16 v[44:47], a[44:47], a[92:95], v[44:47]\n"
    "buffer_load_dwordx4 v1, s[40:43], s70 offen offset:0 lds\n"
    "v_mfma_f32_16x16x32_bf16 v[76:79], a[44:47], a[96:99], v[76:79]\n"
    "s_add_u32 s70, s70, 31744\n"
    "ds_read_b128 a[64:67], v10 offset:0\n"
    "v_mfma_f32_16x16x32_bf16 v[108:111], a[44:47], a[100:103], v[108:111]\n"
    "s_waitcnt lgkmcnt(1)\n"
    "ds_read_b128 a[68:71], v10 offset:2048\n"
    "v_mfma_f32_16x16x32_bf16 v[140:143], a[44:47], a[104:107], v[140:143]\n"
    "ds_read_b128 a[72:75], v10 offset:4096\n"
    "v_mfma_f32_16x16x32_bf16 v[172:175], a[44:47], a[108:111], v[172:175]\n"
    "ds_read_b128 a[76:79], v10 offset:6144\n"
    "v_mfma_f32_16x16x32_bf16 v[204:207], a[44:47], a[112:115], v[204:207]\n"
    "buffer_load_dwordx4 v1, s[40:43], s70 offen offset:1024 lds\n"
    "v_mfma_f32_16x16x32_bf16 v[236:239], a[44:47], a[116:119], v[236:239]\n"
    "s_add_u32 s70, s70, 31744\n"
    "ds_read_b128 a[80:83], v10 offset:40960\n"
    "v_mfma_f32_16x16x32_bf16 v[48:51], a[48:51], a[92:95], v[48:51]\n"
    "ds_read_b128 a[84:87], v10 offset:43008\n"
    "v_mfma_f32_16x16x32_bf16 v[80:83], a[48:51], a[96:99], v[80:83]\n"
    "ds_read_b128 a[88:91], v10 offset:45056\n"
    "v_mfma_f32_16x16x32_bf16 v[112:115], a[48:51], a[100:103], v[112:115]\n"
    "v_mfma_f32_16x16x32_bf16 v[144:147], a[48:51], a[104:107], v[144:147]\n"
    "buffer_load_dwordx4 v2, s[40:43], s70 offen offset:2048 lds\n"
    "v_mfma_f32_16x16x32_bf16 v[176:179], a[48:51], a[108:111], v[176:179]\n"
    "s_add_u32 s70, s70, 31744\n"
    "v_mfma_f32_16x16x32_bf16 v[208:211], a[48:51], a[112:115], v[208:211]\n"
    "v_mfma_f32_16x16x32_bf16 v[240:243], a[48:51], a[116:119], v[240:243]\n"
    "s_waitcnt lgkmcnt(0)\n"
    "v_mfma_f32_16x16x32_bf16 v[52:55], a[52:55], a[92:95], v[52:55]\n"
    "v_mfma_f32_16x16x32_bf16 v[84:87], a[52:55], a[96:99], v[84:87]\n"
    "buffer_load_dwordx4 v2, s[40:43], s70 offen offset:3072 lds\n"
    "v_mfma_f32_16x16x32_bf16 v[116:119], a[52:55], a[100:103], v[116:119]\n"
    "s_add_u32 s70, s70, 35840\n"
    "v_mfma_f32_16x16x32_bf16 v[148:151], a[52:55], a[104:107], v[148:151]\n"
    "s_add_u32 m0, s60, 24576\n"
    "v_mfma_f32_16x16x32_bf16 v[180:183], a[52:55], a[108:111], v[180:183]\n"
    "v_mfma_f32_16x16x32_bf16 v[212:215], a[52:55], a[112:115], v[212:215]\n"
    "buffer_load_dwordx4 v1, s[40:43], s70 offen offset:0 lds\n"
    "v_mfma_f32_16x16x32_bf16 v[244:247], a[52:55], a[116:119], v[244:247]\n"
    "s_add_u32 s70, s70, 31744\n"
    "v_mfma_f32_16x16x32_bf16 v[56:59], a[56:59], a[92:95], v[56:59]\n"
    "v_mfma_f32_16x16x32_bf16 v[88:91], a[56:59], a[96:99], v[88:91]\n"
    "v_mfma_f32_16x16x32_bf16 v[120:123], a[56:59], a[100:103], v[120:123]\n"
    "v_mfma_f32_16x16x32_bf16 v[152:155], a[56:59], a[104:107], v[152:155]\n"
    "buffer_load_dwordx4 v1, s[40:43], s70 offen offset:1024 lds\n"
    "v_mfma_f32_16x16x32_bf16 v[184:187], a[56:59], a[108:111], v[184:187]\n"
    "s_add_u32 s70, s70, 31744\n"
    "v_mfma_f32_16x16x32_bf16 v[216:219], a[56:59], a[112:115], v[216:219]\n"
    "v_mfma_f32_16x16x32_bf16 v[248:251], a[56:59], a[116:119], v[248:251]\n"
    "v_mfma_f32_16x16x32_bf16 v[60:63], a[60:63], a[92:95], v[60:63]\n"
    "v_mfma_f32_16x16x32_bf16 v[92:95], a[60:63], a[96:99], v[92:95]\n"
    "buffer_load_dwordx4 v2, s[40:43], s70 offen offset:2048 lds\n"
    "v_mfma_f32_16x16x32_bf16 v[124:127], a[60:63], a[100:103], v[124:127]\n"
    "s_add_u32 s70, s70, 31744\n"
    "v_mfma_f32_16x16x32_bf16 v[156:159], a[60:63], a[104:107], v[156:159]\n"
    "v_mfma_f32_16x16x32_bf16 v[188:191], a[60:63], a[108:111], v[188:191]\n"
    "v_mfma_f32_16x16x32_bf16 v[220:223], a[60:63], a[112:115], v[220:223]\n"
    "v_mfma_f32_16x16x32_bf16 v[252:255], a[60:63], a[116:119], v[252:255]\n"
    "buffer_load_dwordx4 v2, s[40:43], s70 offen offset:3072 lds\n"
    "v_mfma_f32_16x16x32_bf16 v[32:35], a[0:3], a[64:67], v[32:35]\n"
    "s_waitcnt vmcnt(16)\n"
    "ds_read_b64_tr_b16 a[32:33], v12 offset:0\n"
    "v_mfma_f32_16x16x32_bf16 v[64:67], a[0:3], a[68:71], v[64:67]\n"
    "s_add_u32 s70, s70, 35840\n"
    "ds_read_b64_tr_b16 a[34:35], v12 offset:1024\n"
    "v_mfma_f32_16x16x32_bf16 v[96:99], a[0:3], a[72:75], v[96:99]\n"
    "s_add_u32 m0, s60, 28672\n"
    "ds_read_b64_tr_b16 a[36:37], v12 offset:8\n"
    "v_mfma_f32_16x16x32_bf16 v[128:131], a[0:3], a[76:79], v[128:131]\n"
    "ds_read_b64_tr_b16 a[38:39], v12 offset:1032\n"
    "v_mfma_f32_16x16x32_bf16 v[160:163], a[0:3], a[80:83], v[160:163]\n"
    "buffer_load_dwordx4 v1, s[40:43], s70 offen offset:0 lds\n"
    "v_mfma_f32_16x16x32_bf16 v[192:195], a[0:3], a[84:87], v[192:195]\n"
    "s_add_u32 s70, s70, 31744\n"
    "ds_read_b64_tr_b16 a[40:41], v13 offset:0\n"
    "v_mfma_f32_16x16x32_bf16 v[224:227], a[0:3], a[88:91], v[224:227]\n"
    "ds_read_b64_tr_b16 a[42:43], v13 offset:1024\n"
    "v_mfma_f32_16x16x32_bf16 v[36:39], a[4:7], a[64:67], v[36:39]\n"
    "ds_read_b64_tr_b16 a[44:45], v13 offset:8\n"
    "v_mfma_f32_16x16x32_bf16 v[68:71], a[4:7], a[68:71], v[68:71]\n"
    "ds_read_b64_tr_b16 a[46:47], v13 offset:1032\n"
    "v_mfma_f32_16x16x32_bf16 v[100:103], a[4:7], a[72:75], v[100:103]\n"
    "buffer_load_dwordx4 v1, s[40:43], s70 offen offset:1024 lds\n"
    "v_mfma_f32_16x16x32_bf16 v[132:135], a[4:7], a[76:79], v[132:135]\n"
    "s_add_u32 s70, s70, 31744\n"
    "ds_read_b64_tr_b16 a[48:49], v14 offset:0\n"
    "v_mfma_f32_16x16x32_bf16 v[164:167], a[4:7], a[80:83], v[164:167]\n"
    "ds_read_b64_tr_b16 a[50:51], v14 offset:1024\n"
    "v_mfma_f32_16x16x32_bf16 v[196:199], a[4:7], a[84:87], v[196:199]\n"
    "ds_read_b64_tr_b16 a[52:53], v14 offset:8\n"
    "v_mfma_f32_16x16x32_bf16 v[228:231], a[4:7], a[88:91], v[228:231]\n"
    "ds_read_b64_tr_b16 a[54:55], v14 offset:1032\n"
    "v_mfma_f32_16x16x32_bf16 v[40:43], a[8:11], a[64:67], v[40:43]\n"
    "buffer_load_dwordx4 v2, s[40:43], s70 offen offset:2048 lds\n"
    "v_mfma_f32_16x16x32_bf16 v[72:75], a[8:11], a[68:71], v[72:75]\n"
    "s_add_u32 s70, s70, 31744\n"
    "ds_read_b64_tr_b16 a[56:57], v15 offset:0\n"
    "v_mfma_f32_16x16x32_bf16 v[104:107], a[8:11], a[72:75], v[104:107]\n"
    "ds_read_b64_tr_b16 a[58:59], v15 offset:1024\n"
    "v_mfma_f32_16x16x32_bf16 v[136:139], a[8:11], a[76:79], v[136:139]\n"
    "ds_read_b64_tr_b16 a[60:61], v15 offset:8\n"
    "v_mfma_f32_16x16x32_bf16 v[168:171], a[8:11], a[80:83], v[168:171]\n"
    "buffer_load_dwordx4 v2, s[40:43], s70 offen offset:3072 lds\n"
    "v_mfma_f32_16x16x32_bf16 v[200:203], a[8:11], a[84:87], v[200:203]\n"
    "s_add_u32 s70, s70, 35840\n"
    "ds_read_b64_tr_b16 a[62:63], v15 offset:1032\n"
    "v_mfma_f32_16x16x32_bf16 v[232:235], a[8:11], a[88:91], v[232:235]\n"
    "s_cmp_ge_u32 s70, 4718592\n"
    "ds_read_b128 a[92:95], v11 offset:0\n"
    "v_mfma_f32_16x16x32_bf16 v[44:47], a[12:15], a[64:67], v[44:47]\n"
    "s_cselect_b32 s74, 4194304, 0\n"
    "ds_read_b128 a[96:99], v11 offset:2048\n"
    "v_mfma_f32_16x16x32_bf16 v[76:79], a[12:15], a[68:71], v[76:79]\n"
    "s_sub_u32 s70, s70, s74\n"
    "s_add_u32 m0, s60, 8192\n"
    "ds_read_b128 a[100:103], v11 offset:4096\n"
    "v_mfma_f32_16x16x32_bf16 v[108:111], a[12:15], a[72:75], v[108:111]\n"
    "s_waitcnt lgkmcnt(3)\n"
    "buffer_load_dwordx4 v1, s[40:43], s70 offen offset:0 lds\n"
    "v_mfma_f32_16x16x32_bf16 v[140:143], a[12:15], a[76:79], v[140:143]\n"
    "s_add_u32 s70, s70, 31744\n"
    "ds_read_b128 a[104:107], v11 offset:6144\n"
    "v_mfma_f32_16x16x32_bf16 v[172:175], a[12:15], a[80:83], v[172:175]\n"
    "ds_read_b128 a[108:111], v11 offset:40960\n"
    "v_mfma_f32_16x16x32_bf16 v[204:207], a[12:15], a[84:87], v[204:207]\n"
    "ds_read_b128 a[112:115], v11 offset:43008\n"
    "v_mfma_f32_16x16x32_bf16 v[236:239], a[12:15], a[88:91], v[236:239]\n"
    "ds_read_b128 a[116:119], v11 offset:45056\n"
    "v_mfma_f32_16x16x32_bf16 v[48:51], a[16:19], a[64:67], v[48:51]\n"
    "buffer_load_dwordx4 v1, s[40:43], s70 offen offset:1024 lds\n"
    "v_mfma_f32_16x16x32_bf16 v[80:83], a[16:19], a[68:71], v[80:83]\n"
    "s_add_u32 s70, s70, 31744\n"
    "v_mfma_f32_16x16x32_bf16 v[112:115], a[16:19], a[72:75], v[112:115]\n"
    "v_mfma_f32_16x16x32_bf16 v[144:147], a[16:19], a[76:79], v[144:147]\n"
    "v_mfma_f32_16x16x32_bf16 v[176:179], a[16:19], a[80:83], v[176:179]\n"
    "v_mfma_f32_16x16x32_bf16 v[208:211], a[16:19], a[84:87], v[208:211]\n"
    "buffer_load_dwordx4 v2, s[40:43], s70 offen offset:2048 lds\n"
    "v_mfma_f32_16x16x32_bf16 v[240:243], a[16:19], a[88:91], v[240:243]\n"
    "s_waitcnt lgkmcnt(0)\n"
    "v_mfma_f32_16x16x32_bf16 v[52:55], a[20:23], a[64:67], v[52:55]\n"
    "s_barrier\n"
    "v_mfma_f32_16x16x32_bf16 v[84:87], a[20:23], a[68:71], v[84:87]\n"
    "s_add_u32 s70, s70, 31744\n"
    "v_mfma_f32_16x16x32_bf16 v[116:119], a[20:23], a[72:75], v[116:119]\n"
    "v_mfma_f32_16x16x32_bf16 v[148:151], a[20:23], a[76:79], v[148:151]\n"
    "buffer_load_dwordx4 v2, s[40:43], s70 offen offset:3072 lds\n"
    "v_mfma_f32_16x16x32_bf16 v[180:183], a[20:23], a[80:83], v[180:183]\n"
    "s_add_u32 s70, s70, 35840\n"
    "v_mfma_f32_16x16x32_bf16 v[212:215], a[20:23], a[84:87], v[212:215]\n"
    "s_add_u32 s80, s62, s77\n"
    "v_add_u32 v10, 4294885376, v10\n"
    "v_add_u32 v11, 4294885376, v11\n"
    "v_mfma_f32_16x16x32_bf16 v[244:247], a[20:23], a[88:91], v[244:247]\n"
    "s_add_u32 m0, s61, 81920\n"
    "v_mfma_f32_16x16x32_bf16 v[56:59], a[24:27], a[64:67], v[56:59]\n"
    "v_mfma_f32_16x16x32_bf16 v[88:91], a[24:27], a[68:71], v[88:91]\n"
    "buffer_load_dwordx4 v3, s[44:47], s80 offen offset:0 lds\n"
    "v_mfma_f32_16x16x32_bf16 v[120:123], a[24:27], a[72:75], v[120:123]\n"
    "s_add_u32 s80, s80, 8192\n"
    "v_mfma_f32_16x16x32_bf16 v[152:155], a[24:27], a[76:79], v[152:155]\n"
    "v_mfma_f32_16x16x32_bf16 v[184:187], a[24:27], a[80:83], v[184:187]\n"
    "v_mfma_f32_16x16x32_bf16 v[216:219], a[24:27], a[84:87], v[216:219]\n"
    "v_mfma_f32_16x16x32_bf16 v[248:251], a[24:27], a[88:91], v[248:251]\n"
    "buffer_load_dwordx4 v4, s[44:47], s80 offen offset:1024 lds\n"
    "v_mfma_f32_16x16x32_bf16 v[60:63], a[28:31], a[64:67], v[60:63]\n"
    "s_add_u32 s80, s80, 8192\n"
    "v_mfma_f32_16x16x32_bf16 v[92:95], a[28:31], a[68:71], v[92:95]\n"
    "v_mfma_f32_16x16x32_bf16 v[124:127], a[28:31], a[72:75], v[124:127]\n"
    "v_mfma_f32_16x16x32_bf16 v[156:159], a[28:31], a[76:79], v[156:159]\n"
    "buffer_load_dwordx4 v3, s[44:47], s80 offen offset:2048 lds\n"
    "v_mfma_f32_16x16x32_bf16 v[188:191], a[28:31], a[80:83], v[188:191]\n"
    "s_add_u32 s80, s80, 8192\n"
    "v_mfma_f32_16x16x32_bf16 v[220:223], a[28:31], a[84:87], v[220:223]\n"
    "s_add_u32 s71, s63, 8192\n"
    "v_mfma_f32_16x16x32_bf16 v[252:255], a[28:31], a[88:91], v[252:255]\n"
    "s_and_b32 s71, s71, 131071\n"
    "v_mfma_f32_16x16x32_bf16 v[32:35], a[32:35], a[92:95], v[32:35]\n"
    "s_waitcnt vmcnt(15)\n"
    "ds_read_b64_tr_b16 a[0:1], v12 offset:16384\n"
    "v_mfma_f32_16x16x32_bf16 v[64:67], a[32:35], a[96:99], v[64:67]\n"
    "buffer_load_dwordx4 a[224:227], v5, s[48:51], s71 offen offset:0\n"
    "v_mfma_f32_16x16x32_bf16 v[96:99], a[32:35], a[100:103], v[96:99]\n"
    "s_barrier\n"
    "ds_read_b64_tr_b16 a[2:3], v12 offset:17408\n"
    "v_mfma_f32_16x16x32_bf16 v[128:131], a[32:35], a[104:107], v[128:131]\n"
    "ds_read_b64_tr_b16 a[4:5], v12 offset:16392\n"
    "v_mfma_f32_16x16x32_bf16 v[160:163], a[32:35], a[108:111], v[160:163]\n"
    "ds_read_b64_tr_b16 a[6:7], v12 offset:17416\n"
    "v_mfma_f32_16x16x32_bf16 v[192:195], a[32:35], a[112:115], v[192:195]\n"
    "ds_read_b64_tr_b16 a[8:9], v13 offset:16384\n"
    "v_mfma_f32_16x16x32_bf16 v[224:227], a[32:35], a[116:119], v[224:227]\n"
    "buffer_load_dwordx4 a[228:231], v6, s[48:51], s71 offen offset:1024\n"
    "v_mfma_f32_16x16x32_bf16 v[36:39], a[36:39], a[92:95], v[36:39]\n"
    "ds_read_b64_tr_b16 a[10:11], v13 offset:17408\n"
    "v_mfma_f32_16x16x32_bf16 v[68:71], a[36:39], a[96:99], v[68:71]\n"
    "ds_read_b64_tr_b16 a[12:13], v13 offset:16392\n"
    "v_mfma_f32_16x16x32_bf16 v[100:103], a[36:39], a[100:103], v[100:103]\n"
    "ds_read_b64_tr_b16 a[14:15], v13 offset:17416\n"
    "v_mfma_f32_16x16x32_bf16 v[132:135], a[36:39], a[104:107], v[132:135]\n"
    "ds_read_b64_tr_b16 a[16:17], v14 offset:16384\n"
    "v_mfma_f32_16x16x32_bf16 v[164:167], a[36:39], a[108:111], v[164:167]\n"
    "buffer_load_dwordx4 a[232:235], v7, s[48:51], s71 offen offset:2048\n"
    "v_mfma_f32_16x16x32_bf16 v[196:199], a[36:39], a[112:115], v[196:199]\n"
    "ds_read_b64_tr_b16 a[18:19], v14 offset:17408\n"
    "v_mfma_f32_16x16x32_bf16 v[228:231], a[36:39], a[116:119], v[228:231]\n"
    "ds_read_b64_tr_b16 a[20:21], v14 offset:16392\n"
    "v_mfma_f32_16x16x32_bf16 v[40:43], a[40:43], a[92:95], v[40:43]\n"
    "ds_read_b64_tr_b16 a[22:23], v14 offset:17416\n"
    "v_mfma_f32_16x16x32_bf16 v[72:75], a[40:43], a[96:99], v[72:75]\n"
    "ds_read_b64_tr_b16 a[24:25], v15 offset:16384\n"
    "v_mfma_f32_16x16x32_bf16 v[104:107], a[40:43], a[100:103], v[104:107]\n"
    "buffer_load_dwordx4 a[236:239], v8, s[48:51], s71 offen offset:3072\n"
    "v_mfma_f32_16x16x32_bf16 v[136:139], a[40:43], a[104:107], v[136:139]\n"
    "ds_read_b64_tr_b16 a[26:27], v15 offset:17408\n"
    "v_mfma_f32_16x16x32_bf16 v[168:171], a[40:43], a[108:111], v[168:171]\n"
    "ds_read_b64_tr_b16 a[28:29], v15 offset:16392\n"
    "v_mfma_f32_16x16x32_bf16 v[200:203], a[40:43], a[112:115], v[200:203]\n"
    "ds_read_b64_tr_b16 a[30:31], v15 offset:17416\n"
    "v_mfma_f32_16x16x32_bf16 v[232:235], a[40:43], a[116:119], v[232:235]\n"
    "ds_read_b128 a[64:67], v10 offset:0\n"
    "v_mfma_f32_16x16x32_bf16 v[44:47], a[44:47], a[92:95], v[44:47]\n"
    "buffer_load_dwordx4 v4, s[44:47], s80 offen offset:3072 lds\n"
    "v_mfma_f32_16x16x32_bf16 v[76:79], a[44:47], a[96:99], v[76:79]\n"
    "s_add_u32 s77, s77, 128\n"
    "ds_read_b128 a[68:71], v10 offset:2048\n"
    "v_mfma_f32_16x16x32_bf16 v[108:111], a[44:47], a[100:103], v[108:111]\n"
    "s_waitcnt lgkmcnt(2)\n"
    "ds_read_b128 a[72:75], v10 offset:4096\n"
    "v_mfma_f32_16x16x32_bf16 v[140:143], a[44:47], a[104:107], v[140:143]\n"
    "s_cmp_ge_u32 s77, 1152\n"
    "s_cselect_b32 s74, 1024, 0\n"
    "s_sub_u32 s77, s77, s74\n"
    "s_add_u32 m0, s60, 12288\n"
    "v_mfma_f32_16x16x32_bf16 v[172:175], a[44:47], a[108:111], v[172:175]\n"
    "buffer_load_dwordx4 v1, s[40:43], s70 offen offset:0 lds\n"
    "v_mfma_f32_16x16x32_bf16 v[204:207], a[44:47], a[112:115], v[204:207]\n"
    "s_add_u32 s70, s70, 31744\n"
    "ds_read_b128 a[76:79], v10 offset:6144\n"
    "v_mfma_f32_16x16x32_bf16 v[236:239], a[44:47], a[116:119], v[236:239]\n"
    "ds_read_b128 a[80:83], v10 offset:40960\n"
    "v_mfma_f32_16x16x32_bf16 v[48:51], a[48:51], a[92:95], v[48:51]\n"
    "ds_read_b128 a[84:87], v10 offset:43008\n"
    "v_mfma_f32_16x16x32_bf16 v[80:83], a[48:51], a[96:99], v[80:83]\n"
    "ds_read_b128 a[88:91], v10 offset:45056\n"
    "v_mfma_f32_16x16x32_bf16 v[112:115], a[48:51], a[100:103], v[112:115]\n"
    "buffer_load_dwordx4 v1, s[40:43], s70 offen offset:1024 lds\n"
    "v_mfma_f32_16x16x32_bf16 v[144:147], a[48:51], a[104:107], v[144:147]\n"
    "s_add_u32 s70, s70, 31744\n"
    "v_mfma_f32_16x16x32_bf16 v[176:179], a[48:51], a[108:111], v[176:179]\n"
    "v_mfma_f32_16x16x32_bf16 v[208:211], a[48:51], a[112:115], v[208:211]\n"
    "v_mfma_f32_16x16x32_bf16 v[240:243], a[48:51], a[116:119], v[240:243]\n"
    "s_waitcnt lgkmcnt(0)\n"
    "v_mfma_f32_16x16x32_bf16 v[52:55], a[52:55], a[92:95], v[52:55]\n"
    "buffer_load_dwordx4 v2, s[40:43], s70 offen offset:2048 lds\n"
    "v_mfma_f32_16x16x32_bf16 v[84:87], a[52:55], a[96:99], v[84:87]\n"
    "s_add_u32 s70, s70, 31744\n"
    "v_mfma_f32_16x16x32_bf16 v[116:119], a[52:55], a[100:103], v[116:119]\n"
    "v_mfma_f32_16x16x32_bf16 v[148:151], a[52:55], a[104:107], v[148:151]\n"
    "v_mfma_f32_16x16x32_bf16 v[180:183], a[52:55], a[108:111], v[180:183]\n"
    "v_mfma_f32_16x16x32_bf16 v[212:215], a[52:55], a[112:115], v[212:215]\n"
    "buffer_load_dwordx4 v2, s[40:43], s70 offen offset:3072 lds\n"
    "v_mfma_f32_16x16x32_bf16 v[244:247], a[52:55], a[116:119], v[244:247]\n"
    "s_add_u32 s70, s70, 35840\n"
    "v_mfma_f32_16x16x32_bf16 v[56:59], a[56:59], a[92:95], v[56:59]\n"
    "s_add_u32 m0, s60, 0\n"
    "v_mfma_f32_16x16x32_bf16 v[88:91], a[56:59], a[96:99], v[88:91]\n"
    "v_mfma_f32_16x16x32_bf16 v[120:123], a[56:59], a[100:103], v[120:123]\n"
    "v_mfma_f32_16x16x32_bf16 v[152:155], a[56:59], a[104:107], v[152:155]\n"
    "buffer_load_dwordx4 v1, s[40:43], s70 offen offset:0 lds\n"
    "v_mfma_f32_16x16x32_bf16 v[184:187], a[56:59], a[108:111], v[184:187]\n"
    "s_add_u32 s70, s70, 31744\n"
    "v_mfma_f32_16x16x32_bf16 v[216:219], a[56:59], a[112:115], v[216:219]\n"
    "v_mfma_f32_16x16x32_bf16 v[248:251], a[56:59], a[116:119], v[248:251]\n"
    "v_mfma_f32_16x16x32_bf16 v[60:63], a[60:63], a[92:95], v[60:63]\n"
    "v_mfma_f32_16x16x32_bf16 v[92:95], a[60:63], a[96:99], v[92:95]\n"
    "buffer_load_dwordx4 v1, s[40:43], s70 offen offset:1024 lds\n"
    "v_mfma_f32_16x16x32_bf16 v[124:127], a[60:63], a[100:103], v[124:127]\n"
    "s_add_u32 s70, s70, 31744\n"
    "v_mfma_f32_16x16x32_bf16 v[156:159], a[60:63], a[104:107], v[156:159]\n"
    "v_mfma_f32_16x16x32_bf16 v[188:191], a[60:63], a[108:111], v[188:191]\n"
    "v_mfma_f32_16x16x32_bf16 v[220:223], a[60:63], a[112:115], v[220:223]\n"
    "v_mfma_f32_16x16x32_bf16 v[252:255], a[60:63], a[116:119], v[252:255]\n"
    "buffer_load_dwordx4 v2, s[40:43], s70 offen offset:2048 lds\n"
    "v_mfma_f32_16x16x32_bf16 v[32:35], a[0:3], a[64:67], v[32:35]\n"
    "s_waitcnt vmcnt(19)\n"
    "ds_read_b64_tr_b16 a[32:33], v12 offset:24576\n"
    "v_mfma_f32_16x16x32_bf16 v[64:67], a[0:3], a[68:71], v[64:67]\n"
    "s_add_u32 s70, s70, 31744\n"
    "ds_read_b64_tr_b16 a[34:35], v12 offset:25600\n"
    "v_mfma_f32_16x16x32_bf16 v[96:99], a[0:3], a[72:75], v[96:99]\n"
    "ds_read_b64_tr_b16 a[36:37], v12 offset:24584\n"
    "v_mfma_f32_16x16x32_bf16 v[128:131], a[0:3], a[76:79], v[128:131]\n"
    "buffer_load_dwordx4 v2, s[40:43], s70 offen offset:3072 lds\n"
    "v_mfma_f32_16x16x32_bf16 v[160:163], a[0:3], a[80:83], v[160:163]\n"
    "s_add_u32 s70, s70, 35840\n"
    "ds_read_b64_tr_b16 a[38:39], v12 offset:25608\n"
    "v_mfma_f32_16x16x32_bf16 v[192:195], a[0:3], a[84:87], v[192:195]\n"
    "s_add_u32 m0, s60, 4096\n"
    "ds_read_b64_tr_b16 a[40:41], v13 offset:24576\n"
    "v_mfma_f32_16x16x32_bf16 v[224:227], a[0:3], a[88:91], v[224:227]\n"
    "ds_read_b64_tr_b16 a[42:43], v13 offset:25600\n"
    "v_mfma_f32_16x16x32_bf16 v[36:39], a[4:7], a[64:67], v[36:39]\n"
    "ds_read_b64_tr_b16 a[44:45], v13 offset:24584\n"
    "v_mfma_f32_16x16x32_bf16 v[68:71], a[4:7], a[68:71], v[68:71]\n"
    "buffer_load_dwordx4 v1, s[40:43], s70 offen offset:0 lds\n"
    "v_mfma_f32_16x16x32_bf16 v[100:103], a[4:7], a[72:75], v[100:103]\n"
    "s_add_u32 s70, s70, 31744\n"
    "ds_read_b64_tr_b16 a[46:47], v13 offset:25608\n"
    "v_mfma_f32_16x16x32_bf16 v[132:135], a[4:7], a[76:79], v[132:135]\n"
    "ds_read_b64_tr_b16 a[48:49], v14 offset:24576\n"
    "v_mfma_f32_16x16x32_bf16 v[164:167], a[4:7], a[80:83], v[164:167]\n"
    "ds_read_b64_tr_b16 a[50:51], v14 offset:25600\n"
    "v_mfma_f32_16x16x32_bf16 v[196:199], a[4:7], a[84:87], v[196:199]\n"
    "ds_read_b64_tr_b16 a[52:53], v14 offset:24584\n"
    "v_mfma_f32_16x16x32_bf16 v[228:231], a[4:7], a[88:91], v[228:231]\n"
    "buffer_load_dwordx4 v1, s[40:43], s70 offen offset:1024 lds\n"
    "v_mfma_f32_16x16x32_bf16 v[40:43], a[8:11], a[64:67], v[40:43]\n"
    "s_add_u32 s70, s70, 31744\n"
    "ds_read_b64_tr_b16 a[54:55], v14 offset:25608\n"
    "v_mfma_f32_16x16x32_bf16 v[72:75], a[8:11], a[68:71], v[72:75]\n"
    "ds_read_b64_tr_b16 a[56:57], v15 offset:24576\n"
    "v_mfma_f32_16x16x32_bf16 v[104:107], a[8:11], a[72:75], v[104:107]\n"
    "ds_read_b64_tr_b16 a[58:59], v15 offset:25600\n"
    "v_mfma_f32_16x16x32_bf16 v[136:139], a[8:11], a[76:79], v[136:139]\n"
    "ds_read_b64_tr_b16 a[60:61], v15 offset:24584\n"
    "v_mfma_f32_16x16x32_bf16 v[168:171], a[8:11], a[80:83], v[168:171]\n"
    "buffer_load_dwordx4 v2, s[40:43], s70 offen offset:2048 lds\n"
    "v_mfma_f32_16x16x32_bf16 v[200:203], a[8:11], a[84:87], v[200:203]\n"
    "s_add_u32 s70, s70, 31744\n"
    "ds_read_b64_tr_b16 a[62:63], v15 offset:25608\n"
    "v_mfma_f32_16x16x32_bf16 v[232:235], a[8:11], a[88:91], v[232:235]\n"
    "ds_read_b128 a[92:95], v11 offset:0\n"
    "v_mfma_f32_16x16x32_bf16 v[44:47], a[12:15], a[64:67], v[44:47]\n"
    "ds_read_b128 a[96:99], v11 offset:2048\n"
    "v_mfma_f32_16x16x32_bf16 v[76:79], a[12:15], a[68:71], v[76:79]\n"
    "ds_read_b128 a[100:103], v11 offset:4096\n"
    "v_mfma_f32_16x16x32_bf16 v[108:111], a[12:15], a[72:75], v[108:111]\n"
    "s_waitcnt lgkmcnt(3)\n"
    "buffer_load_dwordx4 v2, s[40:43], s70 offen offset:3072 lds\n"
    "v_mfma_f32_16x16x32_bf16 v[140:143], a[12:15], a[76:79], v[140:143]\n"
    "s_add_u32 s70, s70, 35840\n"
    "ds_read_b128 a[104:107], v11 offset:6144\n"
    "v_mfma_f32_16x16x32_bf16 v[172:175], a[12:15], a[80:83], v[172:175]\n"
    "s_cmp_ge_u32 s70, 4718592\n"
    "ds_read_b128 a[108:111], v11 offset:40960\n"
    "v_mfma_f32_16x16x32_bf16 v[204:207], a[12:15], a[84:87], v[204:207]\n"
    "s_cselect_b32 s74, 4194304, 0\n"
    "ds_read_b128 a[112:115], v11 offset:43008\n"
    "v_mfma_f32_16x16x32_bf16 v[236:239], a[12:15], a[88:91], v[236:239]\n"
    "s_sub_u32 s70, s70, s74\n"
    "s_add_u32 m0, s60, 16384\n"
    "ds_read_b128 a[116:119], v11 offset:45056\n"
    "v_mfma_f32_16x16x32_bf16 v[48:51], a[16:19], a[64:67], v[48:51]\n"
    "buffer_load_dwordx4 v1, s[40:43], s70 offen offset:0 lds\n"
    "v_mfma_f32_16x16x32_bf16 v[80:83], a[16:19], a[68:71], v[80:83]\n"
    "s_add_u32 s70, s70, 31744\n"
    "v_mfma_f32_16x16x32_bf16 v[112:115], a[16:19], a[72:75], v[112:115]\n"
    "v_mfma_f32_16x16x32_bf16 v[144:147], a[16:19], a[76:79], v[144:147]\n"
    "v_mfma_f32_16x16x32_bf16 v[176:179], a[16:19], a[80:83], v[176:179]\n"
    "v_mfma_f32_16x16x32_bf16 v[208:211], a[16:19], a[84:87], v[208:211]\n"
    "buffer_load_dwordx4 v1, s[40:43], s70 offen offset:1024 lds\n"
    "v_mfma_f32_16x16x32_bf16 v[240:243], a[16:19], a[88:91], v[240:243]\n"
    "s_waitcnt lgkmcnt(0)\n"
    "v_mfma_f32_16x16x32_bf16 v[52:55], a[20:23], a[64:67], v[52:55]\n"
    "s_barrier\n"
    "v_mfma_f32_16x16x32_bf16 v[84:87], a[20:23], a[68:71], v[84:87]\n"
    "s_add_u32 s70, s70, 31744\n"
    "v_mfma_f32_16x16x32_bf16 v[116:119], a[20:23], a[72:75], v[116:119]\n"
    "buffer_load_dwordx4 v2, s[40:43], s70 offen offset:2048 lds\n"
    "v_mfma_f32_16x16x32_bf16 v[148:151], a[20:23], a[76:79], v[148:151]\n"
    "s_add_u32 s70, s70, 31744\n"
    "v_mfma_f32_16x16x32_bf16 v[180:183], a[20:23], a[80:83], v[180:183]\n"
    "v_mfma_f32_16x16x32_bf16 v[212:215], a[20:23], a[84:87], v[212:215]\n"
    "v_add_u32 v10, 81920, v10\n"
    "v_add_u32 v11, 81920, v11\n"
    "v_mfma_f32_16x16x32_bf16 v[244:247], a[20:23], a[88:91], v[244:247]\n"
    "v_mfma_f32_16x16x32_bf16 v[56:59], a[24:27], a[64:67], v[56:59]\n"
    "buffer_load_dwordx4 v2, s[40:43], s70 offen offset:3072 lds\n"
    "v_mfma_f32_16x16x32_bf16 v[88:91], a[24:27], a[68:71], v[88:91]\n"
    "s_add_u32 s70, s70, 35840\n"
    "v_mfma_f32_16x16x32_bf16 v[120:123], a[24:27], a[72:75], v[120:123]\n"
    "s_add_u32 s80, s62, s77\n"
    "v_mfma_f32_16x16x32_bf16 v[152:155], a[24:27], a[76:79], v[152:155]\n"
    "s_add_u32 m0, s61, 0\n"
    "v_mfma_f32_16x16x32_bf16 v[184:187], a[24:27], a[80:83], v[184:187]\n"
    "v_mfma_f32_16x16x32_bf16 v[216:219], a[24:27], a[84:87], v[216:219]\n"
    "buffer_load_dwordx4 v3, s[44:47], s80 offen offset:0 lds\n"
    "v_mfma_f32_16x16x32_bf16 v[248:251], a[24:27], a[88:91], v[248:251]\n"
    "s_add_u32 s80, s80, 8192\n"
    "v_mfma_f32_16x16x32_bf16 v[60:63], a[28:31], a[64:67], v[60:63]\n"
    "v_mfma_f32_16x16x32_bf16 v[92:95], a[28:31], a[68:71], v[92:95]\n"
    "v_mfma_f32_16x16x32_bf16 v[124:127], a[28:31], a[72:75], v[124:127]\n"
    "v_mfma_f32_16x16x32_bf16 v[156:159], a[28:31], a[76:79], v[156:159]\n"
    "buffer_load_dwordx4 v4, s[44:47], s80 offen offset:1024 lds\n"
    "v_mfma_f32_16x16x32_bf16 v[188:191], a[28:31], a[80:83], v[188:191]\n"
    "s_add_u32 s80, s80, 8192\n"
    "v_mfma_f32_16x16x32_bf16 v[220:223], a[28:31], a[84:87], v[220:223]\n"
    "s_add_u32 s71, s63, 12288\n"
    "v_mfma_f32_16x16x32_bf16 v[252:255], a[28:31], a[88:91], v[252:255]\n"
    "s_and_b32 s71, s71, 131071\n"
    "v_mfma_f32_16x16x32_bf16 v[32:35], a[32:35], a[92:95], v[32:35]\n"
    "s_waitcnt vmcnt(14)\n"
    "ds_read_b64_tr_b16 a[0:1], v12 offset:8192\n"
    "v_mfma_f32_16x16x32_bf16 v[64:67], a[32:35], a[96:99], v[64:67]\n"
    "buffer_load_dwordx4 a[240:243], v5, s[48:51], s71 offen offset:0\n"
    "v_mfma_f32_16x16x32_bf16 v[96:99], a[32:35], a[100:103], v[96:99]\n"
    "s_barrier\n"
    "ds_read_b64_tr_b16 a[2:3], v12 offset:9216\n"
    "v_mfma_f32_16x16x32_bf16 v[128:131], a[32:35], a[104:107], v[128:131]\n"
    "ds_read_b64_tr_b16 a[4:5], v12 offset:8200\n"
    "v_mfma_f32_16x16x32_bf16 v[160:163], a[32:35], a[108:111], v[160:163]\n"
    "ds_read_b64_tr_b16 a[6:7], v12 offset:9224\n"
    "v_mfma_f32_16x16x32_bf16 v[192:195], a[32:35], a[112:115], v[192:195]\n"
    "ds_read_b64_tr_b16 a[8:9], v13 offset:8192\n"
    "v_mfma_f32_16x16x32_bf16 v[224:227], a[32:35], a[116:119], v[224:227]\n"
    "buffer_load_dwordx4 a[244:247], v6, s[48:51], s71 offen offset:1024\n"
    "v_mfma_f32_16x16x32_bf16 v[36:39], a[36:39], a[92:95], v[36:39]\n"
    "ds_read_b64_tr_b16 a[10:11], v13 offset:9216\n"
    "v_mfma_f32_16x16x32_bf16 v[68:71], a[36:39], a[96:99], v[68:71]\n"
    "ds_read_b64_tr_b16 a[12:13], v13 offset:8200\n"
    "v_mfma_f32_16x16x32_bf16 v[100:103], a[36:39], a[100:103], v[100:103]\n"
    "ds_read_b64_tr_b16 a[14:15], v13 offset:9224\n"
    "v_mfma_f32_16x16x32_bf16 v[132:135], a[36:39], a[104:107], v[132:135]\n"
    "buffer_load_dwordx4 a[248:251], v7, s[48:51], s71 offen offset:2048\n"
    "v_mfma_f32_16x16x32_bf16 v[164:167], a[36:39], a[108:111], v[164:167]\n"
    "ds_read_b64_tr_b16 a[16:17], v14 offset:8192\n"
    "v_mfma_f32_16x16x32_bf16 v[196:199], a[36:39], a[112:115], v[196:199]\n"
    "ds_read_b64_tr_b16 a[18:19], v14 offset:9216\n"
    "v_mfma_f32_16x16x32_bf16 v[228:231], a[36:39], a[116:119], v[228:231]\n"
    "ds_read_b64_tr_b16 a[20:21], v14 offset:8200\n"
    "v_mfma_f32_16x16x32_bf16 v[40:43], a[40:43], a[92:95], v[40:43]\n"
    "ds_read_b64_tr_b16 a[22:23], v14 offset:9224\n"
    "v_mfma_f32_16x16x32_bf16 v[72:75], a[40:43], a[96:99], v[72:75]\n"
    "buffer_load_dwordx4 a[252:255], v8, s[48:51], s71 offen offset:3072\n"
    "v_mfma_f32_16x16x32_bf16 v[104:107], a[40:43], a[100:103], v[104:107]\n"
    "ds_read_b64_tr_b16 a[24:25], v15 offset:8192\n"
    "v_mfma_f32_16x16x32_bf16 v[136:139], a[40:43], a[104:107], v[136:139]\n"
    "ds_read_b64_tr_b16 a[26:27], v15 offset:9216\n"
    "v_mfma_f32_16x16x32_bf16 v[168:171], a[40:43], a[108:111], v[168:171]\n"
    "ds_read_b64_tr_b16 a[28:29], v15 offset:8200\n"
    "v_mfma_f32_16x16x32_bf16 v[200:203], a[40:43], a[112:115], v[200:203]\n"
    "ds_read_b64_tr_b16 a[30:31], v15 offset:9224\n"
    "v_mfma_f32_16x16x32_bf16 v[232:235], a[40:43], a[116:119], v[232:235]\n"
    "buffer_load_dwordx4 v3, s[44:47], s80 offen offset:2048 lds\n"
    "v_mfma_f32_16x16x32_bf16 v[44:47], a[44:47], a[92:95], v[44:47]\n"
    "s_add_u32 s80, s80, 8192\n"
    "ds_read_b128 a[64:67], v10 offset:0\n"
    "v_mfma_f32_16x16x32_bf16 v[76:79], a[44:47], a[96:99], v[76:79]\n"
    "ds_read_b128 a[68:71], v10 offset:2048\n"
    "v_mfma_f32_16x16x32_bf16 v[108:111], a[44:47], a[100:103], v[108:111]\n"
    "s_waitcnt lgkmcnt(2)\n"
    "ds_read_b128 a[72:75], v10 offset:4096\n"
    "v_mfma_f32_16x16x32_bf16 v[140:143], a[44:47], a[104:107], v[140:143]\n"
    "ds_read_b128 a[76:79], v10 offset:6144\n"
    "v_mfma_f32_16x16x32_bf16 v[172:175], a[44:47], a[108:111], v[172:175]\n"
    "buffer_load_dwordx4 v4, s[44:47], s80 offen offset:3072 lds\n"
    "v_mfma_f32_16x16x32_bf16 v[204:207], a[44:47], a[112:115], v[204:207]\n"
    "s_add_u32 s77, s77, 128\n"
    "ds_read_b128 a[80:83], v10 offset:40960\n"
    "v_mfma_f32_16x16x32_bf16 v[236:239], a[44:47], a[116:119], v[236:239]\n"
    "s_cmp_ge_u32 s77, 1152\n"
    "ds_read_b128 a[84:87], v10 offset:43008\n"
    "v_mfma_f32_16x16x32_bf16 v[48:51], a[48:51], a[92:95], v[48:51]\n"
    "s_cselect_b32 s74, 1024, 0\n"
    "ds_read_b128 a[88:91], v10 offset:45056\n"
    "v_mfma_f32_16x16x32_bf16 v[80:83], a[48:51], a[96:99], v[80:83]\n"
    "s_sub_u32 s77, s77, s74\n"
    "s_add_u32 m0, s60, 20480\n"
    "v_mfma_f32_16x16x32_bf16 v[112:115], a[48:51], a[100:103], v[112:115]\n"
    "buffer_load_dwordx4 v1, s[40:43], s70 offen offset:0 lds\n"
    "v_mfma_f32_16x16x32_bf16 v[144:147], a[48:51], a[104:107], v[144:147]\n"
    "s_add_u32 s70, s70, 31744\n"
    "v_mfma_f32_16x16x32_bf16 v[176:179], a[48:51], a[108:111], v[176:179]\n"
    "v_mfma_f32_16x16x32_bf16 v[208:211], a[48:51], a[112:115], v[208:211]\n"
    "v_mfma_f32_16x16x32_bf16 v[240:243], a[48:51], a[116:119], v[240:243]\n"
    "s_waitcnt lgkmcnt(0)\n"
    "v_mfma_f32_16x16x32_bf16 v[52:55], a[52:55], a[92:95], v[52:55]\n"
    "buffer_load_dwordx4 v1, s[40:43], s70 offen offset:1024 lds\n"
    "v_mfma_f32_16x16x32_bf16 v[84:87], a[52:55], a[96:99], v[84:87]\n"
    "s_add_u32 s70, s70, 31744\n"
    "v_mfma_f32_16x16x32_bf16 v[116:119], a[52:55], a[100:103], v[116:119]\n"
    "v_mfma_f32_16x16x32_bf16 v[148:151], a[52:55], a[104:107], v[148:151]\n"
    "v_mfma_f32_16x16x32_bf16 v[180:183], a[52:55], a[108:111], v[180:183]\n"
    "v_mfma_f32_16x16x32_bf16 v[212:215], a[52:55], a[112:115], v[212:215]\n"
    "buffer_load_dwordx4 v2, s[40:43], s70 offen offset:2048 lds\n"
    "v_mfma_f32_16x16x32_bf16 v[244:247], a[52:55], a[116:119], v[244:247]\n"
    "s_add_u32 s70, s70, 31744\n"
    "v_mfma_f32_16x16x32_bf16 v[56:59], a[56:59], a[92:95], v[56:59]\n"
    "v_mfma_f32_16x16x32_bf16 v[88:91], a[56:59], a[96:99], v[88:91]\n"
    "v_mfma_f32_16x16x32_bf16 v[120:123], a[56:59], a[100:103], v[120:123]\n"
    "buffer_load_dwordx4 v2, s[40:43], s70 offen offset:3072 lds\n"
    "v_mfma_f32_16x16x32_bf16 v[152:155], a[56:59], a[104:107], v[152:155]\n"
    "s_add_u32 s70, s70, 35840\n"
    "v_mfma_f32_16x16x32_bf16 v[184:187], a[56:59], a[108:111], v[184:187]\n"
    "s_add_u32 m0, s60, 24576\n"
    "v_mfma_f32_16x16x32_bf16 v[216:219], a[56:59], a[112:115], v[216:219]\n"
    "v_mfma_f32_16x16x32_bf16 v[248:251], a[56:59], a[116:119], v[248:251]\n"
    "v_mfma_f32_16x16x32_bf16 v[60:63], a[60:63], a[92:95], v[60:63]\n"
    "buffer_load_dwordx4 v1, s[40:43], s70 offen offset:0 lds\n"
    "v_mfma_f32_16x16x32_bf16 v[92:95], a[60:63], a[96:99], v[92:95]\n"
    "s_add_u32 s70, s70, 31744\n"
    "v_mfma_f32_16x16x32_bf16 v[124:127], a[60:63], a[100:103], v[124:127]\n"
    "v_mfma_f32_16x16x32_bf16 v[156:159], a[60:63], a[104:107], v[156:159]\n"
    "v_mfma_f32_16x16x32_bf16 v[188:191], a[60:63], a[108:111], v[188:191]\n"
    "v_mfma_f32_16x16x32_bf16 v[220:223], a[60:63], a[112:115], v[220:223]\n"
    "buffer_load_dwordx4 v1, s[40:43], s70 offen offset:1024 lds\n"
    "v_mfma_f32_16x16x32_bf16 v[252:255], a[60:63], a[116:119], v[252:255]\n"
    "s_add_u32 s70, s70, 31744\n"
    "v_mfma_f32_16x16x32_bf16 v[32:35], a[0:3], a[64:67], v[32:35]\n"
    "s_waitcnt vmcnt(18)\n"
    "ds_read_b64_tr_b16 a[32:33], v12 offset:0\n"
    "v_mfma_f32_16x16x32_bf16 v[64:67], a[0:3], a[68:71], v[64:67]\n"
    "ds_read_b64_tr_b16 a[34:35], v12 offset:1024\n"
    "v_mfma_f32_16x16x32_bf16 v[96:99], a[0:3], a[72:75], v[96:99]\n"
    "ds_read_b64_tr_b16 a[36:37], v12 offset:8\n"
    "v_mfma_f32_16x16x32_bf16 v[128:131], a[0:3], a[76:79], v[128:131]\n"
    "buffer_load_dwordx4 v2, s[40:43], s70 offen offset:2048 lds\n"
    "v_mfma_f32_16x16x32_bf16 v[160:163], a[0:3], a[80:83], v[160:163]\n"
    "s_add_u32 s70, s70, 31744\n"
    "ds_read_b64_tr_b16 a[38:39], v12 offset:1032\n"
    "v_mfma_f32_16x16x32_bf16 v[192:195], a[0:3], a[84:87], v[192:195]\n"
    "ds_read_b64_tr_b16 a[40:41], v13 offset:0\n"
    "v_mfma_f32_16x16x32_bf16 v[224:227], a[0:3], a[88:91], v[224:227]\n"
    "ds_read_b64_tr_b16 a[42:43], v13 offset:1024\n"
    "v_mfma_f32_16x16x32_bf16 v[36:39], a[4:7], a[64:67], v[36:39]\n"
    "ds_read_b64_tr_b16 a[44:45], v13 offset:8\n"
    "v_mfma_f32_16x16x32_bf16 v[68:71], a[4:7], a[68:71], v[68:71]\n"
    "buffer_load_dwordx4 v2, s[40:43], s70 offen offset:3072 lds\n"
    "v_mfma_f32_16x16x32_bf16 v[100:103], a[4:7], a[72:75], v[100:103]\n"
    "s_add_u32 s70, s70, 35840\n"
    "ds_read_b64_tr_b16 a[46:47], v13 offset:1032\n"
    "v_mfma_f32_16x16x32_bf16 v[132:135], a[4:7], a[76:79], v[132:135]\n"
    "s_add_u32 m0, s60, 28672\n"
    "ds_read_b64_tr_b16 a[48:49], v14 offset:0\n"
    "v_mfma_f32_16x16x32_bf16 v[164:167], a[4:7], a[80:83], v[164:167]\n"
    "ds_read_b64_tr_b16 a[50:51], v14 offset:1024\n"
    "v_mfma_f32_16x16x32_bf16 v[196:199], a[4:7], a[84:87], v[196:199]\n"
    "ds_read_b64_tr_b16 a[52:53], v14 offset:8\n"
    "v_mfma_f32_16x16x32_bf16 v[228:231], a[4:7], a[88:91], v[228:231]\n"
    "buffer_load_dwordx4 v1, s[40:43], s70 offen offset:0 lds\n"
    "v_mfma_f32_16x16x32_bf16 v[40:43], a[8:11], a[64:67], v[40:43]\n"
    "s_add_u32 s70, s70, 31744\n"
    "ds_read_b64_tr_b16 a[54:55], v14 offset:1032\n"
    "v_mfma_f32_16x16x32_bf16 v[72:75], a[8:11], a[68:71], v[72:75]\n"
    "ds_read_b64_tr_b16 a[56:57], v15 offset:0\n"
    "v_mfma_f32_16x16x32_bf16 v[104:107], a[8:11], a[72:75], v[104:107]\n"
    "ds_read_b64_tr_b16 a[58:59], v15 offset:1024\n"
    "v_mfma_f32_16x16x32_bf16 v[136:139], a[8:11], a[76:79], v[136:139]\n"
    "ds_read_b64_tr_b16 a[60:61], v15 offset:8\n"
    "v_mfma_f32_16x16x32_bf16 v[168:171], a[8:11], a[80:83], v[168:171]\n"
    "buffer_load_dwordx4 v1, s[40:43], s70 offen offset:1024 lds\n"
    "v_mfma_f32_16x16x32_bf16 v[200:203], a[8:11], a[84:87], v[200:203]\n"
    "s_add_u32 s70, s70, 31744\n"
    "ds_read_b64_tr_b16 a[62:63], v15 offset:1032\n"
    "v_mfma_f32_16x16x32_bf16 v[232:235], a[8:11], a[88:91], v[232:235]\n"
    "ds_read_b128 a[92:95], v11 offset:0\n"
    "v_mfma_f32_16x16x32_bf16 v[44:47], a[12:15], a[64:67], v[44:47]\n"
    "ds_read_b128 a[96:99], v11 offset:2048\n"
    "v_mfma_f32_16x16x32_bf16 v[76:79], a[12:15], a[68:71], v[76:79]\n"
    "buffer_load_dwordx4 v2, s[40:43], s70 offen offset:2048 lds\n"
    "v_mfma_f32_16x16x32_bf16 v[108:111], a[12:15], a[72:75], v[108:111]\n"
    "s_waitcnt lgkmcnt(2)\n"
    "ds_read_b128 a[100:103], v11 offset:4096\n"
    "v_mfma_f32_16x16x32_bf16 v[140:143], a[12:15], a[76:79], v[140:143]\n"
    "s_add_u32 s70, s70, 31744\n"
    "ds_read_b128 a[104:107], v11 offset:6144\n"
    "v_mfma_f32_16x16x32_bf16 v[172:175], a[12:15], a[80:83], v[172:175]\n"
    "ds_read_b128 a[108:111], v11 offset:40960\n"
    "v_mfma_f32_16x16x32_bf16 v[204:207], a[12:15], a[84:87], v[204:207]\n"
    "ds_read_b128 a[112:115], v11 offset:43008\n"
    "v_mfma_f32_16x16x32_bf16 v[236:239], a[12:15], a[88:91], v[236:239]\n"
    "buffer_load_dwordx4 v2, s[40:43], s70 offen offset:3072 lds\n"
    "v_mfma_f32_16x16x32_bf16 v[48:51], a[16:19], a[64:67], v[48:51]\n"
    "s_add_u32 s70, s70, 35840\n"
    "ds_read_b128 a[116:119], v11 offset:45056\n"
    "v_mfma_f32_16x16x32_bf16 v[80:83], a[16:19], a[68:71], v[80:83]\n"
    "s_cmp_ge_u32 s70, 4718592\n"
    "v_mfma_f32_16x16x32_bf16 v[112:115], a[16:19], a[72:75], v[112:115]\n"
    "s_cselect_b32 s74, 4194304, 0\n"
    "v_mfma_f32_16x16x32_bf16 v[144:147], a[16:19], a[76:79], v[144:147]\n"
    "s_sub_u32 s70, s70, s74\n"
    "s_add_u32 m0, s60, 8192\n"
    "v_mfma_f32_16x16x32_bf16 v[176:179], a[16:19], a[80:83], v[176:179]\n"
    "buffer_load_dwordx4 v1, s[40:43], s70 offen offset:0 lds\n"
    "v_mfma_f32_16x16x32_bf16 v[208:211], a[16:19], a[84:87], v[208:211]\n"
    "s_add_u32 s70, s70, 31744\n"
    "v_mfma_f32_16x16x32_bf16 v[240:243], a[16:19], a[88:91], v[240:243]\n"
    "s_waitcnt lgkmcnt(0)\n"
    "v_mfma_f32_16x16x32_bf16 v[52:55], a[20:23], a[64:67], v[52:55]\n"
    "s_barrier\n"
    "v_mfma_f32_16x16x32_bf16 v[84:87], a[20:23], a[68:71], v[84:87]\n"
    "v_mfma_f32_16x16x32_bf16 v[116:119], a[20:23], a[72:75], v[116:119]\n"
    "buffer_load_dwordx4 v1, s[40:43], s70 offen offset:1024 lds\n"
    "v_mfma_f32_16x16x32_bf16 v[148:151], a[20:23], a[76:79], v[148:151]\n"
    "s_add_u32 s70, s70, 31744\n"
    "v_mfma_f32_16x16x32_bf16 v[180:183], a[20:23], a[80:83], v[180:183]\n"
    "v_mfma_f32_16x16x32_bf16 v[212:215], a[20:23], a[84:87], v[212:215]\n"
    "v_add_u32 v10, 4294885376, v10\n"
    "v_add_u32 v11, 4294885376, v11\n"
    "v_mfma_f32_16x16x32_bf16 v[244:247], a[20:23], a[88:91], v[244:247]\n"
    "v_mfma_f32_16x16x32_bf16 v[56:59], a[24:27], a[64:67], v[56:59]\n"
    "buffer_load_dwordx4 v2, s[40:43], s70 offen offset:2048 lds\n"
    "v_mfma_f32_16x16x32_bf16 v[88:91], a[24:27], a[68:71], v[88:91]\n"
    "s_add_u32 s70, s70, 31744\n"
    "v_mfma_f32_16x16x32_bf16 v[120:123], a[24:27], a[72:75], v[120:123]\n"
    "v_mfma_f32_16x16x32_bf16 v[152:155], a[24:27], a[76:79], v[152:155]\n"
    "v_mfma_f32_16x16x32_bf16 v[184:187], a[24:27], a[80:83], v[184:187]\n"
    "v_mfma_f32_16x16x32_bf16 v[216:219], a[24:27], a[84:87], v[216:219]\n"
    "buffer_load_dwordx4 v2, s[40:43], s70 offen offset:3072 lds\n"
    "v_mfma_f32_16x16x32_bf16 v[248:251], a[24:27], a[88:91], v[248:251]\n"
    "s_add_u32 s70, s70, 35840\n"
    "v_mfma_f32_16x16x32_bf16 v[60:63], a[28:31], a[64:67], v[60:63]\n"
    "s_add_u32 s80, s62, s77\n"
    "v_mfma_f32_16x16x32_bf16 v[92:95], a[28:31], a[68:71], v[92:95]\n"
    "s_add_u32 m0, s61, 81920\n"
    "v_mfma_f32_16x16x32_bf16 v[124:127], a[28:31], a[72:75], v[124:127]\n"
    "v_mfma_f32_16x16x32_bf16 v[156:159], a[28:31], a[76:79], v[156:159]\n"
    "buffer_load_dwordx4 v3, s[44:47], s80 offen offset:0 lds\n"
    "v_mfma_f32_16x16x32_bf16 v[188:191], a[28:31], a[80:83], v[188:191]\n"
    "s_add_u32 s80, s80, 8192\n"
    "v_mfma_f32_16x16x32_bf16 v[220:223], a[28:31], a[84:87], v[220:223]\n"
    "s_add_u32 s71, s63, 0\n"
    "v_mfma_f32_16x16x32_bf16 v[252:255], a[28:31], a[88:91], v[252:255]\n"
    "s_and_b32 s71, s71, 131071\n"
    "v_mfma_f32_16x16x32_bf16 v[32:35], a[32:35], a[92:95], v[32:35]\n"
    "s_waitcnt vmcnt(13)\n"
    "buffer_load_dwordx4 a[136:139], v5, s[48:51], s71 offen offset:0\n"
    "v_mfma_f32_16x16x32_bf16 v[64:67], a[32:35], a[96:99], v[64:67]\n"
    "ds_read_b64_tr_b16 a[0:1], v12 offset:16384\n"
    "v_mfma_f32_16x16x32_bf16 v[96:99], a[32:35], a[100:103], v[96:99]\n"
    "s_barrier\n"
    "ds_read_b64_tr_b16 a[2:3], v12 offset:17408\n"
    "v_mfma_f32_16x16x32_bf16 v[128:131], a[32:35], a[104:107], v[128:131]\n"
    "ds_read_b64_tr_b16 a[4:5], v12 offset:16392\n"
    "v_mfma_f32_16x16x32_bf16 v[160:163], a[32:35], a[108:111], v[160:163]\n"
    "ds_read_b64_tr_b16 a[6:7], v12 offset:17416\n"
    "v_mfma_f32_16x16x32_bf16 v[192:195], a[32:35], a[112:115], v[192:195]\n"
    "buffer_load_dwordx4 a[140:143], v6, s[48:51], s71 offen offset:1024\n"
    "v_mfma_f32_16x16x32_bf16 v[224:227], a[32:35], a[116:119], v[224:227]\n"
    "ds_read_b64_tr_b16 a[8:9], v13 offset:16384\n"
    "v_mfma_f32_16x16x32_bf16 v[36:39], a[36:39], a[92:95], v[36:39]\n"
    "ds_read_b64_tr_b16 a[10:11], v13 offset:17408\n"
    "v_mfma_f32_16x16x32_bf16 v[68:71], a[36:39], a[96:99], v[68:71]\n"
    "ds_read_b64_tr_b16 a[12:13], v13 offset:16392\n"
    "v_mfma_f32_16x16x32_bf16 v[100:103], a[36:39], a[100:103], v[100:103]\n"
    "ds_read_b64_tr_b16 a[14:15], v13 offset:17416\n"
    "v_mfma_f32_16x16x32_bf16 v[132:135], a[36:39], a[104:107], v[132:135]\n"
    "buffer_load_dwordx4 a[144:147], v7, s[48:51], s71 offen offset:2048\n"
    "v_mfma_f32_16x16x32_bf16 v[164:167], a[36:39], a[108:111], v[164:167]\n"
    "ds_read_b64_tr_b16 a[16:17], v14 offset:16384\n"
    "v_mfma_f32_16x16x32_bf16 v[196:199], a[36:39], a[112:115], v[196:199]\n"
    "ds_read_b64_tr_b16 a[18:19], v14 offset:17408\n"
    "v_mfma_f32_16x16x32_bf16 v[228:231], a[36:39], a[116:119], v[228:231]\n"
    "ds_read_b64_tr_b16 a[20:21], v14 offset:16392\n"
    "v_mfma_f32_16x16x32_bf16 v[40:43], a[40:43], a[92:95], v[40:43]\n"
    "ds_read_b64_tr_b16 a[22:23], v14 offset:17416\n"
    "v_mfma_f32_16x16x32_bf16 v[72:75], a[40:43], a[96:99], v[72:75]\n"
    "buffer_load_dwordx4 a[148:151], v8, s[48:51], s71 offen offset:3072\n"
    "v_mfma_f32_16x16x32_bf16 v[104:107], a[40:43], a[100:103], v[104:107]\n"
    "ds_read_b64_tr_b16 a[24:25], v15 offset:16384\n"
    "v_mfma_f32_16x16x32_bf16 v[136:139], a[40:43], a[104:107], v[136:139]\n"
    "ds_read_b64_tr_b16 a[26:27], v15 offset:17408\n"
    "v_mfma_f32_16x16x32_bf16 v[168:171], a[40:43], a[108:111], v[168:171]\n"
    "ds_read_b64_tr_b16 a[28:29], v15 offset:16392\n"
    "v_mfma_f32_16x16x32_bf16 v[200:203], a[40:43], a[112:115], v[200:203]\n"
    "ds_read_b64_tr_b16 a[30:31], v15 offset:17416\n"
    "v_mfma_f32_16x16x32_bf16 v[232:235], a[40:43], a[116:119], v[232:235]\n"
    "buffer_load_dwordx4 v4, s[44:47], s80 offen offset:1024 lds\n"
    "v_mfma_f32_16x16x32_bf16 v[44:47], a[44:47], a[92:95], v[44:47]\n"
    "s_add_u32 s80, s80, 8192\n"
    "ds_read_b128 a[64:67], v10 offset:0\n"
    "v_mfma_f32_16x16x32_bf16 v[76:79], a[44:47], a[96:99], v[76:79]\n"
    "ds_read_b128 a[68:71], v10 offset:2048\n"
    "v_mfma_f32_16x16x32_bf16 v[108:111], a[44:47], a[100:103], v[108:111]\n"
    "s_waitcnt lgkmcnt(2)\n"
    "ds_read_b128 a[72:75], v10 offset:4096\n"
    "v_mfma_f32_16x16x32_bf16 v[140:143], a[44:47], a[104:107], v[140:143]\n"
    "ds_read_b128 a[76:79], v10 offset:6144\n"
    "v_mfma_f32_16x16x32_bf16 v[172:175], a[44:47], a[108:111], v[172:175]\n"
    "buffer_load_dwordx4 v3, s[44:47], s80 offen offset:2048 lds\n"
    "v_mfma_f32_16x16x32_bf16 v[204:207], a[44:47], a[112:115], v[204:207]\n"
    "s_add_u32 s80, s80, 8192\n"
    "ds_read_b128 a[80:83], v10 offset:40960\n"
    "v_mfma_f32_16x16x32_bf16 v[236:239], a[44:47], a[116:119], v[236:239]\n"
    "ds_read_b128 a[84:87], v10 offset:43008\n"
    "v_mfma_f32_16x16x32_bf16 v[48:51], a[48:51], a[92:95], v[48:51]\n"
    "ds_read_b128 a[88:91], v10 offset:45056\n"
    "v_mfma_f32_16x16x32_bf16 v[80:83], a[48:51], a[96:99], v[80:83]\n"
    "buffer_load_dwordx4 v4, s[44:47], s80 offen offset:3072 lds\n"
    "v_mfma_f32_16x16x32_bf16 v[112:115], a[48:51], a[100:103], v[112:115]\n"
    "s_add_u32 s77, s77, 128\n"
    "v_mfma_f32_16x16x32_bf16 v[144:147], a[48:51], a[104:107], v[144:147]\n"
    "s_cmp_ge_u32 s77, 1152\n"
    "v_mfma_f32_16x16x32_bf16 v[176:179], a[48:51], a[108:111], v[176:179]\n"
    "s_cselect_b32 s74, 1024, 0\n"
    "v_mfma_f32_16x16x32_bf16 v[208:211], a[48:51], a[112:115], v[208:211]\n"
    "s_sub_u32 s77, s77, s74\n"
    "s_add_u32 m0, s60, 12288\n"
    "v_mfma_f32_16x16x32_bf16 v[240:243], a[48:51], a[116:119], v[240:243]\n"
    "s_waitcnt lgkmcnt(0)\n"
    "buffer_load_dwordx4 v1, s[40:43], s70 offen offset:0 lds\n"
    "v_mfma_f32_16x16x32_bf16 v[52:55], a[52:55], a[92:95], v[52:55]\n"
    "s_add_u32 s70, s70, 31744\n"
    "v_mfma_f32_16x16x32_bf16 v[84:87], a[52:55], a[96:99], v[84:87]\n"
    "v_mfma_f32_16x16x32_bf16 v[116:119], a[52:55], a[100:103], v[116:119]\n"
    "v_mfma_f32_16x16x32_bf16 v[148:151], a[52:55], a[104:107], v[148:151]\n"
    "v_mfma_f32_16x16x32_bf16 v[180:183], a[52:55], a[108:111], v[180:183]\n"
    "buffer_load_dwordx4 v1, s[40:43], s70 offen offset:1024 lds\n"
    "v_mfma_f32_16x16x32_bf16 v[212:215], a[52:55], a[112:115], v[212:215]\n"
    "s_add_u32 s70, s70, 31744\n"
    "v_mfma_f32_16x16x32_bf16 v[244:247], a[52:55], a[116:119], v[244:247]\n"
    "v_mfma_f32_16x16x32_bf16 v[56:59], a[56:59], a[92:95], v[56:59]\n"
    "v_mfma_f32_16x16x32_bf16 v[88:91], a[56:59], a[96:99], v[88:91]\n"
    "v_mfma_f32_16x16x32_bf16 v[120:123], a[56:59], a[100:103], v[120:123]\n"
    "buffer_load_dwordx4 v2, s[40:43], s70 offen offset:2048 lds\n"
    "v_mfma_f32_16x16x32_bf16 v[152:155], a[56:59], a[104:107], v[152:155]\n"
    "s_add_u32 s70, s70, 31744\n"
    "v_mfma_f32_16x16x32_bf16 v[184:187], a[56:59], a[108:111], v[184:187]\n"
    "v_mfma_f32_16x16x32_bf16 v[216:219], a[56:59], a[112:115], v[216:219]\n"
    "v_mfma_f32_16x16x32_bf16 v[248:251], a[56:59], a[116:119], v[248:251]\n"
    "v_mfma_f32_16x16x32_bf16 v[60:63], a[60:63], a[92:95], v[60:63]\n"
    "buffer_load_dwordx4 v2, s[40:43], s70 offen offset:3072 lds\n"
    "v_mfma_f32_16x16x32_bf16 v[92:95], a[60:63], a[96:99], v[92:95]\n"
    "s_add_u32 s70, s70, 35840\n"
    "v_mfma_f32_16x16x32_bf16 v[124:127], a[60:63], a[100:103], v[124:127]\n"
    "s_add_u32 m0, s60, 0\n"
    "v_mfma_f32_16x16x32_bf16 v[156:159], a[60:63], a[104:107], v[156:159]\n"
    "v_mfma_f32_16x16x32_bf16 v[188:191], a[60:63], a[108:111], v[188:191]\n"
    "v_mfma_f32_16x16x32_bf16 v[220:223], a[60:63], a[112:115], v[220:223]\n"
    "buffer_load_dwordx4 v1, s[40:43], s70 offen offset:0 lds\n"
    "v_mfma_f32_16x16x32_bf16 v[252:255], a[60:63], a[116:119], v[252:255]\n"
    "s_add_u32 s70, s70, 31744\n"
    "v_mfma_f32_16x16x32_bf16 v[32:35], a[0:3], a[64:67], v[32:35]\n"
    "s_waitcnt vmcnt(17)\n"
    "ds_read_b64_tr_b16 a[32:33], v12 offset:24576\n"
    "v_mfma_f32_16x16x32_bf16 v[64:67], a[0:3], a[68:71], v[64:67]\n"
    "ds_read_b64_tr_b16 a[34:35], v12 offset:25600\n"
    "v_mfma_f32_16x16x32_bf16 v[96:99], a[0:3], a[72:75], v[96:99]\n"
    "ds_read_b64_tr_b16 a[36:37], v12 offset:24584\n"
    "v_mfma_f32_16x16x32_bf16 v[128:131], a[0:3], a[76:79], v[128:131]\n"
    "buffer_load_dwordx4 v1, s[40:43], s70 offen offset:1024 lds\n"
    "v_mfma_f32_16x16x32_bf16 v[160:163], a[0:3], a[80:83], v[160:163]\n"
    "s_add_u32 s70, s70, 31744\n"
    "ds_read_b64_tr_b16 a[38:39], v12 offset:25608\n"
    "v_mfma_f32_16x16x32_bf16 v[192:195], a[0:3], a[84:87], v[192:195]\n"
    "ds_read_b64_tr_b16 a[40:41], v13 offset:24576\n"
    "v_mfma_f32_16x16x32_bf16 v[224:227], a[0:3], a[88:91], v[224:227]\n"
    "ds_read_b64_tr_b16 a[42:43], v13 offset:25600\n"
    "v_mfma_f32_16x16x32_bf16 v[36:39], a[4:7], a[64:67], v[36:39]\n"
    "buffer_load_dwordx4 v2, s[40:43], s70 offen offset:2048 lds\n"
    "v_mfma_f32_16x16x32_bf16 v[68:71], a[4:7], a[68:71], v[68:71]\n"
    "s_add_u32 s70, s70, 31744\n"
    "ds_read_b64_tr_b16 a[44:45], v13 offset:24584\n"
    "v_mfma_f32_16x16x32_bf16 v[100:103], a[4:7], a[72:75], v[100:103]\n"
    "ds_read_b64_tr_b16 a[46:47], v13 offset:25608\n"
    "v_mfma_f32_16x16x32_bf16 v[132:135], a[4:7], a[76:79], v[132:135]\n"
    "ds_read_b64_tr_b16 a[48:49], v14 offset:24576\n"
    "v_mfma_f32_16x16x32_bf16 v[164:167], a[4:7], a[80:83], v[164:167]\n"
    "ds_read_b64_tr_b16 a[50:51], v14 offset:25600\n"
    "v_mfma_f32_16x16x32_bf16 v[196:199], a[4:7], a[84:87], v[196:199]\n"
    "buffer_load_dwordx4 v2, s[40:43], s70 offen offset:3072 lds\n"
    "v_mfma_f32_16x16x32_bf16 v[228:231], a[4:7], a[88:91], v[228:231]\n"
    "s_add_u32 s70, s70, 35840\n"
    "ds_read_b64_tr_b16 a[52:53], v14 offset:24584\n"
    "v_mfma_f32_16x16x32_bf16 v[40:43], a[8:11], a[64:67], v[40:43]\n"
    "s_add_u32 m0, s60, 4096\n"
    "ds_read_b64_tr_b16 a[54:55], v14 offset:25608\n"
    "v_mfma_f32_16x16x32_bf16 v[72:75], a[8:11], a[68:71], v[72:75]\n"
    "ds_read_b64_tr_b16 a[56:57], v15 offset:24576\n"
    "v_mfma_f32_16x16x32_bf16 v[104:107], a[8:11], a[72:75], v[104:107]\n"
    "ds_read_b64_tr_b16 a[58:59], v15 offset:25600\n"
    "v_mfma_f32_16x16x32_bf16 v[136:139], a[8:11], a[76:79], v[136:139]\n"
    "buffer_load_dwordx4 v1, s[40:43], s70 offen offset:0 lds\n"
    "v_mfma_f32_16x16x32_bf16 v[168:171], a[8:11], a[80:83], v[168:171]\n"
    "s_add_u32 s70, s70, 31744\n"
    "ds_read_b64_tr_b16 a[60:61], v15 offset:24584\n"
    "v_mfma_f32_16x16x32_bf16 v[200:203], a[8:11], a[84:87], v[200:203]\n"
    "ds_read_b64_tr_b16 a[62:63], v15 offset:25608\n"
    "v_mfma_f32_16x16x32_bf16 v[232:235], a[8:11], a[88:91], v[232:235]\n"
    "ds_read_b128 a[92:95], v11 offset:0\n"
    "v_mfma_f32_16x16x32_bf16 v[44:47], a[12:15], a[64:67], v[44:47]\n"
    "ds_read_b128 a[96:99], v11 offset:2048\n"
    "v_mfma_f32_16x16x32_bf16 v[76:79], a[12:15], a[68:71], v[76:79]\n"
    "buffer_load_dwordx4 v1, s[40:43], s70 offen offset:1024 lds\n"
    "v_mfma_f32_16x16x32_bf16 v[108:111], a[12:15], a[72:75], v[108:111]\n"
    "s_waitcnt lgkmcnt(2)\n"
    "ds_read_b128 a[100:103], v11 offset:4096\n"
    "v_mfma_f32_16x16x32_bf16 v[140:143], a[12:15], a[76:79], v[140:143]\n"
    "s_add_u32 s70, s70, 31744\n"
    "ds_read_b128 a[104:107], v11 offset:6144\n"
    "v_mfma_f32_16x16x32_bf16 v[172:175], a[12:15], a[80:83], v[172:175]\n"
    "ds_read_b128 a[108:111], v11 offset:40960\n"
    "v_mfma_f32_16x16x32_bf16 v[204:207], a[12:15], a[84:87], v[204:207]\n"
    "ds_read_b128 a[112:115], v11 offset:43008\n"
    "v_mfma_f32_16x16x32_bf16 v[236:239], a[12:15], a[88:91], v[236:239]\n"
    "buffer_load_dwordx4 v2, s[40:43], s70 offen offset:2048 lds\n"
    "v_mfma_f32_16x16x32_bf16 v[48:51], a[16:19], a[64:67], v[48:51]\n"
    "s_add_u32 s70, s70, 31744\n"
    "ds_read_b128 a[116:119], v11 offset:45056\n"
    "v_mfma_f32_16x16x32_bf16 v[80:83], a[16:19], a[68:71], v[80:83]\n"
    "v_mfma_f32_16x16x32_bf16 v[112:115], a[16:19], a[72:75], v[112:115]\n"
    "v_mfma_f32_16x16x32_bf16 v[144:147], a[16:19], a[76:79], v[144:147]\n"
    "v_mfma_f32_16x16x32_bf16 v[176:179], a[16:19], a[80:83], v[176:179]\n"
    "buffer_load_dwordx4 v2, s[40:43], s70 offen offset:3072 lds\n"
    "v_mfma_f32_16x16x32_bf16 v[208:211], a[16:19], a[84:87], v[208:211]\n"
    "s_add_u32 s70, s70, 35840\n"
    "v_mfma_f32_16x16x32_bf16 v[240:243], a[16:19], a[88:91], v[240:243]\n"
    "s_waitcnt lgkmcnt(0)\n"
    "v_mfma_f32_16x16x32_bf16 v[52:55], a[20:23], a[64:67], v[52:55]\n"
    "s_barrier\n"
    "v_mfma_f32_16x16x32_bf16 v[84:87], a[20:23], a[68:71], v[84:87]\n"
    "s_cmp_ge_u32 s70, 4718592\n"
    "s_cselect_b32 s74, 4194304, 0\n"
    "s_sub_u32 s70, s70, s74\n"
    "s_add_u32 s80, s62, s77\n"
    "s_add_u32 m0, s61, 0\n"
    "v_mfma_f32_16x16x32_bf16 v[116:119], a[20:23], a[72:75], v[116:119]\n"
    "buffer_load_dwordx4 v3, s[44:47], s80 offen offset:0 lds\n"
    "v_mfma_f32_16x16x32_bf16 v[148:151], a[20:23], a[76:79], v[148:151]\n"
    "s_add_u32 s80, s80, 8192\n"
    "v_mfma_f32_16x16x32_bf16 v[180:183], a[20:23], a[80:83], v[180:183]\n"
    "v_mfma_f32_16x16x32_bf16 v[212:215], a[20:23], a[84:87], v[212:215]\n"
    "v_add_u32 v10, 81920, v10\n"
    "v_add_u32 v11, 81920, v11\n"
    "v_mfma_f32_16x16x32_bf16 v[244:247], a[20:23], a[88:91], v[244:247]\n"
    "buffer_load_dwordx4 v4, s[44:47], s80 offen offset:1024 lds\n"
    "v_mfma_f32_16x16x32_bf16 v[56:59], a[24:27], a[64:67], v[56:59]\n"
    "s_add_u32 s80, s80, 8192\n"
    "v_mfma_f32_16x16x32_bf16 v[88:91], a[24:27], a[68:71], v[88:91]\n"
    "v_mfma_f32_16x16x32_bf16 v[120:123], a[24:27], a[72:75], v[120:123]\n"
    "v_mfma_f32_16x16x32_bf16 v[152:155], a[24:27], a[76:79], v[152:155]\n"
    "v_mfma_f32_16x16x32_bf16 v[184:187], a[24:27], a[80:83], v[184:187]\n"
    "buffer_load_dwordx4 v3, s[44:47], s80 offen offset:2048 lds\n"
    "v_mfma_f32_16x16x32_bf16 v[216:219], a[24:27], a[84:87], v[216:219]\n"
    "s_add_u32 s80, s80, 8192\n"
    "v_mfma_f32_16x16x32_bf16 v[248:251], a[24:27], a[88:91], v[248:251]\n"
    "v_mfma_f32_16x16x32_bf16 v[60:63], a[28:31], a[64:67], v[60:63]\n"
    "v_mfma_f32_16x16x32_bf16 v[92:95], a[28:31], a[68:71], v[92:95]\n"
    "v_mfma_f32_16x16x32_bf16 v[124:127], a[28:31], a[72:75], v[124:127]\n"
    "buffer_load_dwordx4 v4, s[44:47], s80 offen offset:3072 lds\n"
    "v_mfma_f32_16x16x32_bf16 v[156:159], a[28:31], a[76:79], v[156:159]\n"
    "s_add_u32 s77, s77, 128\n"
    "v_mfma_f32_16x16x32_bf16 v[188:191], a[28:31], a[80:83], v[188:191]\n"
    "s_cmp_ge_u32 s77, 1152\n"
    "v_mfma_f32_16x16x32_bf16 v[220:223], a[28:31], a[84:87], v[220:223]\n"
    "s_cselect_b32 s74, 1024, 0\n"
    "v_mfma_f32_16x16x32_bf16 v[252:255], a[28:31], a[88:91], v[252:255]\n"
    "s_sub_u32 s77, s77, s74\n"
    "s_add_u32 s71, s63, 4096\n"
    "s_and_b32 s71, s71, 131071\n"
    "v_mfma_f32_16x16x32_bf16 v[32:35], a[32:35], a[92:95], v[32:35]\n"
    "s_waitcnt vmcnt(12)\n"
    "buffer_load_dwordx4 a[152:155], v5, s[48:51], s71 offen offset:0\n"
    "v_mfma_f32_16x16x32_bf16 v[64:67], a[32:35], a[96:99], v[64:67]\n"
    "ds_read_b64_tr_b16 a[0:1], v12 offset:8192\n"
    "v_mfma_f32_16x16x32_bf16 v[96:99], a[32:35], a[100:103], v[96:99]\n"
    "s_barrier\n"
    "ds_read_b64_tr_b16 a[2:3], v12 offset:9216\n"
    "v_mfma_f32_16x16x32_bf16 v[128:131], a[32:35], a[104:107], v[128:131]\n"
    "ds_read_b64_tr_b16 a[4:5], v12 offset:8200\n"
    "v_mfma_f32_16x16x32_bf16 v[160:163], a[32:35], a[108:111], v[160:163]\n"
    "ds_read_b64_tr_b16 a[6:7], v12 offset:9224\n"
    "v_mfma_f32_16x16x32_bf16 v[192:195], a[32:35], a[112:115], v[192:195]\n"
    "buffer_load_dwordx4 a[156:159], v6, s[48:51], s71 offen offset:1024\n"
    "v_mfma_f32_16x16x32_bf16 v[224:227], a[32:35], a[116:119], v[224:227]\n"
    "ds_read_b64_tr_b16 a[8:9], v13 offset:8192\n"
    "v_mfma_f32_16x16x32_bf16 v[36:39], a[36:39], a[92:95], v[36:39]\n"
    "ds_read_b64_tr_b16 a[10:11], v13 offset:9216\n"
    "v_mfma_f32_16x16x32_bf16 v[68:71], a[36:39], a[96:99], v[68:71]\n"
    "ds_read_b64_tr_b16 a[12:13], v13 offset:8200\n"
    "v_mfma_f32_16x16x32_bf16 v[100:103], a[36:39], a[100:103], v[100:103]\n"
    "ds_read_b64_tr_b16 a[14:15], v13 offset:9224\n"
    "v_mfma_f32_16x16x32_bf16 v[132:135], a[36:39], a[104:107], v[132:135]\n"
    "buffer_load_dwordx4 a[160:163], v7, s[48:51], s71 offen offset:2048\n"
    "v_mfma_f32_16x16x32_bf16 v[164:167], a[36:39], a[108:111], v[164:167]\n"
    "ds_read_b64_tr_b16 a[16:17], v14 offset:8192\n"
    "v_mfma_f32_16x16x32_bf16 v[196:199], a[36:39], a[112:115], v[196:199]\n"
    "ds_read_b64_tr_b16 a[18:19], v14 offset:9216\n"
    "v_mfma_f32_16x16x32_bf16 v[228:231], a[36:39], a[116:119], v[228:231]\n"
    "ds_read_b64_tr_b16 a[20:21], v14 offset:8200\n"
    "v_mfma_f32_16x16x32_bf16 v[40:43], a[40:43], a[92:95], v[40:43]\n"
    "buffer_load_dwordx4 a[164:167], v8, s[48:51], s71 offen offset:3072\n"
    "v_mfma_f32_16x16x32_bf16 v[72:75], a[40:43], a[96:99], v[72:75]\n"
    "s_add_u32 m0, s60, 16384\n"
    "ds_read_b64_tr_b16 a[22:23], v14 offset:9224\n"
    "v_mfma_f32_16x16x32_bf16 v[104:107], a[40:43], a[100:103], v[104:107]\n"
    "ds_read_b64_tr_b16 a[24:25], v15 offset:8192\n"
    "v_mfma_f32_16x16x32_bf16 v[136:139], a[40:43], a[104:107], v[136:139]\n"
    "ds_read_b64_tr_b16 a[26:27], v15 offset:9216\n"
    "v_mfma_f32_16x16x32_bf16 v[168:171], a[40:43], a[108:111], v[168:171]\n"
    "ds_read_b64_tr_b16 a[28:29], v15 offset:8200\n"
    "v_mfma_f32_16x16x32_bf16 v[200:203], a[40:43], a[112:115], v[200:203]\n"
    "buffer_load_dwordx4 v1, s[40:43], s70 offen offset:0 lds\n"
    "v_mfma_f32_16x16x32_bf16 v[232:235], a[40:43], a[116:119], v[232:235]\n"
    "s_add_u32 s70, s70, 31744\n"
    "ds_read_b64_tr_b16 a[30:31], v15 offset:9224\n"
    "v_mfma_f32_16x16x32_bf16 v[44:47], a[44:47], a[92:95], v[44:47]\n"
    "ds_read_b128 a[64:67], v10 offset:0\n"
    "v_mfma_f32_16x16x32_bf16 v[76:79], a[44:47], a[96:99], v[76:79]\n"
    "ds_read_b128 a[68:71], v10 offset:2048\n"
    "v_mfma_f32_16x16x32_bf16 v[108:111], a[44:47], a[100:103], v[108:111]\n"
    "s_waitcnt lgkmcnt(2)\n"
    "ds_read_b128 a[72:75], v10 offset:4096\n"
    "v_mfma_f32_16x16x32_bf16 v[140:143], a[44:47], a[104:107], v[140:143]\n"
    "buffer_load_dwordx4 v1, s[40:43], s70 offen offset:1024 lds\n"
    "v_mfma_f32_16x16x32_bf16 v[172:175], a[44:47], a[108:111], v[172:175]\n"
    "s_add_u32 s70, s70, 31744\n"
    "ds_read_b128 a[76:79], v10 offset:6144\n"
    "v_mfma_f32_16x16x32_bf16 v[204:207], a[44:47], a[112:115], v[204:207]\n"
    "ds_read_b128 a[80:83], v10 offset:40960\n"
    "v_mfma_f32_16x16x32_bf16 v[236:239], a[44:47], a[116:119], v[236:239]\n"
    "ds_read_b128 a[84:87], v10 offset:43008\n"
    "v_mfma_f32_16x16x32_bf16 v[48:51], a[48:51], a[92:95], v[48:51]\n"
    "ds_read_b128 a[88:91], v10 offset:45056\n"
    "v_mfma_f32_16x16x32_bf16 v[80:83], a[48:51], a[96:99], v[80:83]\n"
    "buffer_load_dwordx4 v2, s[40:43], s70 offen offset:2048 lds\n"
    "v_mfma_f32_16x16x32_bf16 v[112:115], a[48:51], a[100:103], v[112:115]\n"
    "s_add_u32 s70, s70, 31744\n"
    "v_mfma_f32_16x16x32_bf16 v[144:147], a[48:51], a[104:107], v[144:147]\n"
    "v_mfma_f32_16x16x32_bf16 v[176:179], a[48:51], a[108:111], v[176:179]\n"
    "v_mfma_f32_16x16x32_bf16 v[208:211], a[48:51], a[112:115], v[208:211]\n"
    "v_mfma_f32_16x16x32_bf16 v[240:243], a[48:51], a[116:119], v[240:243]\n"
    "s_waitcnt lgkmcnt(0)\n"
    "buffer_load_dwordx4 v2, s[40:43], s70 offen offset:3072 lds\n"
    "v_mfma_f32_16x16x32_bf16 v[52:55], a[52:55], a[92:95], v[52:55]\n"
    "s_add_u32 s70, s70, 35840\n"
    "v_mfma_f32_16x16x32_bf16 v[84:87], a[52:55], a[96:99], v[84:87]\n"
    "s_add_u32 m0, s60, 20480\n"
    "v_mfma_f32_16x16x32_bf16 v[116:119], a[52:55], a[100:103], v[116:119]\n"
    "v_mfma_f32_16x16x32_bf16 v[148:151], a[52:55], a[104:107], v[148:151]\n"
    "v_mfma_f32_16x16x32_bf16 v[180:183], a[52:55], a[108:111], v[180:183]\n"
    "buffer_load_dwordx4 v1, s[40:43], s70 offen offset:0 lds\n"
    "v_mfma_f32_16x16x32_bf16 v[212:215], a[52:55], a[112:115], v[212:215]\n"
    "s_add_u32 s70, s70, 31744\n"
    "v_mfma_f32_16x16x32_bf16 v[244:247], a[52:55], a[116:119], v[244:247]\n"
    "v_mfma_f32_16x16x32_bf16 v[56:59], a[56:59], a[92:95], v[56:59]\n"
    "v_mfma_f32_16x16x32_bf16 v[88:91], a[56:59], a[96:99], v[88:91]\n"
    "v_mfma_f32_16x16x32_bf16 v[120:123], a[56:59], a[100:103], v[120:123]\n"
    "buffer_load_dwordx4 v1, s[40:43], s70 offen offset:1024 lds\n"
    "v_mfma_f32_16x16x32_bf16 v[152:155], a[56:59], a[104:107], v[152:155]\n"
    "s_add_u32 s70, s70, 31744\n"
    "v_mfma_f32_16x16x32_bf16 v[184:187], a[56:59], a[108:111], v[184:187]\n"
    "v_mfma_f32_16x16x32_bf16 v[216:219], a[56:59], a[112:115], v[216:219]\n"
    "v_mfma_f32_16x16x32_bf16 v[248:251], a[56:59], a[116:119], v[248:251]\n"
    "buffer_load_dwordx4 v2, s[40:43], s70 offen offset:2048 lds\n"
    "v_mfma_f32_16x16x32_bf16 v[60:63], a[60:63], a[92:95], v[60:63]\n"
    "s_add_u32 s70, s70, 31744\n"
    "v_mfma_f32_16x16x32_bf16 v[92:95], a[60:63], a[96:99], v[92:95]\n"
    "v_mfma_f32_16x16x32_bf16 v[124:127], a[60:63], a[100:103], v[124:127]\n"
    "v_mfma_f32_16x16x32_bf16 v[156:159], a[60:63], a[104:107], v[156:159]\n"
    "v_mfma_f32_16x16x32_bf16 v[188:191], a[60:63], a[108:111], v[188:191]\n"
    "buffer_load_dwordx4 v2, s[40:43], s70 offen offset:3072 lds\n"
    "v_mfma_f32_16x16x32_bf16 v[220:223], a[60:63], a[112:115], v[220:223]\n"
    "s_add_u32 s70, s70, 35840\n"
    "v_mfma_f32_16x16x32_bf16 v[252:255], a[60:63], a[116:119], v[252:255]\n"
    "s_add_u32 m0, s60, 24576\n"
    "v_mfma_f32_16x16x32_bf16 v[32:35], a[0:3], a[64:67], v[32:35]\n"
    "s_waitcnt vmcnt(16)\n"
    "ds_read_b64_tr_b16 a[32:33], v12 offset:0\n"
    "v_mfma_f32_16x16x32_bf16 v[64:67], a[0:3], a[68:71], v[64:67]\n"
    "ds_read_b64_tr_b16 a[34:35], v12 offset:1024\n"
    "v_mfma_f32_16x16x32_bf16 v[96:99], a[0:3], a[72:75], v[96:99]\n"
    "buffer_load_dwordx4 v1, s[40:43], s70 offen offset:0 lds\n"
    "v_mfma_f32_16x16x32_bf16 v[128:131], a[0:3], a[76:79], v[128:131]\n"
    "s_add_u32 s70, s70, 31744\n"
    "ds_read_b64_tr_b16 a[36:37], v12 offset:8\n"
    "v_mfma_f32_16x16x32_bf16 v[160:163], a[0:3], a[80:83], v[160:163]\n"
    "ds_read_b64_tr_b16 a[38:39], v12 offset:1032\n"
    "v_mfma_f32_16x16x32_bf16 v[192:195], a[0:3], a[84:87], v[192:195]\n"
    "ds_read_b64_tr_b16 a[40:41], v13 offset:0\n"
    "v_mfma_f32_16x16x32_bf16 v[224:227], a[0:3], a[88:91], v[224:227]\n"
    "ds_read_b64_tr_b16 a[42:43], v13 offset:1024\n"
    "v_mfma_f32_16x16x32_bf16 v[36:39], a[4:7], a[64:67], v[36:39]\n"
    "buffer_load_dwordx4 v1, s[40:43], s70 offen offset:1024 lds\n"
    "v_mfma_f32_16x16x32_bf16 v[68:71], a[4:7], a[68:71], v[68:71]\n"
    "s_add_u32 s70, s70, 31744\n"
    "ds_read_b64_tr_b16 a[44:45], v13 offset:8\n"
    "v_mfma_f32_16x16x32_bf16 v[100:103], a[4:7], a[72:75], v[100:103]\n"
    "ds_read_b64_tr_b16 a[46:47], v13 offset:1032\n"
    "v_mfma_f32_16x16x32_bf16 v[132:135], a[4:7], a[76:79], v[132:135]\n"
    "ds_read_b64_tr_b16 a[48:49], v14 offset:0\n"
    "v_mfma_f32_16x16x32_bf16 v[164:167], a[4:7], a[80:83], v[164:167]\n"
    "ds_read_b64_tr_b16 a[50:51], v14 offset:1024\n"
    "v_mfma_f32_16x16x32_bf16 v[196:199], a[4:7], a[84:87], v[196:199]\n"
    "buffer_load_dwordx4 v2, s[40:43], s70 offen offset:2048 lds\n"
    "v_mfma_f32_16x16x32_bf16 v[228:231], a[4:7], a[88:91], v[228:231]\n"
    "s_add_u32 s70, s70, 31744\n"
    "ds_read_b64_tr_b16 a[52:53], v14 offset:8\n"
    "v_mfma_f32_16x16x32_bf16 v[40:43], a[8:11], a[64:67], v[40:43]\n"
    "ds_read_b64_tr_b16 a[54:55], v14 offset:1032\n"
    "v_mfma_f32_16x16x32_bf16 v[72:75], a[8:11], a[68:71], v[72:75]\n"
    "ds_read_b64_tr_b16 a[56:57], v15 offset:0\n"
    "v_mfma_f32_16x16x32_bf16 v[104:107], a[8:11], a[72:75], v[104:107]\n"
    "ds_read_b64_tr_b16 a[58:59], v15 offset:1024\n"
    "v_mfma_f32_16x16x32_bf16 v[136:139], a[8:11], a[76:79], v[136:139]\n"
    "buffer_load_dwordx4 v2, s[40:43], s70 offen offset:3072 lds\n"
    "v_mfma_f32_16x16x32_bf16 v[168:171], a[8:11], a[80:83], v[168:171]\n"
    "s_add_u32 s70, s70, 35840\n"
    "ds_read_b64_tr_b16 a[60:61], v15 offset:8\n"
    "v_mfma_f32_16x16x32_bf16 v[200:203], a[8:11], a[84:87], v[200:203]\n"
    "s_add_u32 m0, s60, 28672\n"
    "ds_read_b64_tr_b16 a[62:63], v15 offset:1032\n"
    "v_mfma_f32_16x16x32_bf16 v[232:235], a[8:11], a[88:91], v[232:235]\n"
    "ds_read_b128 a[92:95], v11 offset:0\n"
    "v_mfma_f32_16x16x32_bf16 v[44:47], a[12:15], a[64:67], v[44:47]\n"
    "ds_read_b128 a[96:99], v11 offset:2048\n"
    "v_mfma_f32_16x16x32_bf16 v[76:79], a[12:15], a[68:71], v[76:79]\n"
    "buffer_load_dwordx4 v1, s[40:43], s70 offen offset:0 lds\n"
    "v_mfma_f32_16x16x32_bf16 v[108:111], a[12:15], a[72:75], v[108:111]\n"
    "s_waitcnt lgkmcnt(2)\n"
    "ds_read_b128 a[100:103], v11 offset:4096\n"
    "v_mfma_f32_16x16x32_bf16 v[140:143], a[12:15], a[76:79], v[140:143]\n"
    "s_add_u32 s70, s70, 31744\n"
    "ds_read_b128 a[104:107], v11 offset:6144\n"
    "v_mfma_f32_16x16x32_bf16 v[172:175], a[12:15], a[80:83], v[172:175]\n"
    "ds_read_b128 a[108:111], v11 offset:40960\n"
    "v_mfma_f32_16x16x32_bf16 v[204:207], a[12:15], a[84:87], v[204:207]\n"
    "buffer_load_dwordx4 v1, s[40:43], s70 offen offset:1024 lds\n"
    "v_mfma_f32_16x16x32_bf16 v[236:239], a[12:15], a[88:91], v[236:239]\n"
    "s_add_u32 s70, s70, 31744\n"
    "ds_read_b128 a[112:115], v11 offset:43008\n"
    "v_mfma_f32_16x16x32_bf16 v[48:51], a[16:19], a[64:67], v[48:51]\n"
    "ds_read_b128 a[116:119], v11 offset:45056\n"
    "v_mfma_f32_16x16x32_bf16 v[80:83], a[16:19], a[68:71], v[80:83]\n"
    "v_mfma_f32_16x16x32_bf16 v[112:115], a[16:19], a[72:75], v[112:115]\n"
    "v_mfma_f32_16x16x32_bf16 v[144:147], a[16:19], a[76:79], v[144:147]\n"
    "buffer_load_dwordx4 v2, s[40:43], s70 offen offset:2048 lds\n"
    "v_mfma_f32_16x16x32_bf16 v[176:179], a[16:19], a[80:83], v[176:179]\n"
    "s_add_u32 s70, s70, 31744\n"
    "v_mfma_f32_16x16x32_bf16 v[208:211], a[16:19], a[84:87], v[208:211]\n"
    "v_mfma_f32_16x16x32_bf16 v[240:243], a[16:19], a[88:91], v[240:243]\n"
    "s_waitcnt lgkmcnt(0)\n"
    "v_mfma_f32_16x16x32_bf16 v[52:55], a[20:23], a[64:67], v[52:55]\n"
    "s_barrier\n"
    "v_mfma_f32_16x16x32_bf16 v[84:87], a[20:23], a[68:71], v[84:87]\n"
    "buffer_load_dwordx4 v2, s[40:43], s70 offen offset:3072 lds\n"
    "v_mfma_f32_16x16x32_bf16 v[116:119], a[20:23], a[72:75], v[116:119]\n"
    "s_add_u32 s70, s70, 35840\n"
    "v_mfma_f32_16x16x32_bf16 v[148:151], a[20:23], a[76:79], v[148:151]\n"
    "s_cmp_ge_u32 s70, 4718592\n"
    "v_mfma_f32_16x16x32_bf16 v[180:183], a[20:23], a[80:83], v[180:183]\n"
    "s_cselect_b32 s74, 4194304, 0\n"
    "v_mfma_f32_16x16x32_bf16 v[212:215], a[20:23], a[84:87], v[212:215]\n"
    "s_sub_u32 s70, s70, s74\n"
    "s_add_u32 s80, s62, s77\n"
    "s_add_u32 m0, s61, 81920\n"
    "v_mfma_f32_16x16x32_bf16 v[244:247], a[20:23], a[88:91], v[244:247]\n"
    "buffer_load_dwordx4 v3, s[44:47], s80 offen offset:0 lds\n"
    "v_mfma_f32_16x16x32_bf16 v[56:59], a[24:27], a[64:67], v[56:59]\n"
    "s_add_u32 s80, s80, 8192\n"
    "v_add_u32 v10, 4294885376, v10\n"
    "v_add_u32 v11, 4294885376, v11\n"
    "v_mfma_f32_16x16x32_bf16 v[88:91], a[24:27], a[68:71], v[88:91]\n"
    "v_mfma_f32_16x16x32_bf16 v[120:123], a[24:27], a[72:75], v[120:123]\n"
    "v_mfma_f32_16x16x32_bf16 v[152:155], a[24:27], a[76:79], v[152:155]\n"
    "v_mfma_f32_16x16x32_bf16 v[184:187], a[24:27], a[80:83], v[184:187]\n"
    "buffer_load_dwordx4 v4, s[44:47], s80 offen offset:1024 lds\n"
    "v_mfma_f32_16x16x32_bf16 v[216:219], a[24:27], a[84:87], v[216:219]\n"
    "s_add_u32 s80, s80, 8192\n"
    "v_mfma_f32_16x16x32_bf16 v[248:251], a[24:27], a[88:91], v[248:251]\n"
    "v_mfma_f32_16x16x32_bf16 v[60:63], a[28:31], a[64:67], v[60:63]\n"
    "v_mfma_f32_16x16x32_bf16 v[92:95], a[28:31], a[68:71], v[92:95]\n"
    "v_mfma_f32_16x16x32_bf16 v[124:127], a[28:31], a[72:75], v[124:127]\n"
    "buffer_load_dwordx4 v3, s[44:47], s80 offen offset:2048 lds\n"
    "v_mfma_f32_16x16x32_bf16 v[156:159], a[28:31], a[76:79], v[156:159]\n"
    "s_add_u32 s80, s80, 8192\n"
    "v_mfma_f32_16x16x32_bf16 v[188:191], a[28:31], a[80:83], v[188:191]\n"
    "v_mfma_f32_16x16x32_bf16 v[220:223], a[28:31], a[84:87], v[220:223]\n"
    "v_mfma_f32_16x16x32_bf16 v[252:255], a[28:31], a[88:91], v[252:255]\n"
    "buffer_load_dwordx4 v4, s[44:47], s80 offen offset:3072 lds\n"
    "v_mfma_f32_16x16x32_bf16 v[32:35], a[32:35], a[92:95], v[32:35]\n"
    "s_waitcnt vmcnt(12)\n"
    "ds_read_b64_tr_b16 a[0:1], v12 offset:16384\n"
    "v_mfma_f32_16x16x32_bf16 v[64:67], a[32:35], a[96:99], v[64:67]\n"
    "s_add_u32 s77, s77, 128\n"
    "ds_read_b64_tr_b16 a[2:3], v12 offset:17408\n"
    "v_mfma_f32_16x16x32_bf16 v[96:99], a[32:35], a[100:103], v[96:99]\n"
    "s_barrier\n"
    "ds_read_b64_tr_b16 a[4:5], v12 offset:16392\n"
    "v_mfma_f32_16x16x32_bf16 v[128:131], a[32:35], a[104:107], v[128:131]\n"
    "s_cmp_ge_u32 s77, 1152\n"
    "s_cselect_b32 s74, 1024, 0\n"
    "s_sub_u32 s77, s77, s74\n"
    "s_add_u32 s71, s63, 32768\n"
    "s_and_b32 s71, s71, 131071\n"
    "v_mfma_f32_16x16x32_bf16 v[160:163], a[32:35], a[108:111], v[160:163]\n"
    "buffer_load_dwordx4 a[168:171], v5, s[48:51], s71 offen offset:0\n"
    "v_mfma_f32_16x16x32_bf16 v[192:195], a[32:35], a[112:115], v[192:195]\n"
    "ds_read_b64_tr_b16 a[6:7], v12 offset:17416\n"
    "v_mfma_f32_16x16x32_bf16 v[224:227], a[32:35], a[116:119], v[224:227]\n"
    "ds_read_b64_tr_b16 a[8:9], v13 offset:16384\n"
    "v_mfma_f32_16x16x32_bf16 v[36:39], a[36:39], a[92:95], v[36:39]\n"
    "ds_read_b64_tr_b16 a[10:11], v13 offset:17408\n"
    "v_mfma_f32_16x16x32_bf16 v[68:71], a[36:39], a[96:99], v[68:71]\n"
    "ds_read_b64_tr_b16 a[12:13], v13 offset:16392\n"
    "v_mfma_f32_16x16x32_bf16 v[100:103], a[36:39], a[100:103], v[100:103]\n"
    "buffer_load_dwordx4 a[172:175], v6, s[48:51], s71 offen offset:1024\n"
    "v_mfma_f32_16x16x32_bf16 v[132:135], a[36:39], a[104:107], v[132:135]\n"
    "ds_read_b64_tr_b16 a[14:15], v13 offset:17416\n"
    "v_mfma_f32_16x16x32_bf16 v[164:167], a[36:39], a[108:111], v[164:167]\n"
    "ds_read_b64_tr_b16 a[16:17], v14 offset:16384\n"
    "v_mfma_f32_16x16x32_bf16 v[196:199], a[36:39], a[112:115], v[196:199]\n"
    "ds_read_b64_tr_b16 a[18:19], v14 offset:17408\n"
    "v_mfma_f32_16x16x32_bf16 v[228:231], a[36:39], a[116:119], v[228:231]\n"
    "ds_read_b64_tr_b16 a[20:21], v14 offset:16392\n"
    "v_mfma_f32_16x16x32_bf16 v[40:43], a[40:43], a[92:95], v[40:43]\n"
    "buffer_load_dwordx4 a[176:179], v7, s[48:51], s71 offen offset:2048\n"
    "v_mfma_f32_16x16x32_bf16 v[72:75], a[40:43], a[96:99], v[72:75]\n"
    "ds_read_b64_tr_b16 a[22:23], v14 offset:17416\n"
    "v_mfma_f32_16x16x32_bf16 v[104:107], a[40:43], a[100:103], v[104:107]\n"
    "ds_read_b64_tr_b16 a[24:25], v15 offset:16384\n"
    "v_mfma_f32_16x16x32_bf16 v[136:139], a[40:43], a[104:107], v[136:139]\n"
    "ds_read_b64_tr_b16 a[26:27], v15 offset:17408\n"
    "v_mfma_f32_16x16x32_bf16 v[168:171], a[40:43], a[108:111], v[168:171]\n"
    "ds_read_b64_tr_b16 a[28:29], v15 offset:16392\n"
    "v_mfma_f32_16x16x32_bf16 v[200:203], a[40:43], a[112:115], v[200:203]\n"
    "buffer_load_dwordx4 a[180:183], v8, s[48:51], s71 offen offset:3072\n"
    "v_mfma_f32_16x16x32_bf16 v[232:235], a[40:43], a[116:119], v[232:235]\n"
    "s_add_u32 m0, s60, 8192\n"
    "ds_read_b64_tr_b16 a[30:31], v15 offset:17416\n"
    "v_mfma_f32_16x16x32_bf16 v[44:47], a[44:47], a[92:95], v[44:47]\n"
    "ds_read_b128 a[64:67], v10 offset:0\n"
    "v_mfma_f32_16x16x32_bf16 v[76:79], a[44:47], a[96:99], v[76:79]\n"
    "ds_read_b128 a[68:71], v10 offset:2048\n"
    "v_mfma_f32_16x16x32_bf16 v[108:111], a[44:47], a[100:103], v[108:111]\n"
    "s_waitcnt lgkmcnt(2)\n"
    "ds_read_b128 a[72:75], v10 offset:4096\n"
    "v_mfma_f32_16x16x32_bf16 v[140:143], a[44:47], a[104:107], v[140:143]\n"
    "buffer_load_dwordx4 v1, s[40:43], s70 offen offset:0 lds\n"
    "v_mfma_f32_16x16x32_bf16 v[172:175], a[44:47], a[108:111], v[172:175]\n"
    "s_add_u32 s70, s70, 31744\n"
    "ds_read_b128 a[76:79], v10 offset:6144\n"
    "v_mfma_f32_16x16x32_bf16 v[204:207], a[44:47], a[112:115], v[204:207]\n"
    "ds_read_b128 a[80:83], v10 offset:40960\n"
    "v_mfma_f32_16x16x32_bf16 v[236:239], a[44:47], a[116:119], v[236:239]\n"
    "ds_read_b128 a[84:87], v10 offset:43008\n"
    "v_mfma_f32_16x16x32_bf16 v[48:51], a[48:51], a[92:95], v[48:51]\n"
    "ds_read_b128 a[88:91], v10 offset:45056\n"
    "v_mfma_f32_16x16x32_bf16 v[80:83], a[48:51], a[96:99], v[80:83]\n"
    "buffer_load_dwordx4 v1, s[40:43], s70 offen offset:1024 lds\n"
    "v_mfma_f32_16x16x32_bf16 v[112:115], a[48:51], a[100:103], v[112:115]\n"
    "s_add_u32 s70, s70, 31744\n"
    "v_mfma_f32_16x16x32_bf16 v[144:147], a[48:51], a[104:107], v[144:147]\n"
    "v_mfma_f32_16x16x32_bf16 v[176:179], a[48:51], a[108:111], v[176:179]\n"
    "v_mfma_f32_16x16x32_bf16 v[208:211], a[48:51], a[112:115], v[208:211]\n"
    "buffer_load_dwordx4 v2, s[40:43], s70 offen offset:2048 lds\n"
    "v_mfma_f32_16x16x32_bf16 v[240:243], a[48:51], a[116:119], v[240:243]\n"
    "s_waitcnt lgkmcnt(0)\n"
    "v_mfma_f32_16x16x32_bf16 v[52:55], a[52:55], a[92:95], v[52:55]\n"
    "s_add_u32 s70, s70, 31744\n"
    "v_mfma_f32_16x16x32_bf16 v[84:87], a[52:55], a[96:99], v[84:87]\n"
    "v_mfma_f32_16x16x32_bf16 v[116:119], a[52:55], a[100:103], v[116:119]\n"
    "v_mfma_f32_16x16x32_bf16 v[148:151], a[52:55], a[104:107], v[148:151]\n"
    "buffer_load_dwordx4 v2, s[40:43], s70 offen offset:3072 lds\n"
    "v_mfma_f32_16x16x32_bf16 v[180:183], a[52:55], a[108:111], v[180:183]\n"
    "s_add_u32 s70, s70, 35840\n"
    "v_mfma_f32_16x16x32_bf16 v[212:215], a[52:55], a[112:115], v[212:215]\n"
    "s_add_u32 m0, s60, 12288\n"
    "v_mfma_f32_16x16x32_bf16 v[244:247], a[52:55], a[116:119], v[244:247]\n"
    "v_mfma_f32_16x16x32_bf16 v[56:59], a[56:59], a[92:95], v[56:59]\n"
    "v_mfma_f32_16x16x32_bf16 v[88:91], a[56:59], a[96:99], v[88:91]\n"
    "buffer_load_dwordx4 v1, s[40:43], s70 offen offset:0 lds\n"
    "v_mfma_f32_16x16x32_bf16 v[120:123], a[56:59], a[100:103], v[120:123]\n"
    "s_add_u32 s70, s70, 31744\n"
    "v_mfma_f32_16x16x32_bf16 v[152:155], a[56:59], a[104:107], v[152:155]\n"
    "v_mfma_f32_16x16x32_bf16 v[184:187], a[56:59], a[108:111], v[184:187]\n"
    "v_mfma_f32_16x16x32_bf16 v[216:219], a[56:59], a[112:115], v[216:219]\n"
    "v_mfma_f32_16x16x32_bf16 v[248:251], a[56:59], a[116:119], v[248:251]\n"
    "buffer_load_dwordx4 v1, s[40:43], s70 offen offset:1024 lds\n"
    "v_mfma_f32_16x16x32_bf16 v[60:63], a[60:63], a[92:95], v[60:63]\n"
    "s_add_u32 s70, s70, 31744\n"
    "v_mfma_f32_16x16x32_bf16 v[92:95], a[60:63], a[96:99], v[92:95]\n"
    "v_mfma_f32_16x16x32_bf16 v[124:127], a[60:63], a[100:103], v[124:127]\n"
    "v_mfma_f32_16x16x32_bf16 v[156:159], a[60:63], a[104:107], v[156:159]\n"
    "v_mfma_f32_16x16x32_bf16 v[188:191], a[60:63], a[108:111], v[188:191]\n"
    "buffer_load_dwordx4 v2, s[40:43], s70 offen offset:2048 lds\n"
    "v_mfma_f32_16x16x32_bf16 v[220:223], a[60:63], a[112:115], v[220:223]\n"
    "s_add_u32 s70, s70, 31744\n"
    "v_mfma_f32_16x16x32_bf16 v[252:255], a[60:63], a[116:119], v[252:255]\n"
    "v_mfma_f32_16x16x32_bf16 v[32:35], a[0:3], a[64:67], v[32:35]\n"
    "s_waitcnt vmcnt(15)\n"
    "ds_read_b64_tr_b16 a[32:33], v12 offset:24576\n"
    "v_mfma_f32_16x16x32_bf16 v[64:67], a[0:3], a[68:71], v[64:67]\n"
    "ds_read_b64_tr_b16 a[34:35], v12 offset:25600\n"
    "v_mfma_f32_16x16x32_bf16 v[96:99], a[0:3], a[72:75], v[96:99]\n"
    "buffer_load_dwordx4 v2, s[40:43], s70 offen offset:3072 lds\n"
    "v_mfma_f32_16x16x32_bf16 v[128:131], a[0:3], a[76:79], v[128:131]\n"
    "s_add_u32 s70, s70, 35840\n"
    "ds_read_b64_tr_b16 a[36:37], v12 offset:24584\n"
    "v_mfma_f32_16x16x32_bf16 v[160:163], a[0:3], a[80:83], v[160:163]\n"
    "s_add_u32 m0, s60, 0\n"
    "ds_read_b64_tr_b16 a[38:39], v12 offset:25608\n"
    "v_mfma_f32_16x16x32_bf16 v[192:195], a[0:3], a[84:87], v[192:195]\n"
    "ds_read_b64_tr_b16 a[40:41], v13 offset:24576\n"
    "v_mfma_f32_16x16x32_bf16 v[224:227], a[0:3], a[88:91], v[224:227]\n"
    "ds_read_b64_tr_b16 a[42:43], v13 offset:25600\n"
    "v_mfma_f32_16x16x32_bf16 v[36:39], a[4:7], a[64:67], v[36:39]\n"
    "buffer_load_dwordx4 v1, s[40:43], s70 offen offset:0 lds\n"
    "v_mfma_f32_16x16x32_bf16 v[68:71], a[4:7], a[68:71], v[68:71]\n"
    "s_add_u32 s70, s70, 31744\n"
    "ds_read_b64_tr_b16 a[44:45], v13 offset:24584\n"
    "v_mfma_f32_16x16x32_bf16 v[100:103], a[4:7], a[72:75], v[100:103]\n"
    "ds_read_b64_tr_b16 a[46:47], v13 offset:25608\n"
    "v_mfma_f32_16x16x32_bf16 v[132:135], a[4:7], a[76:79], v[132:135]\n"
    "ds_read_b64_tr_b16 a[48:49], v14 offset:24576\n"
    "v_mfma_f32_16x16x32_bf16 v[164:167], a[4:7], a[80:83], v[164:167]\n"
    "buffer_load_dwordx4 v1, s[40:43], s70 offen offset:1024 lds\n"
    "v_mfma_f32_16x16x32_bf16 v[196:199], a[4:7], a[84:87], v[196:199]\n"
    "s_add_u32 s70, s70, 31744\n"
    "ds_read_b64_tr_b16 a[50:51], v14 offset:25600\n"
    "v_mfma_f32_16x16x32_bf16 v[228:231], a[4:7], a[88:91], v[228:231]\n"
    "ds_read_b64_tr_b16 a[52:53], v14 offset:24584\n"
    "v_mfma_f32_16x16x32_bf16 v[40:43], a[8:11], a[64:67], v[40:43]\n"
    "ds_read_b64_tr_b16 a[54:55], v14 offset:25608\n"
    "v_mfma_f32_16x16x32_bf16 v[72:75], a[8:11], a[68:71], v[72:75]\n"
    "ds_read_b64_tr_b16 a[56:57], v15 offset:24576\n"
    "v_mfma_f32_16x16x32_bf16 v[104:107], a[8:11], a[72:75], v[104:107]\n"
    "buffer_load_dwordx4 v2, s[40:43], s70 offen offset:2048 lds\n"
    "v_mfma_f32_16x16x32_bf16 v[136:139], a[8:11], a[76:79], v[136:139]\n"
    "s_add_u32 s70, s70, 31744\n"
    "ds_read_b64_tr_b16 a[58:59], v15 offset:25600\n"
    "v_mfma_f32_16x16x32_bf16 v[168:171], a[8:11], a[80:83], v[168:171]\n"
    "ds_read_b64_tr_b16 a[60:61], v15 offset:24584\n"
    "v_mfma_f32_16x16x32_bf16 v[200:203], a[8:11], a[84:87], v[200:203]\n"
    "ds_read_b64_tr_b16 a[62:63], v15 offset:25608\n"
    "v_mfma_f32_16x16x32_bf16 v[232:235], a[8:11], a[88:91], v[232:235]\n"
    "ds_read_b128 a[92:95], v11 offset:0\n"
    "v_mfma_f32_16x16x32_bf16 v[44:47], a[12:15], a[64:67], v[44:47]\n"
    "buffer_load_dwordx4 v2, s[40:43], s70 offen offset:3072 lds\n"
    "v_mfma_f32_16x16x32_bf16 v[76:79], a[12:15], a[68:71], v[76:79]\n"
    "s_add_u32 s70, s70, 35840\n"
    "ds_read_b128 a[96:99], v11 offset:2048\n"
    "v_mfma_f32_16x16x32_bf16 v[108:111], a[12:15], a[72:75], v[108:111]\n"
    "s_waitcnt lgkmcnt(2)\n"
    "ds_read_b128 a[100:103], v11 offset:4096\n"
    "v_mfma_f32_16x16x32_bf16 v[140:143], a[12:15], a[76:79], v[140:143]\n"
    "s_add_u32 m0, s60, 4096\n"
    "ds_read_b128 a[104:107], v11 offset:6144\n"
    "v_mfma_f32_16x16x32_bf16 v[172:175], a[12:15], a[80:83], v[172:175]\n"
    "ds_read_b128 a[108:111], v11 offset:40960\n"
    "v_mfma_f32_16x16x32_bf16 v[204:207], a[12:15], a[84:87], v[204:207]\n"
    "buffer_load_dwordx4 v1, s[40:43], s70 offen offset:0 lds\n"
    "v_mfma_f32_16x16x32_bf16 v[236:239], a[12:15], a[88:91], v[236:239]\n"
    "s_add_u32 s70, s70, 31744\n"
    "ds_read_b128 a[112:115], v11 offset:43008\n"
    "v_mfma_f32_16x16x32_bf16 v[48:51], a[16:19], a[64:67], v[48:51]\n"
    "ds_read_b128 a[116:119], v11 offset:45056\n"
    "v_mfma_f32_16x16x32_bf16 v[80:83], a[16:19], a[68:71], v[80:83]\n"
    "v_mfma_f32_16x16x32_bf16 v[112:115], a[16:19], a[72:75], v[112:115]\n"
    "v_mfma_f32_16x16x32_bf16 v[144:147], a[16:19], a[76:79], v[144:147]\n"
    "buffer_load_dwordx4 v1, s[40:43], s70 offen offset:1024 lds\n"
    "v_mfma_f32_16x16x32_bf16 v[176:179], a[16:19], a[80:83], v[176:179]\n"
    "s_add_u32 s70, s70, 31744\n"
    "v_mfma_f32_16x16x32_bf16 v[208:211], a[16:19], a[84:87], v[208:211]\n"
    "v_mfma_f32_16x16x32_bf16 v[240:243], a[16:19], a[88:91], v[240:243]\n"
    "s_waitcnt lgkmcnt(0)\n"
    "v_mfma_f32_16x16x32_bf16 v[52:55], a[20:23], a[64:67], v[52:55]\n"
    "s_barrier\n"
    "v_mfma_f32_16x16x32_bf16 v[84:87], a[20:23], a[68:71], v[84:87]\n"
    "buffer_load_dwordx4 v2, s[40:43], s70 offen offset:2048 lds\n"
    "v_mfma_f32_16x16x32_bf16 v[116:119], a[20:23], a[72:75], v[116:119]\n"
    "s_add_u32 s70, s70, 31744\n"
    "v_mfma_f32_16x16x32_bf16 v[148:151], a[20:23], a[76:79], v[148:151]\n"
    "v_mfma_f32_16x16x32_bf16 v[180:183], a[20:23], a[80:83], v[180:183]\n"
    "v_mfma_f32_16x16x32_bf16 v[212:215], a[20:23], a[84:87], v[212:215]\n"
    "buffer_load_dwordx4 v2, s[40:43], s70 offen offset:3072 lds\n"
    "v_mfma_f32_16x16x32_bf16 v[244:247], a[20:23], a[88:91], v[244:247]\n"
    "s_add_u32 s70, s70, 35840\n"
    "v_add_u32 v10, 81920, v10\n"
    "v_add_u32 v11, 81920, v11\n"
    "v_mfma_f32_16x16x32_bf16 v[56:59], a[24:27], a[64:67], v[56:59]\n"
    "s_cmp_ge_u32 s70, 4718592\n"
    "v_mfma_f32_16x16x32_bf16 v[88:91], a[24:27], a[68:71], v[88:91]\n"
    "s_cselect_b32 s74, 4194304, 0\n"
    "v_mfma_f32_16x16x32_bf16 v[120:123], a[24:27], a[72:75], v[120:123]\n"
    "s_sub_u32 s70, s70, s74\n"
    "s_add_u32 s80, s62, s77\n"
    "s_add_u32 m0, s61, 0\n"
    "v_mfma_f32_16x16x32_bf16 v[152:155], a[24:27], a[76:79], v[152:155]\n"
    "buffer_load_dwordx4 v3, s[44:47], s80 offen offset:0 lds\n"
    "v_mfma_f32_16x16x32_bf16 v[184:187], a[24:27], a[80:83], v[184:187]\n"
    "s_add_u32 s80, s80, 8192\n"
    "v_mfma_f32_16x16x32_bf16 v[216:219], a[24:27], a[84:87], v[216:219]\n"
    "v_mfma_f32_16x16x32_bf16 v[248:251], a[24:27], a[88:91], v[248:251]\n"
    "v_mfma_f32_16x16x32_bf16 v[60:63], a[28:31], a[64:67], v[60:63]\n"
    "v_mfma_f32_16x16x32_bf16 v[92:95], a[28:31], a[68:71], v[92:95]\n"
    "buffer_load_dwordx4 v4, s[44:47], s80 offen offset:1024 lds\n"
    "v_mfma_f32_16x16x32_bf16 v[124:127], a[28:31], a[72:75], v[124:127]\n"
    "s_add_u32 s80, s80, 8192\n"
    "v_mfma_f32_16x16x32_bf16 v[156:159], a[28:31], a[76:79], v[156:159]\n"
    "v_mfma_f32_16x16x32_bf16 v[188:191], a[28:31], a[80:83], v[188:191]\n"
    "v_mfma_f32_16x16x32_bf16 v[220:223], a[28:31], a[84:87], v[220:223]\n"
    "v_mfma_f32_16x16x32_bf16 v[252:255], a[28:31], a[88:91], v[252:255]\n"
    "buffer_load_dwordx4 v3, s[44:47], s80 offen offset:2048 lds\n"
    "v_mfma_f32_16x16x32_bf16 v[32:35], a[32:35], a[92:95], v[32:35]\n"
    "s_waitcnt vmcnt(11)\n"
    "ds_read_b64_tr_b16 a[0:1], v12 offset:8192\n"
    "v_mfma_f32_16x16x32_bf16 v[64:67], a[32:35], a[96:99], v[64:67]\n"
    "s_add_u32 s80, s80, 8192\n"
    "ds_read_b64_tr_b16 a[2:3], v12 offset:9216\n"
    "v_mfma_f32_16x16x32_bf16 v[96:99], a[32:35], a[100:103], v[96:99]\n"
    "s_barrier\n"
    "ds_read_b64_tr_b16 a[4:5], v12 offset:8200\n"
    "v_mfma_f32_16x16x32_bf16 v[128:131], a[32:35], a[104:107], v[128:131]\n"
    "s_add_u32 s71, s63, 36864\n"
    "s_and_b32 s71, s71, 131071\n"
    "ds_read_b64_tr_b16 a[6:7], v12 offset:9224\n"
    "v_mfma_f32_16x16x32_bf16 v[160:163], a[32:35], a[108:111], v[160:163]\n"
    "buffer_load_dwordx4 a[184:187], v5, s[48:51], s71 offen offset:0\n"
    "v_mfma_f32_16x16x32_bf16 v[192:195], a[32:35], a[112:115], v[192:195]\n"
    "ds_read_b64_tr_b16 a[8:9], v13 offset:8192\n"
    "v_mfma_f32_16x16x32_bf16 v[224:227], a[32:35], a[116:119], v[224:227]\n"
    "ds_read_b64_tr_b16 a[10:11], v13 offset:9216\n"
    "v_mfma_f32_16x16x32_bf16 v[36:39], a[36:39], a[92:95], v[36:39]\n"
    "ds_read_b64_tr_b16 a[12:13], v13 offset:8200\n"
    "v_mfma_f32_16x16x32_bf16 v[68:71], a[36:39], a[96:99], v[68:71]\n"
    "ds_read_b64_tr_b16 a[14:15], v13 offset:9224\n"
    "v_mfma_f32_16x16x32_bf16 v[100:103], a[36:39], a[100:103], v[100:103]\n"
    "buffer_load_dwordx4 a[188:191], v6, s[48:51], s71 offen offset:1024\n"
    "v_mfma_f32_16x16x32_bf16 v[132:135], a[36:39], a[104:107], v[132:135]\n"
    "ds_read_b64_tr_b16 a[16:17], v14 offset:8192\n"
    "v_mfma_f32_16x16x32_bf16 v[164:167], a[36:39], a[108:111], v[164:167]\n"
    "ds_read_b64_tr_b16 a[18:19], v14 offset:9216\n"
    "v_mfma_f32_16x16x32_bf16 v[196:199], a[36:39], a[112:115], v[196:199]\n"
    "ds_read_b64_tr_b16 a[20:21], v14 offset:8200\n"
    "v_mfma_f32_16x16x32_bf16 v[228:231], a[36:39], a[116:119], v[228:231]\n"
    "ds_read_b64_tr_b16 a[22:23], v14 offset:9224\n"
    "v_mfma_f32_16x16x32_bf16 v[40:43], a[40:43], a[92:95], v[40:43]\n"
    "buffer_load_dwordx4 a[192:195], v7, s[48:51], s71 offen offset:2048\n"
    "v_mfma_f32_16x16x32_bf16 v[72:75], a[40:43], a[96:99], v[72:75]\n"
    "ds_read_b64_tr_b16 a[24:25], v15 offset:8192\n"
    "v_mfma_f32_16x16x32_bf16 v[104:107], a[40:43], a[100:103], v[104:107]\n"
    "ds_read_b64_tr_b16 a[26:27], v15 offset:9216\n"
    "v_mfma_f32_16x16x32_bf16 v[136:139], a[40:43], a[104:107], v[136:139]\n"
    "ds_read_b64_tr_b16 a[28:29], v15 offset:8200\n"
    "v_mfma_f32_16x16x32_bf16 v[168:171], a[40:43], a[108:111], v[168:171]\n"
    "buffer_load_dwordx4 a[196:199], v8, s[48:51], s71 offen offset:3072\n"
    "v_mfma_f32_16x16x32_bf16 v[200:203], a[40:43], a[112:115], v[200:203]\n"
    "ds_read_b64_tr_b16 a[30:31], v15 offset:9224\n"
    "v_mfma_f32_16x16x32_bf16 v[232:235], a[40:43], a[116:119], v[232:235]\n"
    "s_waitcnt vmcnt(63)\n"
    "ds_read_b128 a[64:67], v10 offset:0\n"
    "v_mfma_f32_16x16x32_bf16 v[44:47], a[44:47], a[92:95], v[44:47]\n"
    "ds_read_b128 a[68:71], v10 offset:2048\n"
    "v_mfma_f32_16x16x32_bf16 v[76:79], a[44:47], a[96:99], v[76:79]\n"
    "ds_read_b128 a[72:75], v10 offset:4096\n"
    "v_mfma_f32_16x16x32_bf16 v[108:111], a[44:47], a[100:103], v[108:111]\n"
    "s_waitcnt lgkmcnt(3)\n"
    "buffer_load_dwordx4 v4, s[44:47], s80 offen offset:3072 lds\n"
    "v_mfma_f32_16x16x32_bf16 v[140:143], a[44:47], a[104:107], v[140:143]\n"
    "s_add_u32 s77, s77, 128\n"
    "ds_read_b128 a[76:79], v10 offset:6144\n"
    "v_mfma_f32_16x16x32_bf16 v[172:175], a[44:47], a[108:111], v[172:175]\n"
    "s_add_u32 m0, s60, 16384\n"
    "ds_read_b128 a[80:83], v10 offset:40960\n"
    "v_mfma_f32_16x16x32_bf16 v[204:207], a[44:47], a[112:115], v[204:207]\n"
    "ds_read_b128 a[84:87], v10 offset:43008\n"
    "v_mfma_f32_16x16x32_bf16 v[236:239], a[44:47], a[116:119], v[236:239]\n"
    "ds_read_b128 a[88:91], v10 offset:45056\n"
    "v_mfma_f32_16x16x32_bf16 v[48:51], a[48:51], a[92:95], v[48:51]\n"
    "buffer_load_dwordx4 v1, s[40:43], s70 offen offset:0 lds\n"
    "v_mfma_f32_16x16x32_bf16 v[80:83], a[48:51], a[96:99], v[80:83]\n"
    "s_add_u32 s70, s70, 31744\n"
    "ds_write_b128 v9, a[224:227] offset:8192\n"
    "v_mfma_f32_16x16x32_bf16 v[112:115], a[48:51], a[100:103], v[112:115]\n"
    "v_mfma_f32_16x16x32_bf16 v[144:147], a[48:51], a[104:107], v[144:147]\n"
    "ds_write_b128 v9, a[228:231] offset:9216\n"
    "v_mfma_f32_16x16x32_bf16 v[176:179], a[48:51], a[108:111], v[176:179]\n"
    "v_mfma_f32_16x16x32_bf16 v[208:211], a[48:51], a[112:115], v[208:211]\n"
    "buffer_load_dwordx4 v1, s[40:43], s70 offen offset:1024 lds\n"
    "v_mfma_f32_16x16x32_bf16 v[240:243], a[48:51], a[116:119], v[240:243]\n"
    "s_waitcnt lgkmcnt(0)\n"
    "ds_write_b128 v9, a[232:235] offset:10240\n"
    "v_mfma_f32_16x16x32_bf16 v[52:55], a[52:55], a[92:95], v[52:55]\n"
    "s_add_u32 s70, s70, 31744\n"
    "v_mfma_f32_16x16x32_bf16 v[84:87], a[52:55], a[96:99], v[84:87]\n"
    "ds_write_b128 v9, a[236:239] offset:11264\n"
    "v_mfma_f32_16x16x32_bf16 v[116:119], a[52:55], a[100:103], v[116:119]\n"
    "v_mfma_f32_16x16x32_bf16 v[148:151], a[52:55], a[104:107], v[148:151]\n"
    "buffer_load_dwordx4 v2, s[40:43], s70 offen offset:2048 lds\n"
    "v_mfma_f32_16x16x32_bf16 v[180:183], a[52:55], a[108:111], v[180:183]\n"
    "s_add_u32 s70, s70, 31744\n"
    "ds_write_b128 v9, a[240:243] offset:12288\n"
    "v_mfma_f32_16x16x32_bf16 v[212:215], a[52:55], a[112:115], v[212:215]\n"
    "v_mfma_f32_16x16x32_bf16 v[244:247], a[52:55], a[116:119], v[244:247]\n"
    "ds_write_b128 v9, a[244:247] offset:13312\n"
    "v_mfma_f32_16x16x32_bf16 v[56:59], a[56:59], a[92:95], v[56:59]\n"
    "v_mfma_f32_16x16x32_bf16 v[88:91], a[56:59], a[96:99], v[88:91]\n"
    "buffer_load_dwordx4 v2, s[40:43], s70 offen offset:3072 lds\n"
    "v_mfma_f32_16x16x32_bf16 v[120:123], a[56:59], a[100:103], v[120:123]\n"
    "s_add_u32 s70, s70, 35840\n"
    "ds_write_b128 v9, a[248:251] offset:14336\n"
    "v_mfma_f32_16x16x32_bf16 v[152:155], a[56:59], a[104:107], v[152:155]\n"
    "s_add_u32 m0, s60, 20480\n"
    "v_mfma_f32_16x16x32_bf16 v[184:187], a[56:59], a[108:111], v[184:187]\n"
    "ds_write_b128 v9, a[252:255] offset:15360\n"
    "v_mfma_f32_16x16x32_bf16 v[216:219], a[56:59], a[112:115], v[216:219]\n"
    "v_mfma_f32_16x16x32_bf16 v[248:251], a[56:59], a[116:119], v[248:251]\n"
    "buffer_load_dwordx4 v1, s[40:43], s70 offen offset:0 lds\n"
    "v_mfma_f32_16x16x32_bf16 v[60:63], a[60:63], a[92:95], v[60:63]\n"
    "s_add_u32 s70, s70, 31744\n"
    "v_mfma_f32_16x16x32_bf16 v[92:95], a[60:63], a[96:99], v[92:95]\n"
    "v_mfma_f32_16x16x32_bf16 v[124:127], a[60:63], a[100:103], v[124:127]\n"
    "v_mfma_f32_16x16x32_bf16 v[156:159], a[60:63], a[104:107], v[156:159]\n"
    "buffer_load_dwordx4 v1, s[40:43], s70 offen offset:1024 lds\n"
    "v_mfma_f32_16x16x32_bf16 v[188:191], a[60:63], a[108:111], v[188:191]\n"
    "s_add_u32 s70, s70, 31744\n"
    "v_mfma_f32_16x16x32_bf16 v[220:223], a[60:63], a[112:115], v[220:223]\n"
    "v_mfma_f32_16x16x32_bf16 v[252:255], a[60:63], a[116:119], v[252:255]\n"
    "v_mfma_f32_16x16x32_bf16 v[32:35], a[0:3], a[64:67], v[32:35]\n"
    "s_waitcnt vmcnt(14)\n"
    "ds_read_b64_tr_b16 a[32:33], v12 offset:0\n"
    "v_mfma_f32_16x16x32_bf16 v[64:67], a[0:3], a[68:71], v[64:67]\n"
    "buffer_load_dwordx4 v2, s[40:43], s70 offen offset:2048 lds\n"
    "v_mfma_f32_16x16x32_bf16 v[96:99], a[0:3], a[72:75], v[96:99]\n"
    "s_add_u32 s70, s70, 31744\n"
    "ds_read_b64_tr_b16 a[34:35], v12 offset:1024\n"
    "v_mfma_f32_16x16x32_bf16 v[128:131], a[0:3], a[76:79], v[128:131]\n"
    "ds_read_b64_tr_b16 a[36:37], v12 offset:8\n"
    "v_mfma_f32_16x16x32_bf16 v[160:163], a[0:3], a[80:83], v[160:163]\n"
    "ds_read_b64_tr_b16 a[38:39], v12 offset:1032\n"
    "v_mfma_f32_16x16x32_bf16 v[192:195], a[0:3], a[84:87], v[192:195]\n"
    "ds_read_b64_tr_b16 a[40:41], v13 offset:0\n"
    "v_mfma_f32_16x16x32_bf16 v[224:227], a[0:3], a[88:91], v[224:227]\n"
    "buffer_load_dwordx4 v2, s[40:43], s70 offen offset:3072 lds\n"
    "v_mfma_f32_16x16x32_bf16 v[36:39], a[4:7], a[64:67], v[36:39]\n"
    "s_add_u32 s70, s70, 35840\n"
    "ds_read_b64_tr_b16 a[42:43], v13 offset:1024\n"
    "v_mfma_f32_16x16x32_bf16 v[68:71], a[4:7], a[68:71], v[68:71]\n"
    "s_add_u32 m0, s60, 24576\n"
    "ds_read_b64_tr_b16 a[44:45], v13 offset:8\n"
    "v_mfma_f32_16x16x32_bf16 v[100:103], a[4:7], a[72:75], v[100:103]\n"
    "ds_read_b64_tr_b16 a[46:47], v13 offset:1032\n"
    "v_mfma_f32_16x16x32_bf16 v[132:135], a[4:7], a[76:79], v[132:135]\n"
    "ds_read_b64_tr_b16 a[48:49], v14 offset:0\n"
    "v_mfma_f32_16x16x32_bf16 v[164:167], a[4:7], a[80:83], v[164:167]\n"
    "buffer_load_dwordx4 v1, s[40:43], s70 offen offset:0 lds\n"
    "v_mfma_f32_16x16x32_bf16 v[196:199], a[4:7], a[84:87], v[196:199]\n"
    "s_add_u32 s70, s70, 31744\n"
    "ds_read_b64_tr_b16 a[50:51], v14 offset:1024\n"
    "v_mfma_f32_16x16x32_bf16 v[228:231], a[4:7], a[88:91], v[228:231]\n"
    "ds_read_b64_tr_b16 a[52:53], v14 offset:8\n"
    "v_mfma_f32_16x16x32_bf16 v[40:43], a[8:11], a[64:67], v[40:43]\n"
    "ds_read_b64_tr_b16 a[54:55], v14 offset:1032\n"
    "v_mfma_f32_16x16x32_bf16 v[72:75], a[8:11], a[68:71], v[72:75]\n"
    "ds_read_b64_tr_b16 a[56:57], v15 offset:0\n"
    "v_mfma_f32_16x16x32_bf16 v[104:107], a[8:11], a[72:75], v[104:107]\n"
    "buffer_load_dwordx4 v1, s[40:43], s70 offen offset:1024 lds\n"
    "v_mfma_f32_16x16x32_bf16 v[136:139], a[8:11], a[76:79], v[136:139]\n"
    "s_add_u32 s70, s70, 31744\n"
    "ds_read_b64_tr_b16 a[58:59], v15 offset:1024\n"
    "v_mfma_f32_16x16x32_bf16 v[168:171], a[8:11], a[80:83], v[168:171]\n"
    "ds_read_b64_tr_b16 a[60:61], v15 offset:8\n"
    "v_mfma_f32_16x16x32_bf16 v[200:203], a[8:11], a[84:87], v[200:203]\n"
    "ds_read_b64_tr_b16 a[62:63], v15 offset:1032\n"
    "v_mfma_f32_16x16x32_bf16 v[232:235], a[8:11], a[88:91], v[232:235]\n"
    "s_waitcnt vmcnt(58)\n"
    "ds_read_b128 a[92:95], v11 offset:0\n"
    "v_mfma_f32_16x16x32_bf16 v[44:47], a[12:15], a[64:67], v[44:47]\n"
    "buffer_load_dwordx4 v2, s[40:43], s70 offen offset:2048 lds\n"
    "v_mfma_f32_16x16x32_bf16 v[76:79], a[12:15], a[68:71], v[76:79]\n"
    "s_add_u32 s70, s70, 31744\n"
    "ds_read_b128 a[96:99], v11 offset:2048\n"
    "v_mfma_f32_16x16x32_bf16 v[108:111], a[12:15], a[72:75], v[108:111]\n"
    "s_waitcnt lgkmcnt(2)\n"
    "ds_read_b128 a[100:103], v11 offset:4096\n"
    "v_mfma_f32_16x16x32_bf16 v[140:143], a[12:15], a[76:79], v[140:143]\n"
    "ds_read_b128 a[104:107], v11 offset:6144\n"
    "v_mfma_f32_16x16x32_bf16 v[172:175], a[12:15], a[80:83], v[172:175]\n"
    "buffer_load_dwordx4 v2, s[40:43], s70 offen offset:3072 lds\n"
    "v_mfma_f32_16x16x32_bf16 v[204:207], a[12:15], a[84:87], v[204:207]\n"
    "s_add_u32 s70, s70, 35840\n"
    "ds_read_b128 a[108:111], v11 offset:40960\n"
    "v_mfma_f32_16x16x32_bf16 v[236:239], a[12:15], a[88:91], v[236:239]\n"
    "s_add_u32 m0, s60, 28672\n"
    "ds_read_b128 a[112:115], v11 offset:43008\n"
    "v_mfma_f32_16x16x32_bf16 v[48:51], a[16:19], a[64:67], v[48:51]\n"
    "ds_read_b128 a[116:119], v11 offset:45056\n"
    "v_mfma_f32_16x16x32_bf16 v[80:83], a[16:19], a[68:71], v[80:83]\n"
    "ds_write_b128 v9, a[136:139] offset:0\n"
    "v_mfma_f32_16x16x32_bf16 v[112:115], a[16:19], a[72:75], v[112:115]\n"
    "buffer_load_dwordx4 v1, s[40:43], s70 offen offset:0 lds\n"
    "v_mfma_f32_16x16x32_bf16 v[144:147], a[16:19], a[76:79], v[144:147]\n"
    "s_add_u32 s70, s70, 31744\n"
    "ds_write_b128 v9, a[140:143] offset:1024\n"
    "v_mfma_f32_16x16x32_bf16 v[176:179], a[16:19], a[80:83], v[176:179]\n"
    "v_mfma_f32_16x16x32_bf16 v[208:211], a[16:19], a[84:87], v[208:211]\n"
    "ds_write_b128 v9, a[144:147] offset:2048\n"
    "v_mfma_f32_16x16x32_bf16 v[240:243], a[16:19], a[88:91], v[240:243]\n"
    "s_waitcnt lgkmcnt(0)\n"
    "v_mfma_f32_16x16x32_bf16 v[52:55], a[20:23], a[64:67], v[52:55]\n"
    "s_barrier\n"
    "buffer_load_dwordx4 v1, s[40:43], s70 offen offset:1024 lds\n"
    "v_mfma_f32_16x16x32_bf16 v[84:87], a[20:23], a[68:71], v[84:87]\n"
    "s_add_u32 s70, s70, 31744\n"
    "ds_write_b128 v9, a[148:151] offset:3072\n"
    "v_mfma_f32_16x16x32_bf16 v[116:119], a[20:23], a[72:75], v[116:119]\n"
    "v_mfma_f32_16x16x32_bf16 v[148:151], a[20:23], a[76:79], v[148:151]\n"
    "ds_write_b128 v9, a[152:155] offset:4096\n"
    "v_mfma_f32_16x16x32_bf16 v[180:183], a[20:23], a[80:83], v[180:183]\n"
    "v_mfma_f32_16x16x32_bf16 v[212:215], a[20:23], a[84:87], v[212:215]\n"
    "buffer_load_dwordx4 v2, s[40:43], s70 offen offset:2048 lds\n"
    "v_mfma_f32_16x16x32_bf16 v[244:247], a[20:23], a[88:91], v[244:247]\n"
    "s_add_u32 s70, s70, 31744\n"
    "v_add_u32 v10, 4294885376, v10\n"
    "v_add_u32 v11, 4294885376, v11\n"
    "v_mfma_f32_16x16x32_bf16 v[56:59], a[24:27], a[64:67], v[56:59]\n"
    "ds_write_b128 v9, a[156:159] offset:5120\n"
    "v_mfma_f32_16x16x32_bf16 v[88:91], a[24:27], a[68:71], v[88:91]\n"
    "v_mfma_f32_16x16x32_bf16 v[120:123], a[24:27], a[72:75], v[120:123]\n"
    "ds_write_b128 v9, a[160:163] offset:6144\n"
    "v_mfma_f32_16x16x32_bf16 v[152:155], a[24:27], a[76:79], v[152:155]\n"
    "buffer_load_dwordx4 v2, s[40:43], s70 offen offset:3072 lds\n"
    "v_mfma_f32_16x16x32_bf16 v[184:187], a[24:27], a[80:83], v[184:187]\n"
    "s_add_u32 s70, s70, 35840\n"
    "ds_write_b128 v9, a[164:167] offset:7168\n"
    "v_mfma_f32_16x16x32_bf16 v[216:219], a[24:27], a[84:87], v[216:219]\n"
    "v_mfma_f32_16x16x32_bf16 v[248:251], a[24:27], a[88:91], v[248:251]\n"
    "v_mfma_f32_16x16x32_bf16 v[60:63], a[28:31], a[64:67], v[60:63]\n"
    "v_mfma_f32_16x16x32_bf16 v[92:95], a[28:31], a[68:71], v[92:95]\n"
    "buffer_load_dwordx2 a[202:203], v0, s[12:15], 0 offen offset:256 sc1\n"
    "v_mfma_f32_16x16x32_bf16 v[124:127], a[28:31], a[72:75], v[124:127]\n"
    "s_and_b32 s83, s82, 15\n"
    "v_lshlrev_b32 v3, 3, v0\n"
    "v_mfma_f32_16x16x32_bf16 v[156:159], a[28:31], a[76:79], v[156:159]\n"
    "s_lshl_b32 s83, s83, 10\n"
    "v_min_u32 v3, 896, v3\n"
    "v_mfma_f32_16x16x32_bf16 v[188:191], a[28:31], a[80:83], v[188:191]\n"
    "s_add_u32 s83, s83, LATT_%=-LBEG_%=-2048\n"
    "v_mfma_f32_16x16x32_bf16 v[220:223], a[28:31], a[84:87], v[220:223]\n"
    "s_waitcnt lgkmcnt(0)\n"
    "buffer_load_dword a201, v3, s[84:87], s83 offen offset:0\n"
    "v_mfma_f32_16x16x32_bf16 v[252:255], a[28:31], a[88:91], v[252:255]\n"
    "s_add_u32 m0, s60, s73\n"
    "v_mfma_f32_16x16x32_bf16 v[32:35], a[32:35], a[92:95], v[32:35]\n"
    "s_waitcnt vmcnt(10)\n"
    "ds_read_b64_tr_b16 a[0:1], v12 offset:16384\n"
    "v_mfma_f32_16x16x32_bf16 v[64:67], a[32:35], a[96:99], v[64:67]\n"
    "ds_read_b64_tr_b16 a[2:3], v12 offset:17408\n"
    "v_mfma_f32_16x16x32_bf16 v[96:99], a[32:35], a[100:103], v[96:99]\n"
    "s_barrier\n"
    "ds_read_b64_tr_b16 a[4:5], v12 offset:16392\n"
    "v_mfma_f32_16x16x32_bf16 v[128:131], a[32:35], a[104:107], v[128:131]\n"
    "ds_read_b64_tr_b16 a[6:7], v12 offset:17416\n"
    "v_mfma_f32_16x16x32_bf16 v[160:163], a[32:35], a[108:111], v[160:163]\n"
    "ds_read_b64_tr_b16 a[8:9], v13 offset:16384\n"
    "v_mfma_f32_16x16x32_bf16 v[192:195], a[32:35], a[112:115], v[192:195]\n"
    "ds_read_b64_tr_b16 a[10:11], v13 offset:17408\n"
    "v_mfma_f32_16x16x32_bf16 v[224:227], a[32:35], a[116:119], v[224:227]\n"
    "ds_read_b64_tr_b16 a[12:13], v13 offset:16392\n"
    "v_mfma_f32_16x16x32_bf16 v[36:39], a[36:39], a[92:95], v[36:39]\n"
    "ds_read_b64_tr_b16 a[14:15], v13 offset:17416\n"
    "v_mfma_f32_16x16x32_bf16 v[68:71], a[36:39], a[96:99], v[68:71]\n"
    "ds_read_b64_tr_b16 a[16:17], v14 offset:16384\n"
    "v_mfma_f32_16x16x32_bf16 v[100:103], a[36:39], a[100:103], v[100:103]\n"
    "ds_read_b64_tr_b16 a[18:19], v14 offset:17408\n"
    "v_mfma_f32_16x16x32_bf16 v[132:135], a[36:39], a[104:107], v[132:135]\n"
    "ds_read_b64_tr_b16 a[20:21], v14 offset:16392\n"
    "v_mfma_f32_16x16x32_bf16 v[164:167], a[36:39], a[108:111], v[164:167]\n"
    "ds_read_b64_tr_b16 a[22:23], v14 offset:17416\n"
    "v_mfma_f32_16x16x32_bf16 v[196:199], a[36:39], a[112:115], v[196:199]\n"
    "ds_read_b64_tr_b16 a[24:25], v15 offset:16384\n"
    "v_mfma_f32_16x16x32_bf16 v[228:231], a[36:39], a[116:119], v[228:231]\n"
    "ds_read_b64_tr_b16 a[26:27], v15 offset:17408\n"
    "v_mfma_f32_16x16x32_bf16 v[40:43], a[40:43], a[92:95], v[40:43]\n"
    "ds_read_b64_tr_b16 a[28:29], v15 offset:16392\n"
    "v_mfma_f32_16x16x32_bf16 v[72:75], a[40:43], a[96:99], v[72:75]\n"
    "ds_read_b64_tr_b16 a[30:31], v15 offset:17416\n"
    "v_mfma_f32_16x16x32_bf16 v[104:107], a[40:43], a[100:103], v[104:107]\n"
    "ds_read_b128 a[64:67], v10 offset:0\n"
    "v_mfma_f32_16x16x32_bf16 v[136:139], a[40:43], a[104:107], v[136:139]\n"
    "ds_read_b128 a[68:71], v10 offset:2048\n"
    "v_mfma_f32_16x16x32_bf16 v[168:171], a[40:43], a[108:111], v[168:171]\n"
    "ds_read_b128 a[72:75], v10 offset:4096\n"
    "v_mfma_f32_16x16x32_bf16 v[200:203], a[40:43], a[112:115], v[200:203]\n"
    "ds_read_b128 a[76:79], v10 offset:6144\n"
    "v_mfma_f32_16x16x32_bf16 v[232:235], a[40:43], a[116:119], v[232:235]\n"
    "ds_read_b128 a[80:83], v10 offset:40960\n"
    "v_mfma_f32_16x16x32_bf16 v[44:47], a[44:47], a[92:95], v[44:47]\n"
    "ds_read_b128 a[84:87], v10 offset:43008\n"
    "v_mfma_f32_16x16x32_bf16 v[76:79], a[44:47], a[96:99], v[76:79]\n"
    "ds_read_b128 a[88:91], v10 offset:45056\n"
    "v_mfma_f32_16x16x32_bf16 v[108:111], a[44:47], a[100:103], v[108:111]\n"
    "s_waitcnt lgkmcnt(7)\n"
    "v_mfma_f32_16x16x32_bf16 v[140:143], a[44:47], a[104:107], v[140:143]\n"
    "buffer_load_dwordx4 v5, s[48:51], s72 offen offset:0 nt lds\n"
    "v_mfma_f32_16x16x32_bf16 v[172:175], a[44:47], a[108:111], v[172:175]\n"
    "v_mfma_f32_16x16x32_bf16 v[204:207], a[44:47], a[112:115], v[204:207]\n"
    "v_mfma_f32_16x16x32_bf16 v[236:239], a[44:47], a[116:119], v[236:239]\n"
    "v_mfma_f32_16x16x32_bf16 v[48:51], a[48:51], a[92:95], v[48:51]\n"
    "v_mfma_f32_16x16x32_bf16 v[80:83], a[48:51], a[96:99], v[80:83]\n"
    "v_mfma_f32_16x16x32_bf16 v[112:115], a[48:51], a[100:103], v[112:115]\n"
    "v_mfma_f32_16x16x32_bf16 v[144:147], a[48:51], a[104:107], v[144:147]\n"
    "v_mfma_f32_16x16x32_bf16 v[176:179], a[48:51], a[108:111], v[176:179]\n"
    "v_mfma_f32_16x16x32_bf16 v[208:211], a[48:51], a[112:115], v[208:211]\n"
    "v_mfma_f32_16x16x32_bf16 v[240:243], a[48:51], a[116:119], v[240:243]\n"
    "v_mfma_f32_16x16x32_bf16 v[52:55], a[52:55], a[92:95], v[52:55]\n"
    "s_waitcnt vmcnt(3)\n"
    "v_mfma_f32_16x16x32_bf16 v[84:87], a[52:55], a[96:99], v[84:87]\n"
    "ds_read_b64_tr_b16 a[32:33], v12 offset:24576\n"
    "v_mfma_f32_16x16x32_bf16 v[116:119], a[52:55], a[100:103], v[116:119]\n"
    "buffer_load_dwordx4 v6, s[48:51], s72 offen offset:1024 nt lds\n"
    "v_mfma_f32_16x16x32_bf16 v[148:151], a[52:55], a[104:107], v[148:151]\n"
    "ds_read_b64_tr_b16 a[34:35], v12 offset:25600\n"
    "v_mfma_f32_16x16x32_bf16 v[180:183], a[52:55], a[108:111], v[180:183]\n"
    "ds_read_b64_tr_b16 a[36:37], v12 offset:24584\n"
    "v_mfma_f32_16x16x32_bf16 v[212:215], a[52:55], a[112:115], v[212:215]\n"
    "ds_read_b64_tr_b16 a[38:39], v12 offset:25608\n"
    "v_mfma_f32_16x16x32_bf16 v[244:247], a[52:55], a[116:119], v[244:247]\n"
    "ds_read_b64_tr_b16 a[40:41], v13 offset:24576\n"
    "v_mfma_f32_16x16x32_bf16 v[56:59], a[56:59], a[92:95], v[56:59]\n"
    "ds_read_b64_tr_b16 a[42:43], v13 offset:25600\n"
    "v_mfma_f32_16x16x32_bf16 v[88:91], a[56:59], a[96:99], v[88:91]\n"
    "ds_read_b64_tr_b16 a[44:45], v13 offset:24584\n"
    "v_mfma_f32_16x16x32_bf16 v[120:123], a[56:59], a[100:103], v[120:123]\n"
    "ds_read_b64_tr_b16 a[46:47], v13 offset:25608\n"
    "v_mfma_f32_16x16x32_bf16 v[152:155], a[56:59], a[104:107], v[152:155]\n"
    "ds_read_b64_tr_b16 a[48:49], v14 offset:24576\n"
    "v_mfma_f32_16x16x32_bf16 v[184:187], a[56:59], a[108:111], v[184:187]\n"
    "ds_read_b64_tr_b16 a[50:51], v14 offset:25600\n"
    "v_mfma_f32_16x16x32_bf16 v[216:219], a[56:59], a[112:115], v[216:219]\n"
    "ds_read_b64_tr_b16 a[52:53], v14 offset:24584\n"
    "v_mfma_f32_16x16x32_bf16 v[248:251], a[56:59], a[116:119], v[248:251]\n"
    "ds_read_b64_tr_b16 a[54:55], v14 offset:25608\n"
    "v_mfma_f32_16x16x32_bf16 v[60:63], a[60:63], a[92:95], v[60:63]\n"
    "ds_read_b64_tr_b16 a[56:57], v15 offset:24576\n"
    "v_mfma_f32_16x16x32_bf16 v[92:95], a[60:63], a[96:99], v[92:95]\n"
    "buffer_load_dwordx4 v7, s[48:51], s72 offen offset:2048 nt lds\n"
    "v_mfma_f32_16x16x32_bf16 v[124:127], a[60:63], a[100:103], v[124:127]\n"
    "ds_read_b64_tr_b16 a[58:59], v15 offset:25600\n"
    "v_mfma_f32_16x16x32_bf16 v[156:159], a[60:63], a[104:107], v[156:159]\n"
    "v_mfma_f32_16x16x32_bf16 v[188:191], a[60:63], a[108:111], v[188:191]\n"
    "v_mfma_f32_16x16x32_bf16 v[220:223], a[60:63], a[112:115], v[220:223]\n"
    "v_mfma_f32_16x16x32_bf16 v[252:255], a[60:63], a[116:119], v[252:255]\n"
    "s_waitcnt lgkmcnt(15)\n"
    "ds_read_b64_tr_b16 a[60:61], v15 offset:24584\n"
    "v_mfma_f32_16x16x32_bf16 v[32:35], a[0:3], a[64:67], v[32:35]\n"
    "ds_read_b64_tr_b16 a[62:63], v15 offset:25608\n"
    "v_mfma_f32_16x16x32_bf16 v[36:39], a[4:7], a[64:67], v[36:39]\n"
    "ds_read_b128 a[92:95], v11 offset:0\n"
    "v_mfma_f32_16x16x32_bf16 v[40:43], a[8:11], a[64:67], v[40:43]\n"
    "ds_read_b128 a[96:99], v11 offset:2048\n"
    "v_mfma_f32_16x16x32_bf16 v[44:47], a[12:15], a[64:67], v[44:47]\n"
    "ds_read_b128 a[100:103], v11 offset:4096\n"
    "v_mfma_f32_16x16x32_bf16 v[48:51], a[16:19], a[64:67], v[48:51]\n"
    "s_waitcnt lgkmcnt(3)\n"
    "ds_read_b128 a[104:107], v11 offset:6144\n"
    "v_mfma_f32_16x16x32_bf16 v[52:55], a[20:23], a[64:67], v[52:55]\n"
    "ds_read_b128 a[108:111], v11 offset:40960\n"
    "v_mfma_f32_16x16x32_bf16 v[56:59], a[24:27], a[64:67], v[56:59]\n"
    "ds_read_b128 a[112:115], v11 offset:43008\n"
    "v_mfma_f32_16x16x32_bf16 v[60:63], a[28:31], a[64:67], v[60:63]\n"
    "s_waitcnt lgkmcnt(5)\n"
    "buffer_load_dwordx4 v8, s[48:51], s72 offen offset:3072 lds\n"
    "v_mfma_f32_16x16x32_bf16 v[32:35], a[32:35], a[92:95], v[32:35]\n"
    "s_add_u32 s72, s72, 4096\n"
    "ds_read_b128 a[116:119], v11 offset:45056\n"
    "v_mfma_f32_16x16x32_bf16 v[36:39], a[36:39], a[92:95], v[36:39]\n"
    "s_and_b32 s72, s72, 131071\n"
    "ds_read_b128 a[224:227], v20 offset:0\n"
    "v_mfma_f32_16x16x32_bf16 v[40:43], a[40:43], a[92:95], v[40:43]\n"
    "s_add_u32 s73, s73, 4096\n"
    "ds_read_b128 a[228:231], v21 offset:0\n"
    "v_mfma_f32_16x16x32_bf16 v[44:47], a[44:47], a[92:95], v[44:47]\n"
    "s_sub_u32 s74, s73, 40960\n"
    "ds_read_b128 a[232:235], v22 offset:0\n"
    "v_mul_f32 v32, s75, v32\n"
    "v_mul_f32 v33, s75, v33\n"
    "v_mul_f32 v34, s75, v34\n"
    "v_mfma_f32_16x16x32_bf16 v[48:51], a[48:51], a[92:95], v[48:51]\n"
    "s_min_u32 s73, s73, s74\n"
    "ds_read_b128 a[236:239], v23 offset:0\n"
    "v_mul_f32 v35, s75, v35\n"
    "v_mul_f32 v36, s75, v36\n"
    "v_mul_f32 v37, s75, v37\n"
    "v_mfma_f32_16x16x32_bf16 v[52:55], a[52:55], a[92:95], v[52:55]\n"
    "s_add_u32 m0, s60, s73\n"
    "ds_read_b128 a[240:243], v20 offset:4096\n"
    "v_mul_f32 v38, s75, v38\n"
    "v_mul_f32 v39, s75, v39\n"
    "v_cvt_pk_bf16_f32 v32, v32, v33\n"
    "v_mfma_f32_16x16x32_bf16 v[56:59], a[56:59], a[92:95], v[56:59]\n"
    "ds_read_b128 a[244:247], v21 offset:4096\n"
    "v_cvt_pk_bf16_f32 v33, v34, v35\n"
    "v_cvt_pk_bf16_f32 v34, v36, v37\n"
    "v_cvt_pk_bf16_f32 v35, v38, v39\n"
    "v_mul_f32 v40, s75, v40\n"
    "v_mul_f32 v41, s75, v41\n"
    "v_mul_f32 v42, s75, v42\n"
    "v_mfma_f32_16x16x32_bf16 v[60:63], a[60:63], a[92:95], v[60:63]\n"
    "ds_read_b128 a[248:251], v22 offset:4096\n"
    "v_mul_f32 v43, s75, v43\n"
    "v_mul_f32 v44, s75, v44\n"
    "v_mul_f32 v45, s75, v45\n"
    "v_mfma_f32_16x16x32_bf16 v[64:67], a[0:3], a[68:71], v[64:67]\n"
    "s_waitcnt lgkmcnt(12)\n"
    "ds_read_b128 a[252:255], v23 offset:4096\n"
    "v_mul_f32 v46, s75, v46\n"
    "v_mul_f32 v47, s75, v47\n"
    "v_cvt_pk_bf16_f32 v36, v40, v41\n"
    "v_mfma_f32_16x16x32_bf16 v[64:67], a[32:35], a[96:99], v[64:67]\n"
    "v_cvt_pk_bf16_f32 v37, v42, v43\n"
    "v_cvt_pk_bf16_f32 v38, v44, v45\n"
    "v_cvt_pk_bf16_f32 v39, v46, v47\n"
    "v_mul_f32 v48, s75, v48\n"
    "v_mul_f32 v49, s75, v49\n"
    "v_mul_f32 v50, s75, v50\n"
    "v_mfma_f32_16x16x32_bf16 v[68:71], a[4:7], a[68:71], v[68:71]\n"
    "v_mul_f32 v51, s75, v51\n"
    "v_mul_f32 v52, s75, v52\n"
    "v_mul_f32 v53, s75, v53\n"
    "v_mfma_f32_16x16x32_bf16 v[68:71], a[36:39], a[96:99], v[68:71]\n"
    "v_mul_f32 v54, s75, v54\n"
    "v_mul_f32 v55, s75, v55\n"
    "v_cvt_pk_bf16_f32 v40, v48, v49\n"
    "v_mfma_f32_16x16x32_bf16 v[72:75], a[8:11], a[68:71], v[72:75]\n"
    "buffer_load_dwordx4 v5, s[48:51], s72 offen offset:0 nt lds\n"
    "v_mfma_f32_16x16x32_bf16 v[72:75], a[40:43], a[96:99], v[72:75]\n"
    "v_cvt_pk_bf16_f32 v41, v50, v51\n"
    "v_cvt_pk_bf16_f32 v42, v52, v53\n"
    "v_cvt_pk_bf16_f32 v43, v54, v55\n"
    "v_mul_f32 v56, s75, v56\n"
    "v_mul_f32 v57, s75, v57\n"
    "v_mul_f32 v58, s75, v58\n"
    "v_mfma_f32_16x16x32_bf16 v[76:79], a[12:15], a[68:71], v[76:79]\n"
    "s_waitcnt vmcnt(5) lgkmcnt(0)\n"
    "v_mul_f32 v59, s75, v59\n"
    "v_mul_f32 v60, s75, v60\n"
    "v_mul_f32 v61, s75, v61\n"
    "v_mfma_f32_16x16x32_bf16 v[76:79], a[44:47], a[96:99], v[76:79]\n"
    "s_barrier\n"
    "v_mul_f32 v62, s75, v62\n"
    "v_mul_f32 v63, s75, v63\n"
    "v_cvt_pk_bf16_f32 v44, v56, v57\n"
    "v_mfma_f32_16x16x32_bf16 v[80:83], a[16:19], a[68:71], v[80:83]\n"
    "ds_write_b128 v9, a[168:171] offset:32768\n"
    "v_cvt_pk_bf16_f32 v45, v58, v59\n"
    "v_cvt_pk_bf16_f32 v46, v60, v61\n"
    "v_cvt_pk_bf16_f32 v47, v62, v63\n"
    "v_mul_f32 v64, s75, v64\n"
    "v_mul_f32 v65, s75, v65\n"
    "v_mul_f32 v66, s75, v66\n"
    "v_accvgpr_read_b32 v1, a120\n"
    "v_accvgpr_read_b32 v2, a121\n"
    "v_accvgpr_read_b32 v4, a122\n"
    "v_accvgpr_read_b32 v11, a123\n"
    "v_add_u32 v3, 81920, v2\n"
    "v_add_u32 v10, 73728, v4\n"
    "v_accvgpr_read_b32 v16, a128\n"
    "v_mfma_f32_16x16x32_bf16 v[80:83], a[48:51], a[96:99], v[80:83]\n"
    "v_mfma_f32_16x16x32_bf16 v[12:15], a[224:227], v[32:35], 0\n"
    "v_mul_f32 v67, s75, v67\n"
    "v_mul_f32 v68, s75, v68\n"
    "v_mul_f32 v69, s75, v69\n"
    "v_readfirstlane_b32 s40, v16\n"
    "v_accvgpr_read_b32 v17, a129\n"
    "v_mfma_f32_16x16x32_bf16 v[84:87], a[20:23], a[68:71], v[84:87]\n"
    "v_mfma_f32_16x16x32_bf16 v[12:15], a[228:231], v[36:39], v[12:15]\n"
    "v_mul_f32 v70, s75, v70\n"
    "v_mul_f32 v71, s75, v71\n"
    "v_cvt_pk_bf16_f32 v48, v64, v65\n"
    "v_readfirstlane_b32 s41, v17\n"
    "v_accvgpr_read_b32 v16, a130\n"
    "v_mfma_f32_16x16x32_bf16 v[84:87], a[52:55], a[96:99], v[84:87]\n"
    "v_mfma_f32_16x16x32_bf16 v[12:15], a[232:235], v[40:43], v[12:15]\n"
    "v_cvt_pk_bf16_f32 v49, v66, v67\n"
    "v_cvt_pk_bf16_f32 v50, v68, v69\n"
    "v_cvt_pk_bf16_f32 v51, v70, v71\n"
    "v_mul_f32 v72, s75, v72\n"
    "v_mul_f32 v73, s75, v73\n"
    "v_mul_f32 v74, s75, v74\n"
    "v_readfirstlane_b32 s42, v16\n"
    "v_accvgpr_read_b32 v17, a131\n"
    "v_mfma_f32_16x16x32_bf16 v[88:91], a[24:27], a[68:71], v[88:91]\n"
    "buffer_load_dwordx4 v6, s[48:51], s72 offen offset:1024 nt lds\n"
    "v_mfma_f32_16x16x32_bf16 v[88:91], a[56:59], a[96:99], v[88:91]\n"
    "v_mfma_f32_16x16x32_bf16 v[12:15], a[236:239], v[44:47], v[12:15]\n"
    "v_mul_f32 v75, s75, v75\n"
    "v_mul_f32 v76, s75, v76\n"
    "v_mul_f32 v77, s75, v77\n"
    "v_mul_f32 v78, s75, v78\n"
    "v_mul_f32 v79, s75, v79\n"
    "v_cvt_pk_bf16_f32 v52, v72, v73\n"
    "v_readfirstlane_b32 s43, v17\n"
    "v_accvgpr_read_b32 v16, a132\n"
    "v_mfma_f32_16x16x32_bf16 v[92:95], a[28:31], a[68:71], v[92:95]\n"
    "ds_write_b128 v9, a[172:175] offset:33792\n"
    "v_cvt_pk_bf16_f32 v53, v74, v75\n"
    "v_cvt_pk_bf16_f32 v54, v76, v77\n"
    "v_cvt_pk_bf16_f32 v55, v78, v79\n"
    "v_mul_f32 v80, s75, v80\n"
    "v_mul_f32 v81, s75, v81\n"
    "v_mul_f32 v82, s75, v82\n"
    "v_readfirstlane_b32 s44, v16\n"
    "v_accvgpr_read_b32 v17, a133\n"
    "v_mfma_f32_16x16x32_bf16 v[92:95], a[60:63], a[96:99], v[92:95]\n"
    "v_mul_f32 v83, s75, v83\n"
    "v_mul_f32 v84, s75, v84\n"
    "v_mul_f32 v85, s75, v85\n"
    "v_readfirstlane_b32 s45, v17\n"
    "v_accvgpr_read_b32 v16, a134\n"
    "v_mfma_f32_16x16x32_bf16 v[96:99], a[0:3], a[72:75], v[96:99]\n"
    "v_max3_f32 v18, v12, v13, v14\n"
    "v_max_f32 v18, v18, v15\n"
    "v_mov_b32 v19, v18\n"
    "v_mul_f32 v86, s75, v86\n"
    "v_mul_f32 v87, s75, v87\n"
    "v_cvt_pk_bf16_f32 v56, v80, v81\n"
    "v_readfirstlane_b32 s46, v16\n"
    "v_accvgpr_read_b32 v17, a135\n"
    "v_mfma_f32_16x16x32_bf16 v[96:99], a[32:35], a[100:103], v[96:99]\n"
    "v_permlane32_swap_b32 v18, v19\n"
    "v_max_f32 v18, v18, v19\n"
    "v_mov_b32 v19, v18\n"
    "v_cvt_pk_bf16_f32 v57, v82, v83\n"
    "v_cvt_pk_bf16_f32 v58, v84, v85\n"
    "v_cvt_pk_bf16_f32 v59, v86, v87\n"
    "v_mul_f32 v88, s75, v88\n"
    "v_mul_f32 v89, s75, v89\n"
    "v_mul_f32 v90, s75, v90\n"
    "v_readfirstlane_b32 s47, v17\n"
    "v_accvgpr_read_b32 v16, a202\n"
    "v_mfma_f32_16x16x32_bf16 v[100:103], a[4:7], a[72:75], v[100:103]\n"
    "v_permlane16_swap_b32 v18, v19\n"
    "v_min_f32_e64 v18, -v18, -v19\n"
    "v_accvgpr_write_b32 a204, v18\n"
    "v_mul_f32 v91, s75, v91\n"
    "v_mul_f32 v92, s75, v92\n"
    "v_mul_f32 v93, s75, v93\n"
    "v_readfirstlane_b32 s68, v16\n"
    "v_accvgpr_read_b32 v17, a203\n"
    "v_mfma_f32_16x16x32_bf16 v[100:103], a[36:39], a[100:103], v[100:103]\n"
    "v_mul_f32 v94, s75, v94\n"
    "v_mul_f32 v95, s75, v95\n"
    "v_cvt_pk_bf16_f32 v60, v88, v89\n"
    "v_readfirstlane_b32 s69, v17\n"
    "v_accvgpr_read_b32 v16, a126\n"
    "v_mfma_f32_16x16x32_bf16 v[104:107], a[8:11], a[72:75], v[104:107]\n"
    "buffer_load_dwordx4 v7, s[48:51], s72 offen offset:2048 nt lds\n"
    "v_mfma_f32_16x16x32_bf16 v[104:107], a[40:43], a[100:103], v[104:107]\n"
    "ds_write_b128 v9, a[176:179] offset:34816\n"
    "v_cvt_pk_bf16_f32 v61, v90, v91\n"
    "v_cvt_pk_bf16_f32 v62, v92, v93\n"
    "v_cvt_pk_bf16_f32 v63, v94, v95\n"
    "v_mul_f32 v96, s75, v96\n"
    "v_mul_f32 v97, s75, v97\n"
    "v_mul_f32 v98, s75, v98\n"
    "v_readfirstlane_b32 s28, v16\n"
    "v_accvgpr_read_b32 v16, a124\n"
    "v_accvgpr_read_b32 v17, a125\n"
    "v_mfma_f32_16x16x32_bf16 v[108:111], a[12:15], a[72:75], v[108:111]\n"
    "v_mfma_f32_16x16x32_bf16 v[12:15], a[224:227], v[48:51], 0\n"
    "v_mul_f32 v99, s75, v99\n"
    "v_mul_f32 v100, s75, v100\n"
    "v_mul_f32 v101, s75, v101\n"
    "v_mfma_f32_16x16x32_bf16 v[108:111], a[44:47], a[100:103], v[108:111]\n"
    "s_add_u32 s68, s68, 1\n"
    "s_addc_u32 s69, s69, 0\n"
    "v_mfma_f32_16x16x32_bf16 v[12:15], a[228:231], v[52:55], v[12:15]\n"
    "v_mul_f32 v102, s75, v102\n"
    "v_mul_f32 v103, s75, v103\n"
    "v_cvt_pk_bf16_f32 v64, v96, v97\n"
    "v_mfma_f32_16x16x32_bf16 v[112:115], a[16:19], a[72:75], v[112:115]\n"
    "v_mfma_f32_16x16x32_bf16 v[12:15], a[232:235], v[56:59], v[12:15]\n"
    "v_cvt_pk_bf16_f32 v65, v98, v99\n"
    "v_cvt_pk_bf16_f32 v66, v100, v101\n"
    "v_cvt_pk_bf16_f32 v67, v102, v103\n"
    "v_mfma_f32_16x16x32_bf16 v[112:115], a[48:51], a[100:103], v[112:115]\n"
    "v_mfma_f32_16x16x32_bf16 v[12:15], a[236:239], v[60:63], v[12:15]\n"
    "v_mul_f32 v104, s75, v104\n"
    "v_mul_f32 v105, s75, v105\n"
    "v_mul_f32 v106, s75, v106\n"
    "v_mfma_f32_16x16x32_bf16 v[116:119], a[20:23], a[72:75], v[116:119]\n"
    "v_mul_f32 v107, s75, v107\n"
    "v_mul_f32 v108, s75, v108\n"
    "v_mul_f32 v109, s75, v109\n"
    "v_mfma_f32_16x16x32_bf16 v[116:119], a[52:55], a[100:103], v[116:119]\n"
    "ds_write_b128 v9, a[180:183] offset:35840\n"
    "v_mul_f32 v110, s75, v110\n"
    "v_mul_f32 v111, s75, v111\n"
    "v_cvt_pk_bf16_f32 v68, v104, v105\n"
    "v_mfma_f32_16x16x32_bf16 v[120:123], a[24:27], a[72:75], v[120:123]\n"
    "buffer_load_dwordx4 v8, s[48:51], s72 offen offset:3072 lds\n"
    "v_mfma_f32_16x16x32_bf16 v[120:123], a[56:59], a[100:103], v[120:123]\n"
    "s_add_u32 s72, s72, 4096\n"
    "v_max3_f32 v18, v12, v13, v14\n"
    "v_max_f32 v18, v18, v15\n"
    "v_mov_b32 v19, v18\n"
    "v_cvt_pk_bf16_f32 v69, v106, v107\n"
    "v_cvt_pk_bf16_f32 v70, v108, v109\n"
    "v_cvt_pk_bf16_f32 v71, v110, v111\n"
    "v_mul_f32 v112, s75, v112\n"
    "v_mul_f32 v113, s75, v113\n"
    "v_mul_f32 v114, s75, v114\n"
    "v_mfma_f32_16x16x32_bf16 v[124:127], a[28:31], a[72:75], v[124:127]\n"
    "s_and_b32 s72, s72, 131071\n"
    "v_permlane32_swap_b32 v18, v19\n"
    "v_max_f32 v18, v18, v19\n"
    "v_mov_b32 v19, v18\n"
    "v_mul_f32 v115, s75, v115\n"
    "v_mul_f32 v116, s75, v116\n"
    "v_mul_f32 v117, s75, v117\n"
    "v_mfma_f32_16x16x32_bf16 v[124:127], a[60:63], a[100:103], v[124:127]\n"
    "s_add_u32 s73, s73, 4096\n"
    "v_permlane16_swap_b32 v18, v19\n"
    "v_min_f32_e64 v18, -v18, -v19\n"
    "v_accvgpr_write_b32 a205, v18\n"
    "v_mul_f32 v118, s75, v118\n"
    "v_mul_f32 v119, s75, v119\n"
    "v_cvt_pk_bf16_f32 v72, v112, v113\n"
    "v_mfma_f32_16x16x32_bf16 v[128:131], a[0:3], a[76:79], v[128:131]\n"
    "s_sub_u32 s74, s73, 40960\n"
    "v_cvt_pk_bf16_f32 v73, v114, v115\n"
    "v_cvt_pk_bf16_f32 v74, v116, v117\n"
    "v_cvt_pk_bf16_f32 v75, v118, v119\n"
    "v_mfma_f32_16x16x32_bf16 v[128:131], a[32:35], a[104:107], v[128:131]\n"
    "s_min_u32 s73, s73, s74\n"
    "ds_write_b128 v9, a[184:187] offset:36864\n"
    "v_mul_f32 v120, s75, v120\n"
    "v_mul_f32 v121, s75, v121\n"
    "v_mul_f32 v122, s75, v122\n"
    "v_mfma_f32_16x16x32_bf16 v[132:135], a[4:7], a[76:79], v[132:135]\n"
    "s_add_u32 m0, s60, s73\n"
    "v_mul_f32 v123, s75, v123\n"
    "v_mul_f32 v124, s75, v124\n"
    "v_mul_f32 v125, s75, v125\n"
    "v_mfma_f32_16x16x32_bf16 v[132:135], a[36:39], a[104:107], v[132:135]\n"
    "v_mul_f32 v126, s75, v126\n"
    "v_mul_f32 v127, s75, v127\n"
    "v_cvt_pk_bf16_f32 v76, v120, v121\n"
    "v_mfma_f32_16x16x32_bf16 v[136:139], a[8:11], a[76:79], v[136:139]\n"
    "buffer_load_dwordx4 v5, s[48:51], s72 offen offset:0 nt lds\n"
    "v_mfma_f32_16x16x32_bf16 v[136:139], a[40:43], a[104:107], v[136:139]\n"
    "v_cvt_pk_bf16_f32 v77, v122, v123\n"
    "v_cvt_pk_bf16_f32 v78, v124, v125\n"
    "v_cvt_pk_bf16_f32 v79, v126, v127\n"
    "v_mul_f32 v128, s75, v128\n"
    "v_mul_f32 v129, s75, v129\n"
    "v_mul_f32 v130, s75, v130\n"
    "v_mfma_f32_16x16x32_bf16 v[140:143], a[12:15], a[76:79], v[140:143]\n"
    "v_mfma_f32_16x16x32_bf16 v[12:15], a[224:227], v[64:67], 0\n"
    "v_mul_f32 v131, s75, v131\n"
    "v_mul_f32 v132, s75, v132\n"
    "v_mul_f32 v133, s75, v133\n"
    "v_mfma_f32_16x16x32_bf16 v[140:143], a[44:47], a[104:107], v[140:143]\n"
    "ds_write_b128 v9, a[188:191] offset:37888\n"
    "v_mfma_f32_16x16x32_bf16 v[12:15], a[228:231], v[68:71], v[12:15]\n"
    "v_mul_f32 v134, s75, v134\n"
    "v_mul_f32 v135, s75, v135\n"
    "v_cvt_pk_bf16_f32 v80, v128, v129\n"
    "v_mfma_f32_16x16x32_bf16 v[144:147], a[16:19], a[76:79], v[144:147]\n"
    "v_mfma_f32_16x16x32_bf16 v[12:15], a[232:235], v[72:75], v[12:15]\n"
    "v_cvt_pk_bf16_f32 v81, v130, v131\n"
    "v_cvt_pk_bf16_f32 v82, v132, v133\n"
    "v_cvt_pk_bf16_f32 v83, v134, v135\n"
    "v_mfma_f32_16x16x32_bf16 v[144:147], a[48:51], a[104:107], v[144:147]\n"
    "v_mfma_f32_16x16x32_bf16 v[12:15], a[236:239], v[76:79], v[12:15]\n"
    "v_mul_f32 v136, s75, v136\n"
    "v_mul_f32 v137, s75, v137\n"
    "v_mul_f32 v138, s75, v138\n"
    "v_mfma_f32_16x16x32_bf16 v[148:151], a[20:23], a[76:79], v[148:151]\n"
    "v_mul_f32 v139, s75, v139\n"
    "v_mul_f32 v140, s75, v140\n"
    "v_mul_f32 v141, s75, v141\n"
    "v_mfma_f32_16x16x32_bf16 v[148:151], a[52:55], a[104:107], v[148:151]\n"
    "v_mul_f32 v142, s75, v142\n"
    "v_mul_f32 v143, s75, v143\n"
    "v_cvt_pk_bf16_f32 v84, v136, v137\n"
    "v_mfma_f32_16x16x32_bf16 v[152:155], a[24:27], a[76:79], v[152:155]\n"
    "buffer_load_dwordx4 v6, s[48:51], s72 offen offset:1024 nt lds\n"
    "v_mfma_f32_16x16x32_bf16 v[152:155], a[56:59], a[104:107], v[152:155]\n"
    "ds_write_b128 v9, a[192:195] offset:38912\n"
    "v_max3_f32 v18, v12, v13, v14\n"
    "v_max_f32 v18, v18, v15\n"
    "v_mov_b32 v19, v18\n"
    "v_cvt_pk_bf16_f32 v85, v138, v139\n"
    "v_cvt_pk_bf16_f32 v86, v140, v141\n"
    "v_cvt_pk_bf16_f32 v87, v142, v143\n"
    "v_mul_f32 v144, s75, v144\n"
    "v_mul_f32 v145, s75, v145\n"
    "v_mul_f32 v146, s75, v146\n"
    "v_mfma_f32_16x16x32_bf16 v[156:159], a[28:31], a[76:79], v[156:159]\n"
    "v_permlane32_swap_b32 v18, v19\n"
    "v_max_f32 v18, v18, v19\n"
    "v_mov_b32 v19, v18\n"
    "v_mul_f32 v147, s75, v147\n"
    "v_mul_f32 v148, s75, v148\n"
    "v_mul_f32 v149, s75, v149\n"
    "v_mfma_f32_16x16x32_bf16 v[156:159], a[60:63], a[104:107], v[156:159]\n"
    "v_permlane16_swap_b32 v18, v19\n"
    "v_min_f32_e64 v18, -v18, -v19\n"
    "v_accvgpr_write_b32 a206, v18\n"
    "v_mul_f32 v150, s75, v150\n"
    "v_mul_f32 v151, s75, v151\n"
    "v_cvt_pk_bf16_f32 v88, v144, v145\n"
    "v_mfma_f32_16x16x32_bf16 v[160:163], a[0:3], a[80:83], v[160:163]\n"
    "v_cvt_pk_bf16_f32 v89, v146, v147\n"
    "v_cvt_pk_bf16_f32 v90, v148, v149\n"
    "v_cvt_pk_bf16_f32 v91, v150, v151\n"
    "v_mfma_f32_16x16x32_bf16 v[160:163], a[32:35], a[108:111], v[160:163]\n"
    "v_mul_f32 v152, s75, v152\n"
    "v_mul_f32 v153, s75, v153\n"
    "v_mul_f32 v154, s75, v154\n"
    "v_mfma_f32_16x16x32_bf16 v[164:167], a[4:7], a[80:83], v[164:167]\n"
    "v_mul_f32 v155, s75, v155\n"
    "v_mul_f32 v156, s75, v156\n"
    "v_mul_f32 v157, s75, v157\n"
    "v_mfma_f32_16x16x32_bf16 v[164:167], a[36:39], a[108:111], v[164:167]\n"
    "ds_write_b128 v9, a[196:199] offset:39936\n"
    "v_mul_f32 v158, s75, v158\n"
    "v_mul_f32 v159, s75, v159\n"
    "v_cvt_pk_bf16_f32 v92, v152, v153\n"
    "v_mfma_f32_16x16x32_bf16 v[168:171], a[8:11], a[80:83], v[168:171]\n"
    "buffer_load_dwordx4 v7, s[48:51], s72 offen offset:2048 nt lds\n"
    "v_mfma_f32_16x16x32_bf16 v[168:171], a[40:43], a[108:111], v[168:171]\n"
    "v_cvt_pk_bf16_f32 v93, v154, v155\n"
    "v_cvt_pk_bf16_f32 v94, v156, v157\n"
    "v_cvt_pk_bf16_f32 v95, v158, v159\n"
    "v_mul_f32 v160, s75, v160\n"
    "v_mul_f32 v161, s75, v161\n"
    "v_mul_f32 v162, s75, v162\n"
    "v_mfma_f32_16x16x32_bf16 v[172:175], a[12:15], a[80:83], v[172:175]\n"
    "v_mfma_f32_16x16x32_bf16 v[12:15], a[224:227], v[80:83], 0\n"
    "v_mul_f32 v163, s75, v163\n"
    "v_mul_f32 v164, s75, v164\n"
    "v_mul_f32 v165, s75, v165\n"
    "v_accvgpr_read_b32 v144, a204\n"
    "v_accvgpr_read_b32 v145, a204\n"
    "v_accvgpr_read_b32 v148, a205\n"
    "v_accvgpr_read_b32 v149, a205\n"
    "v_accvgpr_read_b32 v152, a206\n"
    "v_accvgpr_read_b32 v153, a206\n"
    "v_mfma_f32_16x16x32_bf16 v[172:175], a[44:47], a[108:111], v[172:175]\n"
    "v_mfma_f32_16x16x32_bf16 v[12:15], a[228:231], v[84:87], v[12:15]\n"
    "v_mul_f32 v166, s75, v166\n"
    "v_mul_f32 v167, s75, v167\n"
    "v_cvt_pk_bf16_f32 v96, v160, v161\n"
    "v_accvgpr_read_b32 v146, a204\n"
    "v_accvgpr_read_b32 v147, a204\n"
    "v_accvgpr_read_b32 v150, a205\n"
    "v_accvgpr_read_b32 v151, a205\n"
    "v_accvgpr_read_b32 v154, a206\n"
    "v_accvgpr_read_b32 v155, a206\n"
    "v_mfma_f32_16x16x32_bf16 v[176:179], a[16:19], a[80:83], v[176:179]\n"
    "v_mfma_f32_16x16x32_bf16 v[12:15], a[232:235], v[88:91], v[12:15]\n"
    "v_cvt_pk_bf16_f32 v97, v162, v163\n"
    "v_cvt_pk_bf16_f32 v98, v164, v165\n"
    "v_cvt_pk_bf16_f32 v99, v166, v167\n"
    "v_mfma_f32_16x16x32_bf16 v[176:179], a[48:51], a[108:111], v[176:179]\n"
    "v_mfma_f32_16x16x32_bf16 v[12:15], a[236:239], v[92:95], v[12:15]\n"
    "v_mul_f32 v168, s75, v168\n"
    "v_mul_f32 v169, s75, v169\n"
    "v_mul_f32 v170, s75, v170\n"
    "v_mfma_f32_16x16x32_bf16 v[180:183], a[20:23], a[80:83], v[180:183]\n"
    "v_mul_f32 v171, s75, v171\n"
    "v_mul_f32 v172, s75, v172\n"
    "v_mul_f32 v173, s75, v173\n"
    "v_mfma_f32_16x16x32_bf16 v[180:183], a[52:55], a[108:111], v[180:183]\n"
    "v_mul_f32 v174, s75, v174\n"
    "v_mul_f32 v175, s75, v175\n"
    "v_cvt_pk_bf16_f32 v100, v168, v169\n"
    "v_mfma_f32_16x16x32_bf16 v[184:187], a[24:27], a[80:83], v[184:187]\n"
    "buffer_load_dwordx4 v8, s[48:51], s72 offen offset:3072 lds\n"
    "v_mfma_f32_16x16x32_bf16 v[184:187], a[56:59], a[108:111], v[184:187]\n"
    "s_add_u32 s72, s72, 4096\n"
    "v_max3_f32 v18, v12, v13, v14\n"
    "v_max_f32 v18, v18, v15\n"
    "v_mov_b32 v19, v18\n"
    "v_cvt_pk_bf16_f32 v101, v170, v171\n"
    "v_cvt_pk_bf16_f32 v102, v172, v173\n"
    "v_cvt_pk_bf16_f32 v103, v174, v175\n"
    "v_mul_f32 v176, s75, v176\n"
    "v_mul_f32 v177, s75, v177\n"
    "v_mul_f32 v178, s75, v178\n"
    "v_mfma_f32_16x16x32_bf16 v[188:191], a[28:31], a[80:83], v[188:191]\n"
    "s_and_b32 s72, s72, 131071\n"
    "v_permlane32_swap_b32 v18, v19\n"
    "v_max_f32 v18, v18, v19\n"
    "v_mov_b32 v19, v18\n"
    "v_mul_f32 v179, s75, v179\n"
    "v_mul_f32 v180, s75, v180\n"
    "v_mul_f32 v181, s75, v181\n"
    "v_mfma_f32_16x16x32_bf16 v[188:191], a[60:63], a[108:111], v[188:191]\n"
    "s_add_u32 s73, s73, 4096\n"
    "v_permlane16_swap_b32 v18, v19\n"
    "v_min_f32_e64 v18, -v18, -v19\n"
    "v_accvgpr_write_b32 a207, v18\n"
    "v_mul_f32 v182, s75, v182\n"
    "v_mul_f32 v183, s75, v183\n"
    "v_cvt_pk_bf16_f32 v104, v176, v177\n"
    "v_mfma_f32_16x16x32_bf16 v[192:195], a[0:3], a[84:87], v[192:195]\n"
    "s_sub_u32 s74, s73, 40960\n"
    "v_cvt_pk_bf16_f32 v105, v178, v179\n"
    "v_cvt_pk_bf16_f32 v106, v180, v181\n"
    "v_cvt_pk_bf16_f32 v107, v182, v183\n"
    "v_accvgpr_read_b32 v156, a207\n"
    "v_accvgpr_read_b32 v157, a207\n"
    "v_mfma_f32_16x16x32_bf16 v[192:195], a[32:35], a[112:115], v[192:195]\n"
    "s_min_u32 s73, s73, s74\n"
    "v_mul_f32 v184, s75, v184\n"
    "v_mul_f32 v185, s75, v185\n"
    "v_mul_f32 v186, s75, v186\n"
    "v_accvgpr_read_b32 v158, a207\n"
    "v_accvgpr_read_b32 v159, a207\n"
    "v_mfma_f32_16x16x32_bf16 v[196:199], a[4:7], a[84:87], v[196:199]\n"
    "s_add_u32 m0, s60, s73\n"
    "v_mul_f32 v187, s75, v187\n"
    "v_mul_f32 v188, s75, v188\n"
    "v_mul_f32 v189, s75, v189\n"
    "v_mfma_f32_16x16x32_bf16 v[196:199], a[36:39], a[112:115], v[196:199]\n"
    "v_mul_f32 v190, s75, v190\n"
    "v_mul_f32 v191, s75, v191\n"
    "v_cvt_pk_bf16_f32 v108, v184, v185\n"
    "v_mfma_f32_16x16x32_bf16 v[200:203], a[8:11], a[84:87], v[200:203]\n"
    "buffer_load_dwordx4 v5, s[48:51], s72 offen offset:0 nt lds\n"
    "v_mfma_f32_16x16x32_bf16 v[200:203], a[40:43], a[112:115], v[200:203]\n"
    "v_cvt_pk_bf16_f32 v109, v186, v187\n"
    "v_cvt_pk_bf16_f32 v110, v188, v189\n"
    "v_cvt_pk_bf16_f32 v111, v190, v191\n"
    "v_mul_f32 v192, s75, v192\n"
    "v_mul_f32 v193, s75, v193\n"
    "v_mul_f32 v194, s75, v194\n"
    "v_mfma_f32_16x16x32_bf16 v[204:207], a[12:15], a[84:87], v[204:207]\n"
    "v_mfma_f32_16x16x32_bf16 v[12:15], a[224:227], v[96:99], 0\n"
    "v_mul_f32 v195, s75, v195\n"
    "v_mul_f32 v196, s75, v196\n"
    "v_mul_f32 v197, s75, v197\n"
    "v_mfma_f32_16x16x32_bf16 v[204:207], a[44:47], a[112:115], v[204:207]\n"
    "v_mfma_f32_16x16x32_bf16 v[12:15], a[228:231], v[100:103], v[12:15]\n"
    "v_mul_f32 v198, s75, v198\n"
    "v_mul_f32 v199, s75, v199\n"
    "v_cvt_pk_bf16_f32 v112, v192, v193\n"
    "v_mfma_f32_16x16x32_bf16 v[208:211], a[16:19], a[84:87], v[208:211]\n"
    "v_mfma_f32_16x16x32_bf16 v[12:15], a[232:235], v[104:107], v[12:15]\n"
    "v_cvt_pk_bf16_f32 v113, v194, v195\n"
    "v_cvt_pk_bf16_f32 v114, v196, v197\n"
    "v_cvt_pk_bf16_f32 v115, v198, v199\n"
    "v_mfma_f32_16x16x32_bf16 v[208:211], a[48:51], a[112:115], v[208:211]\n"
    "v_mfma_f32_16x16x32_bf16 v[12:15], a[236:239], v[108:111], v[12:15]\n"
    "v_mul_f32 v200, s75, v200\n"
    "v_mul_f32 v201, s75, v201\n"
    "v_mul_f32 v202, s75, v202\n"
    "v_mfma_f32_16x16x32_bf16 v[212:215], a[20:23], a[84:87], v[212:215]\n"
    "v_mul_f32 v203, s75, v203\n"
    "v_mul_f32 v204, s75, v204\n"
    "v_mul_f32 v205, s75, v205\n"
    "v_mfma_f32_16x16x32_bf16 v[212:215], a[52:55], a[112:115], v[212:215]\n"
    "v_mul_f32 v206, s75, v206\n"
    "v_mul_f32 v207, s75, v207\n"
    "v_cvt_pk_bf16_f32 v116, v200, v201\n"
    "v_mfma_f32_16x16x32_bf16 v[216:219], a[24:27], a[84:87], v[216:219]\n"
    "buffer_load_dwordx4 v6, s[48:51], s72 offen offset:1024 nt lds\n"
    "v_mfma_f32_16x16x32_bf16 v[216:219], a[56:59], a[112:115], v[216:219]\n"
    "v_max3_f32 v18, v12, v13, v14\n"
    "v_max_f32 v18, v18, v15\n"
    "v_mov_b32 v19, v18\n"
    "v_cvt_pk_bf16_f32 v117, v202, v203\n"
    "v_cvt_pk_bf16_f32 v118, v204, v205\n"
    "v_cvt_pk_bf16_f32 v119, v206, v207\n"
    "v_mul_f32 v208, s75, v208\n"
    "v_mul_f32 v209, s75, v209\n"
    "v_mul_f32 v210, s75, v210\n"
    "v_mfma_f32_16x16x32_bf16 v[220:223], a[28:31], a[84:87], v[220:223]\n"
    "v_permlane32_swap_b32 v18, v19\n"
    "v_max_f32 v18, v18, v19\n"
    "v_mov_b32 v19, v18\n"
    "v_mul_f32 v211, s75, v211\n"
    "v_mul_f32 v212, s75, v212\n"
    "v_mul_f32 v213, s75, v213\n"
    "v_mfma_f32_16x16x32_bf16 v[220:223], a[60:63], a[112:115], v[220:223]\n"
    "v_permlane16_swap_b32 v18, v19\n"
    "v_min_f32_e64 v18, -v18, -v19\n"
    "v_accvgpr_write_b32 a208, v18\n"
    "v_mul_f32 v214, s75, v214\n"
    "v_mul_f32 v215, s75, v215\n"
    "v_cvt_pk_bf16_f32 v120, v208, v209\n"
    "v_mfma_f32_16x16x32_bf16 v[224:227], a[0:3], a[88:91], v[224:227]\n"
    "v_cvt_pk_bf16_f32 v121, v210, v211\n"
    "v_cvt_pk_bf16_f32 v122, v212, v213\n"
    "v_cvt_pk_bf16_f32 v123, v214, v215\n"
    "v_accvgpr_read_b32 v160, a208\n"
    "v_accvgpr_read_b32 v161, a208\n"
    "v_mfma_f32_16x16x32_bf16 v[224:227], a[32:35], a[116:119], v[224:227]\n"
    "v_mul_f32 v216, s75, v216\n"
    "v_mul_f32 v217, s75, v217\n"
    "v_mul_f32 v218, s75, v218\n"
    "v_accvgpr_read_b32 v162, a208\n"
    "v_accvgpr_read_b32 v163, a208\n"
    "v_mfma_f32_16x16x32_bf16 v[228:231], a[4:7], a[88:91], v[228:231]\n"
    "v_mul_f32 v219, s75, v219\n"
    "v_mul_f32 v220, s75, v220\n"
    "v_mul_f32 v221, s75, v221\n"
    "v_mfma_f32_16x16x32_bf16 v[228:231], a[36:39], a[116:119], v[228:231]\n"
    "v_mul_f32 v222, s75, v222\n"
    "v_mul_f32 v223, s75, v223\n"
    "v_cvt_pk_bf16_f32 v124, v216, v217\n"
    "v_mfma_f32_16x16x32_bf16 v[232:235], a[8:11], a[88:91], v[232:235]\n"
    "buffer_load_dwordx4 v7, s[48:51], s72 offen offset:2048 nt lds\n"
    "v_mfma_f32_16x16x32_bf16 v[232:235], a[40:43], a[116:119], v[232:235]\n"
    "v_cvt_pk_bf16_f32 v125, v218, v219\n"
    "v_cvt_pk_bf16_f32 v126, v220, v221\n"
    "v_cvt_pk_bf16_f32 v127, v222, v223\n"
    "v_mul_f32 v224, s75, v224\n"
    "v_mul_f32 v225, s75, v225\n"
    "v_mul_f32 v226, s75, v226\n"
    "v_mfma_f32_16x16x32_bf16 v[236:239], a[12:15], a[88:91], v[236:239]\n"
    "v_mfma_f32_16x16x32_bf16 v[12:15], a[224:227], v[112:115], 0\n"
    "v_mul_f32 v227, s75, v227\n"
    "v_mul_f32 v228, s75, v228\n"
    "v_mul_f32 v229, s75, v229\n"
    "v_mfma_f32_16x16x32_bf16 v[236:239], a[44:47], a[116:119], v[236:239]\n"
    "v_mfma_f32_16x16x32_bf16 v[12:15], a[228:231], v[116:119], v[12:15]\n"
    "v_mul_f32 v230, s75, v230\n"
    "v_mul_f32 v231, s75, v231\n"
    "v_cvt_pk_bf16_f32 v128, v224, v225\n"
    "v_mfma_f32_16x16x32_bf16 v[240:243], a[16:19], a[88:91], v[240:243]\n"
    "v_mfma_f32_16x16x32_bf16 v[12:15], a[232:235], v[120:123], v[12:15]\n"
    "v_cvt_pk_bf16_f32 v129, v226, v227\n"
    "v_cvt_pk_bf16_f32 v130, v228, v229\n"
    "v_cvt_pk_bf16_f32 v131, v230, v231\n"
    "v_mfma_f32_16x16x32_bf16 v[240:243], a[48:51], a[116:119], v[240:243]\n"
    "v_mfma_f32_16x16x32_bf16 v[12:15], a[236:239], v[124:127], v[12:15]\n"
    "v_mul_f32 v232, s75, v232\n"
    "v_mul_f32 v233, s75, v233\n"
    "v_mul_f32 v234, s75, v234\n"
    "v_mfma_f32_16x16x32_bf16 v[244:247], a[20:23], a[88:91], v[244:247]\n"
    "v_mul_f32 v235, s75, v235\n"
    "v_mul_f32 v236, s75, v236\n"
    "v_mul_f32 v237, s75, v237\n"
    "v_mfma_f32_16x16x32_bf16 v[244:247], a[52:55], a[116:119], v[244:247]\n"
    "v_mul_f32 v238, s75, v238\n"
    "v_mul_f32 v239, s75, v239\n"
    "v_cvt_pk_bf16_f32 v132, v232, v233\n"
    "v_mfma_f32_16x16x32_bf16 v[248:251], a[24:27], a[88:91], v[248:251]\n"
    "buffer_load_dwordx4 v8, s[48:51], s72 offen offset:3072 lds\n"
    "v_mfma_f32_16x16x32_bf16 v[248:251], a[56:59], a[116:119], v[248:251]\n"
    "s_add_u32 s72, s72, 4096\n"
    "v_max3_f32 v18, v12, v13, v14\n"
    "v_max_f32 v18, v18, v15\n"
    "v_mov_b32 v19, v18\n"
    "v_cvt_pk_bf16_f32 v133, v234, v235\n"
    "v_cvt_pk_bf16_f32 v134, v236, v237\n"
    "v_cvt_pk_bf16_f32 v135, v238, v239\n"
    "v_mul_f32 v240, s75, v240\n"
    "v_mul_f32 v241, s75, v241\n"
    "v_mul_f32 v242, s75, v242\n"
    "v_mfma_f32_16x16x32_bf16 v[252:255], a[28:31], a[88:91], v[252:255]\n"
    "s_and_b32 s72, s72, 131071\n"
    "v_permlane32_swap_b32 v18, v19\n"
    "v_max_f32 v18, v18, v19\n"
    "v_mov_b32 v19, v18\n"
    "v_mul_f32 v243, s75, v243\n"
    "v_mul_f32 v244, s75, v244\n"
    "v_mul_f32 v245, s75, v245\n"
    "v_mfma_f32_16x16x32_bf16 v[252:255], a[60:63], a[116:119], v[252:255]\n"
    "s_add_u32 s73, s73, 4096\n"
    "v_permlane16_swap_b32 v18, v19\n"
    "v_min_f32_e64 v18, -v18, -v19\n"
    "v_accvgpr_write_b32 a209, v18\n"
    "v_mul_f32 v246, s75, v246\n"
    "v_mul_f32 v247, s75, v247\n"
    "v_cvt_pk_bf16_f32 v136, v240, v241\n"
    "s_nop 1\n"
    "s_sub_u32 s74, s73, 40960\n"
    "v_cvt_pk_bf16_f32 v137, v242, v243\n"
    "v_cvt_pk_bf16_f32 v138, v244, v245\n"
    "v_cvt_pk_bf16_f32 v139, v246, v247\n"
    "v_accvgpr_read_b32 v164, a209\n"
    "v_accvgpr_read_b32 v165, a209\n"
    "s_nop 1\n"
    "s_min_u32 s73, s73, s74\n"
    "v_mul_f32 v248, s75, v248\n"
    "v_mul_f32 v249, s75, v249\n"
    "v_mul_f32 v250, s75, v250\n"
    "v_accvgpr_read_b32 v166, a209\n"
    "v_accvgpr_read_b32 v167, a209\n"
    "v_mul_f32 v251, s75, v251\n"
    "v_mul_f32 v252, s75, v252\n"
    "v_mul_f32 v253, s75, v253\n"
    "v_mul_f32 v254, s75, v254\n"
    "v_mul_f32 v255, s75, v255\n"
    "v_cvt_pk_bf16_f32 v140, v248, v249\n"
    "v_cvt_pk_bf16_f32 v141, v250, v251\n"
    "v_cvt_pk_bf16_f32 v142, v252, v253\n"
    "v_cvt_pk_bf16_f32 v143, v254, v255\n"
    "s_nop 1\n"
    "v_mfma_f32_16x16x32_bf16 v[12:15], a[224:227], v[128:131], 0\n"
    "v_mfma_f32_16x16x32_bf16 v[12:15], a[228:231], v[132:135], v[12:15]\n"
    "v_mfma_f32_16x16x32_bf16 v[12:15], a[232:235], v[136:139], v[12:15]\n"
    "v_mfma_f32_16x16x32_bf16 v[12:15], a[236:239], v[140:143], v[12:15]\n"
    "v_add_u32 v228, 147456, v4\n"
    "s_add_u32 s72, s72, 8192\n"
    "s_and_b32 s72, s72, 131071\n"
    "s_mov_b32 s73, 0\n"
    "s_nop 3\n"
    "v_max3_f32 v18, v12, v13, v14\n"
    "v_max_f32 v18, v18, v15\n"
    "v_mov_b32 v19, v18\n"
    "s_nop 1\n"
    "v_permlane32_swap_b32 v18, v19\n"
    "v_max_f32 v18, v18, v19\n"
    "v_mov_b32 v19, v18\n"
    "s_nop 1\n"
    "v_permlane16_swap_b32 v18, v19\n"
    "v_min_f32_e64 v18, -v18, -v19\n"
    "v_accvgpr_write_b32 a210, v18\n"
    "v_mov_b32 v168, v18\n"
    "v_mov_b32 v169, v18\n"
    "v_mov_b32 v170, v18\n"
    "v_mov_b32 v171, v18\n"
    "v_mov_b32 v18, v228\n"
    "LATT_%=:\n"
    "s_mov_b32 s76, 3\n"
    "s_waitcnt lgkmcnt(0)\n"
    "v_mfma_f32_16x16x32_bf16 v[204:207], a[224:227], v[32:35], v[144:147]\n"
    "v_mfma_f32_16x16x32_bf16 v[204:207], a[228:231], v[36:39], v[204:207]\n"
    "v_mfma_f32_16x16x32_bf16 v[204:207], a[232:235], v[40:43], v[204:207]\n"
    "v_mfma_f32_16x16x32_bf16 v[204:207], a[236:239], v[44:47], v[204:207]\n"
    "v_lshlrev_b32 v228, 3, v0\n"
    "v_min_u32 v229, 4480, v228\n"
    "v_min_u32 v228, 896, v228\n"
    "v_add_u32 v228, LE_%=-LBEG_%=, v228\n"
    "v_accvgpr_write_b32 a48, 0\n"
    "v_accvgpr_write_b32 a49, 0\n"
    "v_accvgpr_write_b32 a50, 0\n"
    "v_accvgpr_write_b32 a51, 0\n"
    "v_accvgpr_write_b32 a52, 0\n"
    "v_accvgpr_write_b32 a53, 0\n"
    "v_accvgpr_write_b32 a54, 0\n"
    "v_accvgpr_write_b32 a55, 0\n"
    "v_accvgpr_write_b32 a104, 0\n"
    "v_accvgpr_write_b32 a105, 0\n"
    "v_accvgpr_write_b32 a106, 0\n"
    "v_exp_f32 v204, v204\n"
    "v_accvgpr_write_b32 a107, 0\n"
    "v_accvgpr_write_b32 a108, 0\n"
    "v_accvgpr_write_b32 a109, 0\n"
    "v_accvgpr_write_b32 a110, 0\n"
    "v_accvgpr_write_b32 a111, 0\n"
    "v_mov_b32 v220, v204\n"
    "v_mfma_f32_16x16x32_bf16 v[208:211], a[240:243], v[32:35], v[144:147]\n"
    "v_exp_f32 v205, v205\n"
    "v_mfma_f32_16x16x32_bf16 v[208:211], a[244:247], v[36:39], v[208:211]\n"
    "v_cvt_pk_bf16_f32 v212, v204, v205\n"
    "v_add_f32 v220, v220, v205\n"
    "v_mfma_f32_16x16x32_bf16 v[208:211], a[248:251], v[40:43], v[208:211]\n"
    "v_exp_f32 v206, v206\n"
    "v_mfma_f32_16x16x32_bf16 v[208:211], a[252:255], v[44:47], v[208:211]\n"
    "v_add_f32 v220, v220, v206\n"
    "v_accvgpr_write_b32 a160, 0\n"
    "v_accvgpr_write_b32 a161, 0\n"
    "v_accvgpr_write_b32 a162, 0\n"
    "v_accvgpr_write_b32 a163, 0\n"
    "v_accvgpr_write_b32 a164, 0\n"
    "ds_read_b64_tr_b16 v[188:189], v28 offset:0\n"
    "v_exp_f32 v207, v207\n"
    "v_accvgpr_write_b32 a165, 0\n"
    "v_accvgpr_write_b32 a166, 0\n"
    "v_accvgpr_write_b32 a167, 0\n"
    "v_accvgpr_write_b32 a216, 0\n"
    "v_accvgpr_write_b32 a217, 0\n"
    "v_cvt_pk_bf16_f32 v213, v206, v207\n"
    "v_add_f32 v220, v220, v207\n"
    "v_accvgpr_write_b32 a218, 0\n"
    "v_accvgpr_write_b32 a219, 0\n"
    "v_accvgpr_write_b32 a220, 0\n"
    "v_accvgpr_write_b32 a221, 0\n"
    "v_accvgpr_write_b32 a222, 0\n"
    "ds_read_b64_tr_b16 v[190:191], v28 offset:4096\n"
    "v_exp_f32 v208, v208\n"
    "v_accvgpr_write_b32 a223, 0\n"
    "v_accvgpr_write_b32 a40, 0\n"
    "v_accvgpr_write_b32 a41, 0\n"
    "v_accvgpr_write_b32 a42, 0\n"
    "v_accvgpr_write_b32 a43, 0\n"
    "ds_read_b64_tr_b16 v[192:193], v29 offset:0\n"
    "v_add_f32 v220, v220, v208\n"
    "v_mfma_f32_16x16x32_bf16 v[204:207], a[224:227], v[48:51], v[148:151]\n"
    "ds_read_b64_tr_b16 v[194:195], v29 offset:4096\n"
    "v_exp_f32 v209, v209\n"
    "v_mfma_f32_16x16x32_bf16 v[204:207], a[228:231], v[52:55], v[204:207]\n"
    "v_cvt_pk_bf16_f32 v214, v208, v209\n"
    "v_add_f32 v220, v220, v209\n"
    "v_mfma_f32_16x16x32_bf16 v[204:207], a[232:235], v[56:59], v[204:207]\n"
    "ds_read_b64_tr_b16 v[196:197], v30 offset:0\n"
    "v_exp_f32 v210, v210\n"
    "v_mfma_f32_16x16x32_bf16 v[204:207], a[236:239], v[60:63], v[204:207]\n"
    "ds_read_b64_tr_b16 v[198:199], v30 offset:4096\n"
    "v_add_f32 v220, v220, v210\n"
    "v_accvgpr_write_b32 a44, 0\n"
    "v_accvgpr_write_b32 a45, 0\n"
    "v_accvgpr_write_b32 a46, 0\n"
    "v_accvgpr_write_b32 a47, 0\n"
    "v_accvgpr_write_b32 a96, 0\n"
    "ds_read_b64_tr_b16 v[200:201], v31 offset:0\n"
    "v_exp_f32 v211, v211\n"
    "v_accvgpr_write_b32 a97, 0\n"
    "v_accvgpr_write_b32 a98, 0\n"
    "v_accvgpr_write_b32 a99, 0\n"
    "v_accvgpr_write_b32 a100, 0\n"
    "v_accvgpr_write_b32 a101, 0\n"
    "v_cvt_pk_bf16_f32 v215, v210, v211\n"
    "v_add_f32 v220, v220, v211\n"
    "v_accvgpr_write_b32 a102, 0\n"
    "v_accvgpr_write_b32 a103, 0\n"
    "ds_read_b64_tr_b16 v[202:203], v31 offset:4096\n"
    "v_exp_f32 v204, v204\n"
    "s_nop 2\n"
    "ds_read_b64_tr_b16 v[172:173], v24 offset:0\n"
    "v_mov_b32 v221, v204\n"
    "v_mfma_f32_16x16x32_bf16 v[208:211], a[240:243], v[48:51], v[148:151]\n"
    "ds_read_b64_tr_b16 v[174:175], v24 offset:4096\n"
    "v_exp_f32 v205, v205\n"
    "v_mfma_f32_16x16x32_bf16 v[208:211], a[244:247], v[52:55], v[208:211]\n"
    "v_cvt_pk_bf16_f32 v216, v204, v205\n"
    "v_add_f32 v221, v221, v205\n"
    "v_mfma_f32_16x16x32_bf16 v[208:211], a[248:251], v[56:59], v[208:211]\n"
    "ds_read_b64_tr_b16 v[176:177], v25 offset:0\n"
    "v_exp_f32 v206, v206\n"
    "v_mfma_f32_16x16x32_bf16 v[208:211], a[252:255], v[60:63], v[208:211]\n"
    "s_waitcnt lgkmcnt(9)\n"
    "ds_read_b64_tr_b16 v[178:179], v25 offset:4096\n"
    "v_add_f32 v221, v221, v206\n"
    "v_mfma_f32_16x16x32_bf16 a[112:115], v[188:191], v[212:215], 0\n"
    "s_waitcnt lgkmcnt(8)\n"
    "ds_read_b64_tr_b16 v[180:181], v26 offset:0\n"
    "v_exp_f32 v207, v207\n"
    "v_mfma_f32_16x16x32_bf16 a[116:119], v[192:195], v[212:215], 0\n"
    "s_waitcnt lgkmcnt(7)\n"
    "v_cvt_pk_bf16_f32 v217, v206, v207\n"
    "v_add_f32 v221, v221, v207\n"
    "v_mfma_f32_16x16x32_bf16 a[168:171], v[196:199], v[212:215], 0\n"
    "s_waitcnt lgkmcnt(5)\n"
    "ds_read_b64_tr_b16 v[182:183], v26 offset:4096\n"
    "v_exp_f32 v208, v208\n"
    "v_mfma_f32_16x16x32_bf16 a[172:175], v[200:203], v[212:215], 0\n"
    "ds_read_b64_tr_b16 v[184:185], v27 offset:0\n"
    "v_add_f32 v221, v221, v208\n"
    "v_mfma_f32_16x16x32_bf16 v[204:207], a[224:227], v[64:67], v[152:155]\n"
    "ds_read_b64_tr_b16 v[186:187], v27 offset:4096\n"
    "v_exp_f32 v209, v209\n"
    "v_mfma_f32_16x16x32_bf16 v[204:207], a[228:231], v[68:71], v[204:207]\n"
    "v_cvt_pk_bf16_f32 v218, v208, v209\n"
    "v_add_f32 v221, v221, v209\n"
    "v_mfma_f32_16x16x32_bf16 v[204:207], a[232:235], v[72:75], v[204:207]\n"
    "s_memrealtime s[36:37]\n"
    "v_exp_f32 v210, v210\n"
    "v_mfma_f32_16x16x32_bf16 v[204:207], a[236:239], v[76:79], v[204:207]\n"
    "s_waitcnt lgkmcnt(6)\n"
    "v_add_f32 v221, v221, v210\n"
    "v_mfma_f32_16x16x32_bf16 a[0:3], v[172:175], v[212:215], 0\n"
    "s_waitcnt lgkmcnt(4)\n"
    "v_exp_f32 v211, v211\n"
    "v_mfma_f32_16x16x32_bf16 a[4:7], v[176:179], v[212:215], 0\n"
    "s_waitcnt lgkmcnt(2)\n"
    "v_cvt_pk_bf16_f32 v219, v210, v211\n"
    "v_add_f32 v221, v221, v211\n"
    "v_mfma_f32_16x16x32_bf16 a[56:59], v[180:183], v[212:215], 0\n"
    "s_waitcnt lgkmcnt(0)\n"
    "v_exp_f32 v204, v204\n"
    "v_mfma_f32_16x16x32_bf16 a[60:63], v[184:187], v[212:215], 0\n"
    "s_barrier\n"
    "v_mov_b32 v222, v204\n"
    "v_mfma_f32_16x16x32_bf16 v[208:211], a[240:243], v[64:67], v[152:155]\n"
    "v_exp_f32 v205, v205\n"
    "v_mfma_f32_16x16x32_bf16 v[208:211], a[244:247], v[68:71], v[208:211]\n"
    "v_cvt_pk_bf16_f32 v212, v204, v205\n"
    "v_add_f32 v222, v222, v205\n"
    "v_mfma_f32_16x16x32_bf16 v[208:211], a[248:251], v[72:75], v[208:211]\n"
    "s_add_u32 m0, s60, s73\n"
    "v_exp_f32 v206, v206\n"
    "v_mfma_f32_16x16x32_bf16 v[208:211], a[252:255], v[76:79], v[208:211]\n"
    "buffer_load_dwordx4 v5, s[48:51], s72 offen offset:0 nt lds\n"
    "v_mfma_f32_16x16x32_bf16 a[120:123], v[188:191], v[216:219], 0\n"
    "s_mov_b32 s77, s36\n"
    "v_exp_f32 v207, v207\n"
    "v_mfma_f32_16x16x32_bf16 a[124:127], v[192:195], v[216:219], 0\n"
    "s_mov_b64 vcc, 0\n"
    "v_cvt_pk_bf16_f32 v213, v206, v207\n"
    "v_add_f32 v222, v222, v207\n"
    "v_mfma_f32_16x16x32_bf16 a[176:179], v[196:199], v[216:219], 0\n"
    "v_exp_f32 v208, v208\n"
    "v_mfma_f32_16x16x32_bf16 a[180:183], v[200:203], v[216:219], 0\n"
    "v_add_f32 v222, v222, v206\n"
    "v_add_f32 v222, v222, v208\n"
    "v_mfma_f32_16x16x32_bf16 v[204:207], a[224:227], v[80:83], v[156:159]\n"
    "v_exp_f32 v209, v209\n"
    "v_mfma_f32_16x16x32_bf16 v[204:207], a[228:231], v[84:87], v[204:207]\n"
    "v_cvt_pk_bf16_f32 v214, v208, v209\n"
    "v_add_f32 v222, v222, v209\n"
    "v_mfma_f32_16x16x32_bf16 v[204:207], a[232:235], v[88:91], v[204:207]\n"
    "v_exp_f32 v210, v210\n"
    "v_mfma_f32_16x16x32_bf16 v[204:207], a[236:239], v[92:95], v[204:207]\n"
    "buffer_load_dwordx4 v6, s[48:51], s72 offen offset:1024 nt lds\n"
    "v_mfma_f32_16x16x32_bf16 a[8:11], v[172:175], v[216:219], 0\n"
    "v_exp_f32 v211, v211\n"
    "v_mfma_f32_16x16x32_bf16 a[12:15], v[176:179], v[216:219], 0\n"
    "v_cvt_pk_bf16_f32 v215, v210, v211\n"
    "v_add_f32 v222, v222, v211\n"
    "v_mfma_f32_16x16x32_bf16 a[64:67], v[180:183], v[216:219], 0\n"
    "v_exp_f32 v204, v204\n"
    "v_mfma_f32_16x16x32_bf16 a[68:71], v[184:187], v[216:219], 0\n"
    "v_add_f32 v222, v222, v210\n"
    "v_mov_b32 v223, v204\n"
    "v_mfma_f32_16x16x32_bf16 v[208:211], a[240:243], v[80:83], v[156:159]\n"
    "v_exp_f32 v205, v205\n"
    "v_mfma_f32_16x16x32_bf16 v[208:211], a[244:247], v[84:87], v[208:211]\n"
    "v_cvt_pk_bf16_f32 v216, v204, v205\n"
    "v_add_f32 v223, v223, v205\n"
    "v_mfma_f32_16x16x32_bf16 v[208:211], a[248:251], v[88:91], v[208:211]\n"
    "v_exp_f32 v206, v206\n"
    "v_mfma_f32_16x16x32_bf16 v[208:211], a[252:255], v[92:95], v[208:211]\n"
    "buffer_load_dwordx4 v7, s[48:51], s72 offen offset:2048 nt lds\n"
    "v_mfma_f32_16x16x32_bf16 a[128:131], v[188:191], v[212:215], 0\n"
    "v_exp_f32 v207, v207\n"
    "v_mfma_f32_16x16x32_bf16 a[132:135], v[192:195], v[212:215], 0\n"
    "v_cvt_pk_bf16_f32 v217, v206, v207\n"
    "v_add_f32 v223, v223, v207\n"
    "v_mfma_f32_16x16x32_bf16 a[184:187], v[196:199], v[212:215], 0\n"
    "v_exp_f32 v208, v208\n"
    "v_mfma_f32_16x16x32_bf16 a[188:191], v[200:203], v[212:215], 0\n"
    "v_add_f32 v223, v223, v206\n"
    "v_add_f32 v223, v223, v208\n"
    "v_mfma_f32_16x16x32_bf16 v[204:207], a[224:227], v[96:99], v[160:163]\n"
    "v_exp_f32 v209, v209\n"
    "v_mfma_f32_16x16x32_bf16 v[204:207], a[228:231], v[100:103], v[204:207]\n"
    "v_cvt_pk_bf16_f32 v218, v208, v209\n"
    "v_add_f32 v223, v223, v209\n"
    "v_mfma_f32_16x16x32_bf16 v[204:207], a[232:235], v[104:107], v[204:207]\n"
    "v_exp_f32 v210, v210\n"
    "v_mfma_f32_16x16x32_bf16 v[204:207], a[236:239], v[108:111], v[204:207]\n"
    "buffer_load_dwordx4 v8, s[48:51], s72 offen offset:3072 lds\n"
    "v_mfma_f32_16x16x32_bf16 a[16:19], v[172:175], v[212:215], 0\n"
    "s_add_u32 s72, s72, 4096\n"
    "v_exp_f32 v211, v211\n"
    "v_mfma_f32_16x16x32_bf16 a[20:23], v[176:179], v[212:215], 0\n"
    "s_and_b32 s72, s72, 131071\n"
    "v_cvt_pk_bf16_f32 v219, v210, v211\n"
    "v_add_f32 v223, v223, v211\n"
    "v_mfma_f32_16x16x32_bf16 a[72:75], v[180:183], v[212:215], 0\n"
    "s_add_u32 s73, s73, 4096\n"
    "v_exp_f32 v204, v204\n"
    "v_mfma_f32_16x16x32_bf16 a[76:79], v[184:187], v[212:215], 0\n"
    "s_sub_u32 s74, s73, 40960\n"
    "v_add_f32 v223, v223, v210\n"
    "v_mov_b32 v224, v204\n"
    "v_mfma_f32_16x16x32_bf16 v[208:211], a[240:243], v[96:99], v[160:163]\n"
    "s_min_u32 s73, s73, s74\n"
    "v_exp_f32 v205, v205\n"
    "v_mfma_f32_16x16x32_bf16 v[208:211], a[244:247], v[100:103], v[208:211]\n"
    "v_cvt_pk_bf16_f32 v212, v204, v205\n"
    "v_add_f32 v224, v224, v205\n"
    "v_mfma_f32_16x16x32_bf16 v[208:211], a[248:251], v[104:107], v[208:211]\n"
    "s_add_u32 m0, s60, s73\n"
    "v_exp_f32 v206, v206\n"
    "v_mfma_f32_16x16x32_bf16 v[208:211], a[252:255], v[108:111], v[208:211]\n"
    "buffer_load_dwordx4 v5, s[48:51], s72 offen offset:0 nt lds\n"
    "v_mfma_f32_16x16x32_bf16 a[136:139], v[188:191], v[216:219], 0\n"
    "v_exp_f32 v207, v207\n"
    "v_mfma_f32_16x16x32_bf16 a[140:143], v[192:195], v[216:219], 0\n"
    "v_cvt_pk_bf16_f32 v213, v206, v207\n"
    "v_add_f32 v224, v224, v207\n"
    "v_mfma_f32_16x16x32_bf16 a[192:195], v[196:199], v[216:219], 0\n"
    "v_exp_f32 v208, v208\n"
    "v_mfma_f32_16x16x32_bf16 a[196:199], v[200:203], v[216:219], 0\n"
    "v_add_f32 v224, v224, v206\n"
    "v_add_f32 v224, v224, v208\n"
    "v_mfma_f32_16x16x32_bf16 v[204:207], a[224:227], v[112:115], v[164:167]\n"
    "v_exp_f32 v209, v209\n"
    "v_mfma_f32_16x16x32_bf16 v[204:207], a[228:231], v[116:119], v[204:207]\n"
    "v_cvt_pk_bf16_f32 v214, v208, v209\n"
    "v_add_f32 v224, v224, v209\n"
    "v_mfma_f32_16x16x32_bf16 v[204:207], a[232:235], v[120:123], v[204:207]\n"
    "v_exp_f32 v210, v210\n"
    "v_mfma_f32_16x16x32_bf16 v[204:207], a[236:239], v[124:127], v[204:207]\n"
    "buffer_load_dwordx4 v6, s[48:51], s72 offen offset:1024 nt lds\n"
    "v_mfma_f32_16x16x32_bf16 a[24:27], v[172:175], v[216:219], 0\n"
    "v_exp_f32 v211, v211\n"
    "v_mfma_f32_16x16x32_bf16 a[28:31], v[176:179], v[216:219], 0\n"
    "v_cvt_pk_bf16_f32 v215, v210, v211\n"
    "v_add_f32 v224, v224, v211\n"
    "v_mfma_f32_16x16x32_bf16 a[80:83], v[180:183], v[216:219], 0\n"
    "v_exp_f32 v204, v204\n"
    "v_mfma_f32_16x16x32_bf16 a[84:87], v[184:187], v[216:219], 0\n"
    "v_add_f32 v224, v224, v210\n"
    "v_mov_b32 v225, v204\n"
    "v_mfma_f32_16x16x32_bf16 v[208:211], a[240:243], v[112:115], v[164:167]\n"
    "v_exp_f32 v205, v205\n"
    "v_mfma_f32_16x16x32_bf16 v[208:211], a[244:247], v[116:119], v[208:211]\n"
    "v_cvt_pk_bf16_f32 v216, v204, v205\n"
    "v_add_f32 v225, v225, v205\n"
    "v_mfma_f32_16x16x32_bf16 v[208:211], a[248:251], v[120:123], v[208:211]\n"
    "v_exp_f32 v206, v206\n"
    "v_mfma_f32_16x16x32_bf16 v[208:211], a[252:255], v[124:127], v[208:211]\n"
    "s_barrier\n"
    "v_mfma_f32_16x16x32_bf16 a[144:147], v[188:191], v[212:215], 0\n"
    "v_exp_f32 v207, v207\n"
    "v_mfma_f32_16x16x32_bf16 a[148:151], v[192:195], v[212:215], 0\n"
    "v_cvt_pk_bf16_f32 v217, v206, v207\n"
    "v_add_f32 v225, v225, v207\n"
    "v_mfma_f32_16x16x32_bf16 a[200:203], v[196:199], v[212:215], 0\n"
    "v_exp_f32 v208, v208\n"
    "v_mfma_f32_16x16x32_bf16 a[204:207], v[200:203], v[212:215], 0\n"
    "v_add_f32 v225, v225, v206\n"
    "v_add_f32 v225, v225, v208\n"
    "v_mfma_f32_16x16x32_bf16 v[204:207], a[224:227], v[128:131], v[168:171]\n"
    "v_exp_f32 v209, v209\n"
    "v_mfma_f32_16x16x32_bf16 v[204:207], a[228:231], v[132:135], v[204:207]\n"
    "v_cvt_pk_bf16_f32 v218, v208, v209\n"
    "v_add_f32 v225, v225, v209\n"
    "v_mfma_f32_16x16x32_bf16 v[204:207], a[232:235], v[136:139], v[204:207]\n"
    "s_sub_u32 s29, s36, s38\n"
    "v_exp_f32 v210, v210\n"
    "v_mfma_f32_16x16x32_bf16 v[204:207], a[236:239], v[140:143], v[204:207]\n"
    "s_sub_u32 s29, s29, 1462\n"
    "ds_read_b128 a[224:227], v20 offset:8192\n"
    "v_add_f32 v225, v225, v210\n"
    "v_mfma_f32_16x16x32_bf16 a[32:35], v[172:175], v[212:215], 0\n"
    "s_mul_i32 s29, s29, 1\n"
    "ds_read_b128 a[228:231], v21 offset:8192\n"
    "v_exp_f32 v211, v211\n"
    "v_mfma_f32_16x16x32_bf16 a[36:39], v[176:179], v[212:215], 0\n"
    "s_ashr_i32 s29, s29, 4\n"
    "v_cvt_pk_bf16_f32 v219, v210, v211\n"
    "v_add_f32 v225, v225, v211\n"
    "v_mfma_f32_16x16x32_bf16 a[88:91], v[180:183], v[212:215], 0\n"
    "s_min_i32 s29, s29, 8\n"
    "ds_read_b128 a[232:235], v22 offset:8192\n"
    "v_exp_f32 v204, v204\n"
    "v_mfma_f32_16x16x32_bf16 a[92:95], v[184:187], v[212:215], 0\n"
    "s_max_i32 s29, s29, -8\n"
    "ds_read_b128 a[236:239], v23 offset:8192\n"
    "v_mov_b32 v226, v204\n"
    "v_mfma_f32_16x16x32_bf16 v[208:211], a[240:243], v[128:131], v[168:171]\n"
    "s_sub_u32 s29, 118, s29\n"
    "v_exp_f32 v205, v205\n"
    "v_mfma_f32_16x16x32_bf16 v[208:211], a[244:247], v[132:135], v[208:211]\n"
    "buffer_load_dwordx4 v7, s[48:51], s72 offen offset:2048 nt lds\n"
    "v_cvt_pk_bf16_f32 v12, v204, v205\n"
    "v_add_f32 v226, v226, v205\n"
    "v_mfma_f32_16x16x32_bf16 v[208:211], a[248:251], v[136:139], v[208:211]\n"
    "v_exp_f32 v206, v206\n"
    "v_mfma_f32_16x16x32_bf16 v[208:211], a[252:255], v[140:143], v[208:211]\n"
    "ds_read_b128 a[240:243], v20 offset:12288\n"
    "v_add_f32 v226, v226, v206\n"
    "v_mfma_f32_16x16x32_bf16 a[152:155], v[188:191], v[216:219], 0\n"
    "ds_read_b128 a[244:247], v21 offset:12288\n"
    "v_exp_f32 v207, v207\n"
    "v_mfma_f32_16x16x32_bf16 a[156:159], v[192:195], v[216:219], 0\n"
    "v_cvt_pk_bf16_f32 v13, v206, v207\n"
    "v_add_f32 v226, v226, v207\n"
    "v_mfma_f32_16x16x32_bf16 a[208:211], v[196:199], v[216:219], 0\n"
    "v_exp_f32 v208, v208\n"
    "v_mfma_f32_16x16x32_bf16 a[212:215], v[200:203], v[216:219], 0\n"
    "s_waitcnt lgkmcnt(5)\n"
    "ds_read_b128 a[248:251], v22 offset:12288\n"
    "v_add_f32 v226, v226, v208\n"
    "LA_%=:\n"
    "v_mfma_f32_16x16x32_bf16 v[204:207], a[224:227], v[32:35], v[144:147]\n"
    "s_waitcnt lgkmcnt(5)\n"
    "v_exp_f32 v209, v209\n"
    "v_mfma_f32_16x16x32_bf16 v[204:207], a[228:231], v[36:39], v[204:207]\n"
    "s_waitcnt lgkmcnt(4)\n"
    "v_cvt_pk_bf16_f32 v14, v208, v209\n"
    "v_add_f32 v226, v226, v209\n"
    "v_mfma_f32_16x16x32_bf16 v[204:207], a[232:235], v[40:43], v[204:207]\n"
    "s_waitcnt lgkmcnt(3)\n"
    "v_exp_f32 v210, v210\n"
    "v_mfma_f32_16x16x32_bf16 v[204:207], a[236:239], v[44:47], v[204:207]\n"
    "s_cmp_eq_u32 s76, 1\n"
    "ds_read_b128 a[252:255], v23 offset:12288\n"
    "v_add_f32 v226, v226, v210\n"
    "v_mfma_f32_16x16x32_bf16 a[40:43], v[172:175], v[216:219], a[40:43]\n"
    "s_cselect_b32 s71, s46, 1073741824\n"
    "v_exp_f32 v211, v211\n"
    "v_mfma_f32_16x16x32_bf16 a[44:47], v[176:179], v[216:219], a[44:47]\n"
    "v_cvt_pk_bf16_f32 v15, v210, v211\n"
    "v_add_f32 v226, v226, v211\n"
    "s_cmp_eq_u32 s76, 3\n"
    "s_cselect_b32 s78, 5, 0\n"
    "s_add_u32 s78, s78, s29\n"
    "v_mfma_f32_16x16x32_bf16 a[96:99], v[180:183], v[216:219], a[96:99]\n"
    "v_exp_f32 v204, v204\n"
    "v_mfma_f32_16x16x32_bf16 a[100:103], v[184:187], v[216:219], a[100:103]\n"
    "s_waitcnt lgkmcnt(3)\n"
    "v_add_f32 v220, v220, v204\n"
    "v_mfma_f32_16x16x32_bf16 v[208:211], a[240:243], v[32:35], v[144:147]\n"
    "s_waitcnt lgkmcnt(2)\n"
    "v_exp_f32 v205, v205\n"
    "v_mfma_f32_16x16x32_bf16 v[208:211], a[244:247], v[36:39], v[208:211]\n"
    "s_waitcnt lgkmcnt(1)\n"
    "v_cvt_pk_bf16_f32 v212, v204, v205\n"
    "v_add_f32 v220, v220, v205\n"
    "v_mfma_f32_16x16x32_bf16 v[208:211], a[248:251], v[40:43], v[208:211]\n"
    "s_waitcnt lgkmcnt(0)\n"
    "v_exp_f32 v206, v206\n"
    "v_mfma_f32_16x16x32_bf16 v[208:211], a[252:255], v[44:47], v[208:211]\n"
    "v_add_f32 v220, v220, v206\n"
    "v_mfma_f32_16x16x32_bf16 a[160:163], v[188:191], v[12:15], a[160:163]\n"
    "ds_read_b64_tr_b16 v[188:189], v28 offset:8192\n"
    "v_exp_f32 v207, v207\n"
    "v_mfma_f32_16x16x32_bf16 a[164:167], v[192:195], v[12:15], a[164:167]\n"
    "v_cvt_pk_bf16_f32 v213, v206, v207\n"
    "v_add_f32 v220, v220, v207\n"
    "v_mfma_f32_16x16x32_bf16 a[216:219], v[196:199], v[12:15], a[216:219]\n"
    "ds_read_b64_tr_b16 v[190:191], v28 offset:12288\n"
    "v_exp_f32 v208, v208\n"
    "v_mfma_f32_16x16x32_bf16 a[220:223], v[200:203], v[12:15], a[220:223]\n"
    "ds_read_b64_tr_b16 v[192:193], v29 offset:8192\n"
    "v_add_f32 v220, v220, v208\n"
    "v_mfma_f32_16x16x32_bf16 v[204:207], a[224:227], v[48:51], v[148:151]\n"
    "ds_read_b64_tr_b16 v[194:195], v29 offset:12288\n"
    "v_exp_f32 v209, v209\n"
    "v_mfma_f32_16x16x32_bf16 v[204:207], a[228:231], v[52:55], v[204:207]\n"
    "v_cvt_pk_bf16_f32 v214, v208, v209\n"
    "v_add_f32 v220, v220, v209\n"
    "v_mfma_f32_16x16x32_bf16 v[204:207], a[232:235], v[56:59], v[204:207]\n"
    "ds_read_b64_tr_b16 v[196:197], v30 offset:8192\n"
    "v_exp_f32 v210, v210\n"
    "v_mfma_f32_16x16x32_bf16 v[204:207], a[236:239], v[60:63], v[204:207]\n"
    "ds_read_b64_tr_b16 v[198:199], v30 offset:12288\n"
    "v_add_f32 v220, v220, v210\n"
    "v_mfma_f32_16x16x32_bf16 a[48:51], v[172:175], v[12:15], a[48:51]\n"
    "ds_read_b64_tr_b16 v[200:201], v31 offset:8192\n"
    "v_exp_f32 v211, v211\n"
    "v_mfma_f32_16x16x32_bf16 a[52:55], v[176:179], v[12:15], a[52:55]\n"
    "s_add_u32 s77, s77, s78\n"
    "v_cvt_pk_bf16_f32 v215, v210, v211\n"
    "v_add_f32 v220, v220, v211\n"
    "v_mfma_f32_16x16x32_bf16 a[104:107], v[180:183], v[12:15], a[104:107]\n"
    "ds_read_b64_tr_b16 v[202:203], v31 offset:12288\n"
    "v_exp_f32 v204, v204\n"
    "v_mfma_f32_16x16x32_bf16 a[108:111], v[184:187], v[12:15], a[108:111]\n"
    "ds_read_b64_tr_b16 v[172:173], v24 offset:8192\n"
    "v_add_f32 v221, v221, v204\n"
    "v_mfma_f32_16x16x32_bf16 v[208:211], a[240:243], v[48:51], v[148:151]\n"
    "ds_read_b64_tr_b16 v[174:175], v24 offset:12288\n"
    "v_exp_f32 v205, v205\n"
    "v_mfma_f32_16x16x32_bf16 v[208:211], a[244:247], v[52:55], v[208:211]\n"
    "v_cvt_pk_bf16_f32 v216, v204, v205\n"
    "v_add_f32 v221, v221, v205\n"
    "v_mfma_f32_16x16x32_bf16 v[208:211], a[248:251], v[56:59], v[208:211]\n"
    "ds_read_b64_tr_b16 v[176:177], v25 offset:8192\n"
    "v_exp_f32 v206, v206\n"
    "v_mfma_f32_16x16x32_bf16 v[208:211], a[252:255], v[60:63], v[208:211]\n"
    "s_waitcnt lgkmcnt(9)\n"
    "ds_read_b64_tr_b16 v[178:179], v25 offset:12288\n"
    "v_add_f32 v221, v221, v206\n"
    "v_mfma_f32_16x16x32_bf16 a[112:115], v[188:191], v[212:215], a[112:115]\n"
    "s_waitcnt lgkmcnt(8)\n"
    "ds_read_b64_tr_b16 v[180:181], v26 offset:8192\n"
    "v_exp_f32 v207, v207\n"
    "v_mfma_f32_16x16x32_bf16 a[116:119], v[192:195], v[212:215], a[116:119]\n"
    "s_waitcnt lgkmcnt(7)\n"
    "v_cvt_pk_bf16_f32 v217, v206, v207\n"
    "v_add_f32 v221, v221, v207\n"
    "v_mfma_f32_16x16x32_bf16 a[168:171], v[196:199], v[212:215], a[168:171]\n"
    "s_waitcnt lgkmcnt(5)\n"
    "ds_read_b64_tr_b16 v[182:183], v26 offset:12288\n"
    "v_exp_f32 v208, v208\n"
    "v_mfma_f32_16x16x32_bf16 a[172:175], v[200:203], v[212:215], a[172:175]\n"
    "ds_read_b64_tr_b16 v[184:185], v27 offset:8192\n"
    "v_add_f32 v221, v221, v208\n"
    "v_mfma_f32_16x16x32_bf16 v[204:207], a[224:227], v[64:67], v[152:155]\n"
    "ds_read_b64_tr_b16 v[186:187], v27 offset:12288\n"
    "v_exp_f32 v209, v209\n"
    "v_mfma_f32_16x16x32_bf16 v[204:207], a[228:231], v[68:71], v[204:207]\n"
    "v_cvt_pk_bf16_f32 v218, v208, v209\n"
    "v_add_f32 v221, v221, v209\n"
    "v_mfma_f32_16x16x32_bf16 v[204:207], a[232:235], v[72:75], v[204:207]\n"
    "s_memrealtime s[36:37]\n"
    "v_exp_f32 v210, v210\n"
    "v_mfma_f32_16x16x32_bf16 v[204:207], a[236:239], v[76:79], v[204:207]\n"
    "s_waitcnt lgkmcnt(6)\n"
    "v_add_f32 v221, v221, v210\n"
    "v_mfma_f32_16x16x32_bf16 a[0:3], v[172:175], v[212:215], a[0:3]\n"
    "s_waitcnt lgkmcnt(4)\n"
    "v_exp_f32 v211, v211\n"
    "v_mfma_f32_16x16x32_bf16 a[4:7], v[176:179], v[212:215], a[4:7]\n"
    "s_waitcnt lgkmcnt(2)\n"
    "v_cvt_pk_bf16_f32 v219, v210, v211\n"
    "v_add_f32 v221, v221, v211\n"
    "v_mfma_f32_16x16x32_bf16 a[56:59], v[180:183], v[212:215], a[56:59]\n"
    "s_waitcnt lgkmcnt(0)\n"
    "v_exp_f32 v204, v204\n"
    "v_mfma_f32_16x16x32_bf16 a[60:63], v[184:187], v[212:215], a[60:63]\n"
    "s_barrier\n"
    "v_add_f32 v222, v222, v204\n"
    "v_mfma_f32_16x16x32_bf16 v[208:211], a[240:243], v[64:67], v[152:155]\n"
    "v_exp_f32 v205, v205\n"
    "v_mfma_f32_16x16x32_bf16 v[208:211], a[244:247], v[68:71], v[208:211]\n"
    "v_cvt_pk_bf16_f32 v212, v204, v205\n"
    "v_add_f32 v222, v222, v205\n"
    "v_mfma_f32_16x16x32_bf16 v[208:211], a[248:251], v[72:75], v[208:211]\n"
    "v_exp_f32 v206, v206\n"
    "v_mfma_f32_16x16x32_bf16 v[208:211], a[252:255], v[76:79], v[208:211]\n"
    "buffer_load_dwordx4 v8, s[48:51], s72 offen offset:3072 lds\n"
    "v_mfma_f32_16x16x32_bf16 a[120:123], v[188:191], v[216:219], a[120:123]\n"
    "s_sub_u32 s36, s36, s77\n"
    "v_exp_f32 v207, v207\n"
    "v_mfma_f32_16x16x32_bf16 a[124:127], v[192:195], v[216:219], a[124:127]\n"
    "s_cmp_lt_i32 s36, 0\n"
    "v_cvt_pk_bf16_f32 v213, v206, v207\n"
    "v_add_f32 v222, v222, v207\n"
    "v_mfma_f32_16x16x32_bf16 a[176:179], v[196:199], v[216:219], a[176:179]\n"
    "s_cbranch_scc1 LPW1_%=\n"
    "LPR1_%=:\n"
    "v_exp_f32 v208, v208\n"
    "v_mfma_f32_16x16x32_bf16 a[180:183], v[200:203], v[216:219], a[180:183]\n"
    "s_add_u32 s72, s72, 4096\n"
    "v_add_f32 v222, v222, v206\n"
    "v_add_f32 v222, v222, v208\n"
    "v_mfma_f32_16x16x32_bf16 v[204:207], a[224:227], v[80:83], v[156:159]\n"
    "s_and_b32 s72, s72, 131071\n"
    "v_exp_f32 v209, v209\n"
    "v_mfma_f32_16x16x32_bf16 v[204:207], a[228:231], v[84:87], v[204:207]\n"
    "s_add_u32 s73, s73, 4096\n"
    "s_sub_u32 s74, s73, 40960\n"
    "s_min_u32 s73, s73, s74\n"
    "v_cvt_pk_bf16_f32 v214, v208, v209\n"
    "v_add_f32 v222, v222, v209\n"
    "v_mfma_f32_16x16x32_bf16 v[204:207], a[232:235], v[88:91], v[204:207]\n"
    "s_add_u32 m0, s60, s73\n"
    "v_exp_f32 v210, v210\n"
    "v_mfma_f32_16x16x32_bf16 v[204:207], a[236:239], v[92:95], v[204:207]\n"
    "buffer_load_dwordx4 v5, s[48:51], s72 offen offset:0 nt lds\n"
    "v_mfma_f32_16x16x32_bf16 a[8:11], v[172:175], v[216:219], a[8:11]\n"
    "v_exp_f32 v211, v211\n"
    "v_mfma_f32_16x16x32_bf16 a[12:15], v[176:179], v[216:219], a[12:15]\n"
    "v_cvt_pk_bf16_f32 v215, v210, v211\n"
    "v_add_f32 v222, v222, v211\n"
    "v_mfma_f32_16x16x32_bf16 a[64:67], v[180:183], v[216:219], a[64:67]\n"
    "v_exp_f32 v204, v204\n"
    "v_mfma_f32_16x16x32_bf16 a[68:71], v[184:187], v[216:219], a[68:71]\n"
    "v_add_f32 v222, v222, v210\n"
    "v_add_f32 v223, v223, v204\n"
    "v_mfma_f32_16x16x32_bf16 v[208:211], a[240:243], v[80:83], v[156:159]\n"
    "v_exp_f32 v205, v205\n"
    "v_mfma_f32_16x16x32_bf16 v[208:211], a[244:247], v[84:87], v[208:211]\n"
    "v_cvt_pk_bf16_f32 v216, v204, v205\n"
    "v_add_f32 v223, v223, v205\n"
    "v_mfma_f32_16x16x32_bf16 v[208:211], a[248:251], v[88:91], v[208:211]\n"
    "v_exp_f32 v206, v206\n"
    "v_mfma_f32_16x16x32_bf16 v[208:211], a[252:255], v[92:95], v[208:211]\n"
    "buffer_load_dwordx4 v6, s[48:51], s72 offen offset:1024 nt lds\n"
    "v_mfma_f32_16x16x32_bf16 a[128:131], v[188:191], v[212:215], a[128:131]\n"
    "v_exp_f32 v207, v207\n"
    "v_mfma_f32_16x16x32_bf16 a[132:135], v[192:195], v[212:215], a[132:135]\n"
    "v_cvt_pk_bf16_f32 v217, v206, v207\n"
    "v_add_f32 v223, v223, v207\n"
    "v_mfma_f32_16x16x32_bf16 a[184:187], v[196:199], v[212:215], a[184:187]\n"
    "v_exp_f32 v208, v208\n"
    "v_mfma_f32_16x16x32_bf16 a[188:191], v[200:203], v[212:215], a[188:191]\n"
    "v_add_f32 v223, v223, v206\n"
    "v_add_f32 v223, v223, v208\n"
    "v_mfma_f32_16x16x32_bf16 v[204:207], a[224:227], v[96:99], v[160:163]\n"
    "v_exp_f32 v209, v209\n"
    "v_mfma_f32_16x16x32_bf16 v[204:207], a[228:231], v[100:103], v[204:207]\n"
    "v_cvt_pk_bf16_f32 v218, v208, v209\n"
    "v_add_f32 v223, v223, v209\n"
    "v_mfma_f32_16x16x32_bf16 v[204:207], a[232:235], v[104:107], v[204:207]\n"
    "v_exp_f32 v210, v210\n"
    "v_mfma_f32_16x16x32_bf16 v[204:207], a[236:239], v[108:111], v[204:207]\n"
    "buffer_load_dwordx4 v7, s[48:51], s72 offen offset:2048 nt lds\n"
    "v_mfma_f32_16x16x32_bf16 a[16:19], v[172:175], v[212:215], a[16:19]\n"
    "v_exp_f32 v211, v211\n"
    "v_mfma_f32_16x16x32_bf16 a[20:23], v[176:179], v[212:215], a[20:23]\n"
    "v_cvt_pk_bf16_f32 v219, v210, v211\n"
    "v_add_f32 v223, v223, v211\n"
    "v_mfma_f32_16x16x32_bf16 a[72:75], v[180:183], v[212:215], a[72:75]\n"
    "v_exp_f32 v204, v204\n"
    "v_mfma_f32_16x16x32_bf16 a[76:79], v[184:187], v[212:215], a[76:79]\n"
    "v_add_f32 v223, v223, v210\n"
    "v_add_f32 v224, v224, v204\n"
    "v_mfma_f32_16x16x32_bf16 v[208:211], a[240:243], v[96:99], v[160:163]\n"
    "v_exp_f32 v205, v205\n"
    "v_mfma_f32_16x16x32_bf16 v[208:211], a[244:247], v[100:103], v[208:211]\n"
    "v_cvt_pk_bf16_f32 v212, v204, v205\n"
    "v_add_f32 v224, v224, v205\n"
    "v_mfma_f32_16x16x32_bf16 v[208:211], a[248:251], v[104:107], v[208:211]\n"
    "v_exp_f32 v206, v206\n"
    "v_mfma_f32_16x16x32_bf16 v[208:211], a[252:255], v[108:111], v[208:211]\n"
    "buffer_load_dwordx4 v8, s[48:51], s72 offen offset:3072 lds\n"
    "v_mfma_f32_16x16x32_bf16 a[136:139], v[188:191], v[216:219], a[136:139]\n"
    "s_add_u32 s72, s72, 4096\n"
    "v_exp_f32 v207, v207\n"
    "v_mfma_f32_16x16x32_bf16 a[140:143], v[192:195], v[216:219], a[140:143]\n"
    "s_and_b32 s72, s72, 131071\n"
    "v_cvt_pk_bf16_f32 v213, v206, v207\n"
    "v_add_f32 v224, v224, v207\n"
    "v_mfma_f32_16x16x32_bf16 a[192:195], v[196:199], v[216:219], a[192:195]\n"
    "s_add_u32 s73, s73, 4096\n"
    "v_exp_f32 v208, v208\n"
    "v_mfma_f32_16x16x32_bf16 a[196:199], v[200:203], v[216:219], a[196:199]\n"
    "s_sub_u32 s74, s73, 40960\n"
    "v_add_f32 v224, v224, v206\n"
    "v_add_f32 v224, v224, v208\n"
    "v_mfma_f32_16x16x32_bf16 v[204:207], a[224:227], v[112:115], v[164:167]\n"
    "s_min_u32 s73, s73, s74\n"
    "v_exp_f32 v209, v209\n"
    "v_mfma_f32_16x16x32_bf16 v[204:207], a[228:231], v[116:119], v[204:207]\n"
    "v_cvt_pk_bf16_f32 v214, v208, v209\n"
    "v_add_f32 v224, v224, v209\n"
    "v_mfma_f32_16x16x32_bf16 v[204:207], a[232:235], v[120:123], v[204:207]\n"
    "s_add_u32 m0, s60, s73\n"
    "v_exp_f32 v210, v210\n"
    "v_mfma_f32_16x16x32_bf16 v[204:207], a[236:239], v[124:127], v[204:207]\n"
    "buffer_load_dwordx4 v5, s[48:51], s72 offen offset:0 nt lds\n"
    "v_mfma_f32_16x16x32_bf16 a[24:27], v[172:175], v[216:219], a[24:27]\n"
    "v_exp_f32 v211, v211\n"
    "v_mfma_f32_16x16x32_bf16 a[28:31], v[176:179], v[216:219], a[28:31]\n"
    "v_cvt_pk_bf16_f32 v215, v210, v211\n"
    "v_add_f32 v224, v224, v211\n"
    "v_mfma_f32_16x16x32_bf16 a[80:83], v[180:183], v[216:219], a[80:83]\n"
    "v_exp_f32 v204, v204\n"
    "v_mfma_f32_16x16x32_bf16 a[84:87], v[184:187], v[216:219], a[84:87]\n"
    "v_add_f32 v224, v224, v210\n"
    "v_add_f32 v225, v225, v204\n"
    "v_mfma_f32_16x16x32_bf16 v[208:211], a[240:243], v[112:115], v[164:167]\n"
    "v_exp_f32 v205, v205\n"
    "v_mfma_f32_16x16x32_bf16 v[208:211], a[244:247], v[116:119], v[208:211]\n"
    "v_cvt_pk_bf16_f32 v216, v204, v205\n"
    "v_add_f32 v225, v225, v205\n"
    "v_mfma_f32_16x16x32_bf16 v[208:211], a[248:251], v[120:123], v[208:211]\n"
    "s_waitcnt vmcnt(14)\n"
    "v_exp_f32 v206, v206\n"
    "v_mfma_f32_16x16x32_bf16 v[208:211], a[252:255], v[124:127], v[208:211]\n"
    "s_barrier\n"
    "v_mfma_f32_16x16x32_bf16 a[144:147], v[188:191], v[212:215], a[144:147]\n"
    "v_exp_f32 v207, v207\n"
    "v_mfma_f32_16x16x32_bf16 a[148:151], v[192:195], v[212:215], a[148:151]\n"
    "v_cvt_pk_bf16_f32 v217, v206, v207\n"
    "v_add_f32 v225, v225, v207\n"
    "v_mfma_f32_16x16x32_bf16 a[200:203], v[196:199], v[212:215], a[200:203]\n"
    "v_exp_f32 v208, v208\n"
    "v_mfma_f32_16x16x32_bf16 a[204:207], v[200:203], v[212:215], a[204:207]\n"
    "v_add_f32 v225, v225, v206\n"
    "v_add_f32 v225, v225, v208\n"
    "v_mfma_f32_16x16x32_bf16 v[204:207], a[224:227], v[128:131], v[168:171]\n"
    "v_exp_f32 v209, v209\n"
    "v_mfma_f32_16x16x32_bf16 v[204:207], a[228:231], v[132:135], v[204:207]\n"
    "v_cvt_pk_bf16_f32 v218, v208, v209\n"
    "v_add_f32 v225, v225, v209\n"
    "v_mfma_f32_16x16x32_bf16 v[204:207], a[232:235], v[136:139], v[204:207]\n"
    "v_exp_f32 v210, v210\n"
    "v_mfma_f32_16x16x32_bf16 v[204:207], a[236:239], v[140:143], v[204:207]\n"
    "ds_read_b128 a[224:227], v20 offset:16384\n"
    "v_add_f32 v225, v225, v210\n"
    "v_mfma_f32_16x16x32_bf16 a[32:35], v[172:175], v[212:215], a[32:35]\n"
    "ds_read_b128 a[228:231], v21 offset:16384\n"
    "v_exp_f32 v211, v211\n"
    "v_mfma_f32_16x16x32_bf16 a[36:39], v[176:179], v[212:215], a[36:39]\n"
    "v_cvt_pk_bf16_f32 v219, v210, v211\n"
    "v_add_f32 v225, v225, v211\n"
    "v_mfma_f32_16x16x32_bf16 a[88:91], v[180:183], v[212:215], a[88:91]\n"
    "ds_read_b128 a[232:235], v22 offset:16384\n"
    "v_exp_f32 v204, v204\n"
    "v_mfma_f32_16x16x32_bf16 a[92:95], v[184:187], v[212:215], a[92:95]\n"
    "ds_read_b128 a[236:239], v23 offset:16384\n"
    "v_add_f32 v226, v226, v204\n"
    "v_mfma_f32_16x16x32_bf16 v[208:211], a[240:243], v[128:131], v[168:171]\n"
    "v_exp_f32 v205, v205\n"
    "v_mfma_f32_16x16x32_bf16 v[208:211], a[244:247], v[132:135], v[208:211]\n"
    "v_cvt_pk_bf16_f32 v12, v204, v205\n"
    "v_add_f32 v226, v226, v205\n"
    "v_mfma_f32_16x16x32_bf16 v[208:211], a[248:251], v[136:139], v[208:211]\n"
    "v_exp_f32 v206, v206\n"
    "v_mfma_f32_16x16x32_bf16 v[208:211], a[252:255], v[140:143], v[208:211]\n"
    "ds_read_b128 a[240:243], v20 offset:20480\n"
    "v_add_f32 v226, v226, v206\n"
    "v_mfma_f32_16x16x32_bf16 a[152:155], v[188:191], v[216:219], a[152:155]\n"
    "ds_read_b128 a[244:247], v21 offset:20480\n"
    "v_exp_f32 v207, v207\n"
    "v_mfma_f32_16x16x32_bf16 a[156:159], v[192:195], v[216:219], a[156:159]\n"
    "v_cvt_pk_bf16_f32 v13, v206, v207\n"
    "v_add_f32 v226, v226, v207\n"
    "v_mfma_f32_16x16x32_bf16 a[208:211], v[196:199], v[216:219], a[208:211]\n"
    "v_exp_f32 v208, v208\n"
    "v_mfma_f32_16x16x32_bf16 a[212:215], v[200:203], v[216:219], a[212:215]\n"
    "s_waitcnt lgkmcnt(5)\n"
    "ds_read_b128 a[248:251], v22 offset:20480\n"
    "v_add_f32 v226, v226, v208\n"
    "v_mfma_f32_16x16x32_bf16 v[204:207], a[224:227], v[32:35], v[144:147]\n"
    "s_waitcnt lgkmcnt(5)\n"
    "v_exp_f32 v209, v209\n"
    "v_mfma_f32_16x16x32_bf16 v[204:207], a[228:231], v[36:39], v[204:207]\n"
    "s_waitcnt lgkmcnt(4)\n"
    "v_cvt_pk_bf16_f32 v14, v208, v209\n"
    "v_add_f32 v226, v226, v209\n"
    "v_mfma_f32_16x16x32_bf16 v[204:207], a[232:235], v[40:43], v[204:207]\n"
    "s_waitcnt lgkmcnt(3)\n"
    "v_exp_f32 v210, v210\n"
    "v_mfma_f32_16x16x32_bf16 v[204:207], a[236:239], v[44:47], v[204:207]\n"
    "ds_read_b128 a[252:255], v23 offset:20480\n"
    "v_add_f32 v226, v226, v210\n"
    "v_mfma_f32_16x16x32_bf16 a[40:43], v[172:175], v[216:219], a[40:43]\n"
    "v_exp_f32 v211, v211\n"
    "v_mfma_f32_16x16x32_bf16 a[44:47], v[176:179], v[216:219], a[44:47]\n"
    "v_cvt_pk_bf16_f32 v15, v210, v211\n"
    "v_add_f32 v226, v226, v211\n"
    "v_mfma_f32_16x16x32_bf16 a[96:99], v[180:183], v[216:219], a[96:99]\n"
    "v_exp_f32 v204, v204\n"
    "v_mfma_f32_16x16x32_bf16 a[100:103], v[184:187], v[216:219], a[100:103]\n"
    "s_waitcnt lgkmcnt(3)\n"
    "buffer_load_dwordx4 v6, s[48:51], s72 offen offset:1024 nt lds\n"
    "v_mfma_f32_16x16x32_bf16 v[208:211], a[240:243], v[32:35], v[144:147]\n"
    "s_waitcnt lgkmcnt(2)\n"
    "v_exp_f32 v205, v205\n"
    "v_mfma_f32_16x16x32_bf16 v[208:211], a[244:247], v[36:39], v[208:211]\n"
    "s_waitcnt lgkmcnt(1)\n"
    "v_cvt_pk_bf16_f32 v212, v204, v205\n"
    "v_add_f32 v220, v220, v205\n"
    "v_mfma_f32_16x16x32_bf16 v[208:211], a[248:251], v[40:43], v[208:211]\n"
    "s_waitcnt lgkmcnt(0)\n"
    "v_exp_f32 v206, v206\n"
    "v_mfma_f32_16x16x32_bf16 v[208:211], a[252:255], v[44:47], v[208:211]\n"
    "v_add_f32 v220, v220, v204\n"
    "v_add_f32 v220, v220, v206\n"
    "v_mfma_f32_16x16x32_bf16 a[160:163], v[188:191], v[12:15], a[160:163]\n"
    "ds_read_b64_tr_b16 v[188:189], v28 offset:16384\n"
    "v_exp_f32 v207, v207\n"
    "v_mfma_f32_16x16x32_bf16 a[164:167], v[192:195], v[12:15], a[164:167]\n"
    "v_cvt_pk_bf16_f32 v213, v206, v207\n"
    "v_add_f32 v220, v220, v207\n"
    "v_mfma_f32_16x16x32_bf16 a[216:219], v[196:199], v[12:15], a[216:219]\n"
    "ds_read_b64_tr_b16 v[190:191], v28 offset:20480\n"
    "v_exp_f32 v208, v208\n"
    "v_mfma_f32_16x16x32_bf16 a[220:223], v[200:203], v[12:15], a[220:223]\n"
    "ds_read_b64_tr_b16 v[192:193], v29 offset:16384\n"
    "v_add_f32 v220, v220, v208\n"
    "v_mfma_f32_16x16x32_bf16 v[204:207], a[224:227], v[48:51], v[148:151]\n"
    "ds_read_b64_tr_b16 v[194:195], v29 offset:20480\n"
    "v_exp_f32 v209, v209\n"
    "v_mfma_f32_16x16x32_bf16 v[204:207], a[228:231], v[52:55], v[204:207]\n"
    "v_cvt_pk_bf16_f32 v214, v208, v209\n"
    "v_add_f32 v220, v220, v209\n"
    "v_mfma_f32_16x16x32_bf16 v[204:207], a[232:235], v[56:59], v[204:207]\n"
    "ds_read_b64_tr_b16 v[196:197], v30 offset:16384\n"
    "v_exp_f32 v210, v210\n"
    "v_mfma_f32_16x16x32_bf16 v[204:207], a[236:239], v[60:63], v[204:207]\n"
    "ds_read_b64_tr_b16 v[198:199], v30 offset:20480\n"
    "v_add_f32 v220, v220, v210\n"
    "v_mfma_f32_16x16x32_bf16 a[48:51], v[172:175], v[12:15], a[48:51]\n"
    "ds_read_b64_tr_b16 v[200:201], v31 offset:16384\n"
    "v_exp_f32 v211, v211\n"
    "v_mfma_f32_16x16x32_bf16 a[52:55], v[176:179], v[12:15], a[52:55]\n"
    "s_add_u32 s77, s77, s78\n"
    "v_cvt_pk_bf16_f32 v215, v210, v211\n"
    "v_add_f32 v220, v220, v211\n"
    "v_mfma_f32_16x16x32_bf16 a[104:107], v[180:183], v[12:15], a[104:107]\n"
    "ds_read_b64_tr_b16 v[202:203], v31 offset:20480\n"
    "v_exp_f32 v204, v204\n"
    "v_mfma_f32_16x16x32_bf16 a[108:111], v[184:187], v[12:15], a[108:111]\n"
    "ds_read_b64_tr_b16 v[172:173], v24 offset:16384\n"
    "v_add_f32 v221, v221, v204\n"
    "v_mfma_f32_16x16x32_bf16 v[208:211], a[240:243], v[48:51], v[148:151]\n"
    "ds_read_b64_tr_b16 v[174:175], v24 offset:20480\n"
    "v_exp_f32 v205, v205\n"
    "v_mfma_f32_16x16x32_bf16 v[208:211], a[244:247], v[52:55], v[208:211]\n"
    "v_cvt_pk_bf16_f32 v216, v204, v205\n"
    "v_add_f32 v221, v221, v205\n"
    "v_mfma_f32_16x16x32_bf16 v[208:211], a[248:251], v[56:59], v[208:211]\n"
    "ds_read_b64_tr_b16 v[176:177], v25 offset:16384\n"
    "v_exp_f32 v206, v206\n"
    "v_mfma_f32_16x16x32_bf16 v[208:211], a[252:255], v[60:63], v[208:211]\n"
    "s_waitcnt lgkmcnt(9)\n"
    "ds_read_b64_tr_b16 v[178:179], v25 offset:20480\n"
    "v_add_f32 v221, v221, v206\n"
    "v_mfma_f32_16x16x32_bf16 a[112:115], v[188:191], v[212:215], a[112:115]\n"
    "s_waitcnt lgkmcnt(8)\n"
    "ds_read_b64_tr_b16 v[180:181], v26 offset:16384\n"
    "v_exp_f32 v207, v207\n"
    "v_mfma_f32_16x16x32_bf16 a[116:119], v[192:195], v[212:215], a[116:119]\n"
    "s_waitcnt lgkmcnt(7)\n"
    "v_cvt_pk_bf16_f32 v217, v206, v207\n"
    "v_add_f32 v221, v221, v207\n"
    "v_mfma_f32_16x16x32_bf16 a[168:171], v[196:199], v[212:215], a[168:171]\n"
    "s_waitcnt lgkmcnt(5)\n"
    "ds_read_b64_tr_b16 v[182:183], v26 offset:20480\n"
    "v_exp_f32 v208, v208\n"
    "v_mfma_f32_16x16x32_bf16 a[172:175], v[200:203], v[212:215], a[172:175]\n"
    "ds_read_b64_tr_b16 v[184:185], v27 offset:16384\n"
    "v_add_f32 v221, v221, v208\n"
    "v_mfma_f32_16x16x32_bf16 v[204:207], a[224:227], v[64:67], v[152:155]\n"
    "ds_read_b64_tr_b16 v[186:187], v27 offset:20480\n"
    "v_exp_f32 v209, v209\n"
    "v_mfma_f32_16x16x32_bf16 v[204:207], a[228:231], v[68:71], v[204:207]\n"
    "v_cvt_pk_bf16_f32 v218, v208, v209\n"
    "v_add_f32 v221, v221, v209\n"
    "v_mfma_f32_16x16x32_bf16 v[204:207], a[232:235], v[72:75], v[204:207]\n"
    "s_memrealtime s[36:37]\n"
    "v_exp_f32 v210, v210\n"
    "v_mfma_f32_16x16x32_bf16 v[204:207], a[236:239], v[76:79], v[204:207]\n"
    "s_waitcnt lgkmcnt(6)\n"
    "v_add_f32 v221, v221, v210\n"
    "v_mfma_f32_16x16x32_bf16 a[0:3], v[172:175], v[212:215], a[0:3]\n"
    "s_waitcnt lgkmcnt(4)\n"
    "v_exp_f32 v211, v211\n"
    "v_mfma_f32_16x16x32_bf16 a[4:7], v[176:179], v[212:215], a[4:7]\n"
    "s_waitcnt lgkmcnt(2)\n"
    "v_cvt_pk_bf16_f32 v219, v210, v211\n"
    "v_add_f32 v221, v221, v211\n"
    "v_mfma_f32_16x16x32_bf16 a[56:59], v[180:183], v[212:215], a[56:59]\n"
    "s_waitcnt lgkmcnt(0)\n"
    "v_exp_f32 v204, v204\n"
    "v_mfma_f32_16x16x32_bf16 a[60:63], v[184:187], v[212:215], a[60:63]\n"
    "s_barrier\n"
    "v_add_f32 v222, v222, v204\n"
    "v_mfma_f32_16x16x32_bf16 v[208:211], a[240:243], v[64:67], v[152:155]\n"
    "v_exp_f32 v205, v205\n"
    "v_mfma_f32_16x16x32_bf16 v[208:211], a[244:247], v[68:71], v[208:211]\n"
    "v_cvt_pk_bf16_f32 v212, v204, v205\n"
    "v_add_f32 v222, v222, v205\n"
    "v_mfma_f32_16x16x32_bf16 v[208:211], a[248:251], v[72:75], v[208:211]\n"
    "v_exp_f32 v206, v206\n"
    "v_mfma_f32_16x16x32_bf16 v[208:211], a[252:255], v[76:79], v[208:211]\n"
    "buffer_load_dwordx4 v7, s[48:51], s72 offen offset:2048 nt lds\n"
    "v_mfma_f32_16x16x32_bf16 a[120:123], v[188:191], v[216:219], a[120:123]\n"
    "s_sub_u32 s36, s36, s77\n"
    "v_exp_f32 v207, v207\n"
    "v_mfma_f32_16x16x32_bf16 a[124:127], v[192:195], v[216:219], a[124:127]\n"
    "s_cmp_lt_i32 s36, 0\n"
    "v_cvt_pk_bf16_f32 v213, v206, v207\n"
    "v_add_f32 v222, v222, v207\n"
    "v_mfma_f32_16x16x32_bf16 a[176:179], v[196:199], v[216:219], a[176:179]\n"
    "s_cbranch_scc1 LPW2_%=\n"
    "LPR2_%=:\n"
    "v_exp_f32 v208, v208\n"
    "v_mfma_f32_16x16x32_bf16 a[180:183], v[200:203], v[216:219], a[180:183]\n"
    "v_add_f32 v222, v222, v206\n"
    "v_add_f32 v222, v222, v208\n"
    "v_mfma_f32_16x16x32_bf16 v[204:207], a[224:227], v[80:83], v[156:159]\n"
    "v_exp_f32 v209, v209\n"
    "v_mfma_f32_16x16x32_bf16 v[204:207], a[228:231], v[84:87], v[204:207]\n"
    "v_cvt_pk_bf16_f32 v214, v208, v209\n"
    "v_add_f32 v222, v222, v209\n"
    "v_mfma_f32_16x16x32_bf16 v[204:207], a[232:235], v[88:91], v[204:207]\n"
    "v_exp_f32 v210, v210\n"
    "v_mfma_f32_16x16x32_bf16 v[204:207], a[236:239], v[92:95], v[204:207]\n"
    "buffer_load_dwordx4 v8, s[48:51], s72 offen offset:3072 lds\n"
    "v_mfma_f32_16x16x32_bf16 a[8:11], v[172:175], v[216:219], a[8:11]\n"
    "s_add_u32 s72, s72, 4096\n"
    "v_exp_f32 v211, v211\n"
    "v_mfma_f32_16x16x32_bf16 a[12:15], v[176:179], v[216:219], a[12:15]\n"
    "s_and_b32 s72, s72, 131071\n"
    "v_cvt_pk_bf16_f32 v215, v210, v211\n"
    "v_add_f32 v222, v222, v211\n"
    "v_mfma_f32_16x16x32_bf16 a[64:67], v[180:183], v[216:219], a[64:67]\n"
    "s_add_u32 s73, s73, 4096\n"
    "v_exp_f32 v204, v204\n"
    "v_mfma_f32_16x16x32_bf16 a[68:71], v[184:187], v[216:219], a[68:71]\n"
    "s_sub_u32 s74, s73, 40960\n"
    "v_add_f32 v222, v222, v210\n"
    "v_add_f32 v223, v223, v204\n"
    "v_mfma_f32_16x16x32_bf16 v[208:211], a[240:243], v[80:83], v[156:159]\n"
    "s_min_u32 s73, s73, s74\n"
    "v_exp_f32 v205, v205\n"
    "v_mfma_f32_16x16x32_bf16 v[208:211], a[244:247], v[84:87], v[208:211]\n"
    "v_cvt_pk_bf16_f32 v216, v204, v205\n"
    "v_add_f32 v223, v223, v205\n"
    "v_mfma_f32_16x16x32_bf16 v[208:211], a[248:251], v[88:91], v[208:211]\n"
    "s_add_u32 m0, s60, s73\n"
    "v_exp_f32 v206, v206\n"
    "v_mfma_f32_16x16x32_bf16 v[208:211], a[252:255], v[92:95], v[208:211]\n"
    "buffer_load_dwordx4 v5, s[48:51], s72 offen offset:0 nt lds\n"
    "v_mfma_f32_16x16x32_bf16 a[128:131], v[188:191], v[212:215], a[128:131]\n"
    "v_exp_f32 v207, v207\n"
    "v_mfma_f32_16x16x32_bf16 a[132:135], v[192:195], v[212:215], a[132:135]\n"
    "v_cvt_pk_bf16_f32 v217, v206, v207\n"
    "v_add_f32 v223, v223, v207\n"
    "v_mfma_f32_16x16x32_bf16 a[184:187], v[196:199], v[212:215], a[184:187]\n"
    "v_exp_f32 v208, v208\n"
    "v_mfma_f32_16x16x32_bf16 a[188:191], v[200:203], v[212:215], a[188:191]\n"
    "v_add_f32 v223, v223, v206\n"
    "v_add_f32 v223, v223, v208\n"
    "v_mfma_f32_16x16x32_bf16 v[204:207], a[224:227], v[96:99], v[160:163]\n"
    "v_exp_f32 v209, v209\n"
    "v_mfma_f32_16x16x32_bf16 v[204:207], a[228:231], v[100:103], v[204:207]\n"
    "v_cvt_pk_bf16_f32 v218, v208, v209\n"
    "v_add_f32 v223, v223, v209\n"
    "v_mfma_f32_16x16x32_bf16 v[204:207], a[232:235], v[104:107], v[204:207]\n"
    "v_exp_f32 v210, v210\n"
    "v_mfma_f32_16x16x32_bf16 v[204:207], a[236:239], v[108:111], v[204:207]\n"
    "v_add_f32 v223, v223, v210\n"
    "v_mfma_f32_16x16x32_bf16 a[16:19], v[172:175], v[212:215], a[16:19]\n"
    "v_exp_f32 v211, v211\n"
    "v_mfma_f32_16x16x32_bf16 a[20:23], v[176:179], v[212:215], a[20:23]\n"
    "v_cvt_pk_bf16_f32 v219, v210, v211\n"
    "v_add_f32 v223, v223, v211\n"
    "v_mfma_f32_16x16x32_bf16 a[72:75], v[180:183], v[212:215], a[72:75]\n"
    "v_exp_f32 v204, v204\n"
    "v_mfma_f32_16x16x32_bf16 a[76:79], v[184:187], v[212:215], a[76:79]\n"
    "v_add_f32 v224, v224, v204\n"
    "v_mfma_f32_16x16x32_bf16 v[208:211], a[240:243], v[96:99], v[160:163]\n"
    "v_exp_f32 v205, v205\n"
    "v_mfma_f32_16x16x32_bf16 v[208:211], a[244:247], v[100:103], v[208:211]\n"
    "v_cvt_pk_bf16_f32 v212, v204, v205\n"
    "v_add_f32 v224, v224, v205\n"
    "v_mfma_f32_16x16x32_bf16 v[208:211], a[248:251], v[104:107], v[208:211]\n"
    "v_exp_f32 v206, v206\n"
    "v_mfma_f32_16x16x32_bf16 v[208:211], a[252:255], v[108:111], v[208:211]\n"
    "buffer_load_dwordx4 v6, s[48:51], s72 offen offset:1024 nt lds\n"
    "v_mfma_f32_16x16x32_bf16 a[136:139], v[188:191], v[216:219], a[136:139]\n"
    "v_exp_f32 v207, v207\n"
    "v_mfma_f32_16x16x32_bf16 a[140:143], v[192:195], v[216:219], a[140:143]\n"
    "v_cvt_pk_bf16_f32 v213, v206, v207\n"
    "v_add_f32 v224, v224, v207\n"
    "v_mfma_f32_16x16x32_bf16 a[192:195], v[196:199], v[216:219], a[192:195]\n"
    "v_exp_f32 v208, v208\n"
    "v_mfma_f32_16x16x32_bf16 a[196:199], v[200:203], v[216:219], a[196:199]\n"
    "v_add_f32 v224, v224, v206\n"
    "v_add_f32 v224, v224, v208\n"
    "v_mfma_f32_16x16x32_bf16 v[204:207], a[224:227], v[112:115], v[164:167]\n"
    "v_exp_f32 v209, v209\n"
    "v_mfma_f32_16x16x32_bf16 v[204:207], a[228:231], v[116:119], v[204:207]\n"
    "v_cvt_pk_bf16_f32 v214, v208, v209\n"
    "v_add_f32 v224, v224, v209\n"
    "v_mfma_f32_16x16x32_bf16 v[204:207], a[232:235], v[120:123], v[204:207]\n"
    "v_exp_f32 v210, v210\n"
    "v_mfma_f32_16x16x32_bf16 v[204:207], a[236:239], v[124:127], v[204:207]\n"
    "buffer_load_dwordx4 v7, s[48:51], s72 offen offset:2048 nt lds\n"
    "v_mfma_f32_16x16x32_bf16 a[24:27], v[172:175], v[216:219], a[24:27]\n"
    "v_exp_f32 v211, v211\n"
    "v_mfma_f32_16x16x32_bf16 a[28:31], v[176:179], v[216:219], a[28:31]\n"
    "v_cvt_pk_bf16_f32 v215, v210, v211\n"
    "v_add_f32 v224, v224, v211\n"
    "v_mfma_f32_16x16x32_bf16 a[80:83], v[180:183], v[216:219], a[80:83]\n"
    "v_exp_f32 v204, v204\n"
    "v_mfma_f32_16x16x32_bf16 a[84:87], v[184:187], v[216:219], a[84:87]\n"
    "v_add_f32 v224, v224, v210\n"
    "v_add_f32 v225, v225, v204\n"
    "v_mfma_f32_16x16x32_bf16 v[208:211], a[240:243], v[112:115], v[164:167]\n"
    "v_exp_f32 v205, v205\n"
    "v_mfma_f32_16x16x32_bf16 v[208:211], a[244:247], v[116:119], v[208:211]\n"
    "v_cvt_pk_bf16_f32 v216, v204, v205\n"
    "v_add_f32 v225, v225, v205\n"
    "v_mfma_f32_16x16x32_bf16 v[208:211], a[248:251], v[120:123], v[208:211]\n"
    "s_waitcnt vmcnt(11)\n"
    "v_exp_f32 v206, v206\n"
    "v_mfma_f32_16x16x32_bf16 v[208:211], a[252:255], v[124:127], v[208:211]\n"
    "s_barrier\n"
    "v_mfma_f32_16x16x32_bf16 a[144:147], v[188:191], v[212:215], a[144:147]\n"
    "v_exp_f32 v207, v207\n"
    "v_mfma_f32_16x16x32_bf16 a[148:151], v[192:195], v[212:215], a[148:151]\n"
    "v_cvt_pk_bf16_f32 v217, v206, v207\n"
    "v_add_f32 v225, v225, v207\n"
    "v_mfma_f32_16x16x32_bf16 a[200:203], v[196:199], v[212:215], a[200:203]\n"
    "v_exp_f32 v208, v208\n"
    "v_mfma_f32_16x16x32_bf16 a[204:207], v[200:203], v[212:215], a[204:207]\n"
    "v_add_f32 v225, v225, v206\n"
    "v_add_f32 v225, v225, v208\n"
    "v_mfma_f32_16x16x32_bf16 v[204:207], a[224:227], v[128:131], v[168:171]\n"
    "v_exp_f32 v209, v209\n"
    "v_mfma_f32_16x16x32_bf16 v[204:207], a[228:231], v[132:135], v[204:207]\n"
    "v_cvt_pk_bf16_f32 v218, v208, v209\n"
    "v_add_f32 v225, v225, v209\n"
    "v_mfma_f32_16x16x32_bf16 v[204:207], a[232:235], v[136:139], v[204:207]\n"
    "v_exp_f32 v210, v210\n"
    "v_mfma_f32_16x16x32_bf16 v[204:207], a[236:239], v[140:143], v[204:207]\n"
    "ds_read_b128 a[224:227], v20 offset:24576\n"
    "v_add_f32 v225, v225, v210\n"
    "v_mfma_f32_16x16x32_bf16 a[32:35], v[172:175], v[212:215], a[32:35]\n"
    "ds_read_b128 a[228:231], v21 offset:24576\n"
    "v_exp_f32 v211, v211\n"
    "v_mfma_f32_16x16x32_bf16 a[36:39], v[176:179], v[212:215], a[36:39]\n"
    "v_cvt_pk_bf16_f32 v219, v210, v211\n"
    "v_add_f32 v225, v225, v211\n"
    "v_mfma_f32_16x16x32_bf16 a[88:91], v[180:183], v[212:215], a[88:91]\n"
    "ds_read_b128 a[232:235], v22 offset:24576\n"
    "v_exp_f32 v204, v204\n"
    "v_mfma_f32_16x16x32_bf16 a[92:95], v[184:187], v[212:215], a[92:95]\n"
    "ds_read_b128 a[236:239], v23 offset:24576\n"
    "v_add_f32 v226, v226, v204\n"
    "v_mfma_f32_16x16x32_bf16 v[208:211], a[240:243], v[128:131], v[168:171]\n"
    "v_exp_f32 v205, v205\n"
    "v_mfma_f32_16x16x32_bf16 v[208:211], a[244:247], v[132:135], v[208:211]\n"
    "v_cvt_pk_bf16_f32 v12, v204, v205\n"
    "v_add_f32 v226, v226, v205\n"
    "v_mfma_f32_16x16x32_bf16 v[208:211], a[248:251], v[136:139], v[208:211]\n"
    "v_exp_f32 v206, v206\n"
    "v_mfma_f32_16x16x32_bf16 v[208:211], a[252:255], v[140:143], v[208:211]\n"
    "ds_read_b128 a[240:243], v20 offset:28672\n"
    "v_add_f32 v226, v226, v206\n"
    "v_mfma_f32_16x16x32_bf16 a[152:155], v[188:191], v[216:219], a[152:155]\n"
    "ds_read_b128 a[244:247], v21 offset:28672\n"
    "v_exp_f32 v207, v207\n"
    "v_mfma_f32_16x16x32_bf16 a[156:159], v[192:195], v[216:219], a[156:159]\n"
    "v_cvt_pk_bf16_f32 v13, v206, v207\n"
    "v_add_f32 v226, v226, v207\n"
    "v_mfma_f32_16x16x32_bf16 a[208:211], v[196:199], v[216:219], a[208:211]\n"
    "v_exp_f32 v208, v208\n"
    "v_mfma_f32_16x16x32_bf16 a[212:215], v[200:203], v[216:219], a[212:215]\n"
    "s_waitcnt lgkmcnt(5)\n"
    "ds_read_b128 a[248:251], v22 offset:28672\n"
    "v_add_f32 v226, v226, v208\n"
    "v_mfma_f32_16x16x32_bf16 v[204:207], a[224:227], v[32:35], v[144:147]\n"
    "s_waitcnt lgkmcnt(5)\n"
    "v_exp_f32 v209, v209\n"
    "v_mfma_f32_16x16x32_bf16 v[204:207], a[228:231], v[36:39], v[204:207]\n"
    "s_waitcnt lgkmcnt(4)\n"
    "v_cvt_pk_bf16_f32 v14, v208, v209\n"
    "v_add_f32 v226, v226, v209\n"
    "v_mfma_f32_16x16x32_bf16 v[204:207], a[232:235], v[40:43], v[204:207]\n"
    "s_waitcnt lgkmcnt(3)\n"
    "v_exp_f32 v210, v210\n"
    "v_mfma_f32_16x16x32_bf16 v[204:207], a[236:239], v[44:47], v[204:207]\n"
    "ds_read_b128 a[252:255], v23 offset:28672\n"
    "v_add_f32 v226, v226, v210\n"
    "v_mfma_f32_16x16x32_bf16 a[40:43], v[172:175], v[216:219], a[40:43]\n"
    "v_exp_f32 v211, v211\n"
    "v_mfma_f32_16x16x32_bf16 a[44:47], v[176:179], v[216:219], a[44:47]\n"
    "v_cvt_pk_bf16_f32 v15, v210, v211\n"
    "v_add_f32 v226, v226, v211\n"
    "v_mfma_f32_16x16x32_bf16 a[96:99], v[180:183], v[216:219], a[96:99]\n"
    "v_exp_f32 v204, v204\n"
    "v_mfma_f32_16x16x32_bf16 a[100:103], v[184:187], v[216:219], a[100:103]\n"
    "s_waitcnt lgkmcnt(3)\n"
    "buffer_load_dwordx4 v8, s[48:51], s72 offen offset:3072 lds\n"
    "v_mfma_f32_16x16x32_bf16 v[208:211], a[240:243], v[32:35], v[144:147]\n"
    "s_waitcnt lgkmcnt(2)\n"
    "v_exp_f32 v205, v205\n"
    "v_mfma_f32_16x16x32_bf16 v[208:211], a[244:247], v[36:39], v[208:211]\n"
    "s_waitcnt lgkmcnt(1)\n"
    "v_cvt_pk_bf16_f32 v212, v204, v205\n"
    "v_add_f32 v220, v220, v205\n"
    "v_mfma_f32_16x16x32_bf16 v[208:211], a[248:251], v[40:43], v[208:211]\n"
    "s_waitcnt lgkmcnt(0)\n"
    "v_exp_f32 v206, v206\n"
    "v_mfma_f32_16x16x32_bf16 v[208:211], a[252:255], v[44:47], v[208:211]\n"
    "s_add_u32 s72, s72, 4096\n"
    "v_add_f32 v220, v220, v204\n"
    "v_add_f32 v220, v220, v206\n"
    "v_mfma_f32_16x16x32_bf16 a[160:163], v[188:191], v[12:15], a[160:163]\n"
    "s_and_b32 s72, s72, 131071\n"
    "ds_read_b64_tr_b16 v[188:189], v28 offset:24576\n"
    "v_exp_f32 v207, v207\n"
    "v_mfma_f32_16x16x32_bf16 a[164:167], v[192:195], v[12:15], a[164:167]\n"
    "s_add_u32 s73, s73, 4096\n"
    "v_cvt_pk_bf16_f32 v213, v206, v207\n"
    "v_add_f32 v220, v220, v207\n"
    "v_mfma_f32_16x16x32_bf16 a[216:219], v[196:199], v[12:15], a[216:219]\n"
    "s_sub_u32 s74, s73, 40960\n"
    "ds_read_b64_tr_b16 v[190:191], v28 offset:28672\n"
    "v_exp_f32 v208, v208\n"
    "v_mfma_f32_16x16x32_bf16 a[220:223], v[200:203], v[12:15], a[220:223]\n"
    "s_min_u32 s73, s73, s74\n"
    "ds_read_b64_tr_b16 v[192:193], v29 offset:24576\n"
    "v_add_f32 v220, v220, v208\n"
    "v_mfma_f32_16x16x32_bf16 v[204:207], a[224:227], v[48:51], v[148:151]\n"
    "ds_read_b64_tr_b16 v[194:195], v29 offset:28672\n"
    "v_exp_f32 v209, v209\n"
    "v_mfma_f32_16x16x32_bf16 v[204:207], a[228:231], v[52:55], v[204:207]\n"
    "v_cvt_pk_bf16_f32 v214, v208, v209\n"
    "v_add_f32 v220, v220, v209\n"
    "v_mfma_f32_16x16x32_bf16 v[204:207], a[232:235], v[56:59], v[204:207]\n"
    "ds_read_b64_tr_b16 v[196:197], v30 offset:24576\n"
    "v_exp_f32 v210, v210\n"
    "v_mfma_f32_16x16x32_bf16 v[204:207], a[236:239], v[60:63], v[204:207]\n"
    "ds_read_b64_tr_b16 v[198:199], v30 offset:28672\n"
    "v_add_f32 v220, v220, v210\n"
    "v_mfma_f32_16x16x32_bf16 a[48:51], v[172:175], v[12:15], a[48:51]\n"
    "ds_read_b64_tr_b16 v[200:201], v31 offset:24576\n"
    "v_exp_f32 v211, v211\n"
    "v_mfma_f32_16x16x32_bf16 a[52:55], v[176:179], v[12:15], a[52:55]\n"
    "s_add_u32 s77, s77, s78\n"
    "v_cvt_pk_bf16_f32 v215, v210, v211\n"
    "v_add_f32 v220, v220, v211\n"
    "v_mfma_f32_16x16x32_bf16 a[104:107], v[180:183], v[12:15], a[104:107]\n"
    "ds_read_b64_tr_b16 v[202:203], v31 offset:28672\n"
    "v_exp_f32 v204, v204\n"
    "v_mfma_f32_16x16x32_bf16 a[108:111], v[184:187], v[12:15], a[108:111]\n"
    "ds_read_b64_tr_b16 v[172:173], v24 offset:24576\n"
    "v_add_f32 v221, v221, v204\n"
    "v_mfma_f32_16x16x32_bf16 v[208:211], a[240:243], v[48:51], v[148:151]\n"
    "ds_read_b64_tr_b16 v[174:175], v24 offset:28672\n"
    "v_exp_f32 v205, v205\n"
    "v_mfma_f32_16x16x32_bf16 v[208:211], a[244:247], v[52:55], v[208:211]\n"
    "v_cvt_pk_bf16_f32 v216, v204, v205\n"
    "v_add_f32 v221, v221, v205\n"
    "v_mfma_f32_16x16x32_bf16 v[208:211], a[248:251], v[56:59], v[208:211]\n"
    "ds_read_b64_tr_b16 v[176:177], v25 offset:24576\n"
    "v_exp_f32 v206, v206\n"
    "v_mfma_f32_16x16x32_bf16 v[208:211], a[252:255], v[60:63], v[208:211]\n"
    "s_waitcnt lgkmcnt(9)\n"
    "ds_read_b64_tr_b16 v[178:179], v25 offset:28672\n"
    "v_add_f32 v221, v221, v206\n"
    "v_mfma_f32_16x16x32_bf16 a[112:115], v[188:191], v[212:215], a[112:115]\n"
    "s_waitcnt lgkmcnt(8)\n"
    "ds_read_b64_tr_b16 v[180:181], v26 offset:24576\n"
    "v_exp_f32 v207, v207\n"
    "v_mfma_f32_16x16x32_bf16 a[116:119], v[192:195], v[212:215], a[116:119]\n"
    "s_waitcnt lgkmcnt(7)\n"
    "v_cvt_pk_bf16_f32 v217, v206, v207\n"
    "v_add_f32 v221, v221, v207\n"
    "v_mfma_f32_16x16x32_bf16 a[168:171], v[196:199], v[212:215], a[168:171]\n"
    "s_waitcnt lgkmcnt(5)\n"
    "ds_read_b64_tr_b16 v[182:183], v26 offset:28672\n"
    "v_exp_f32 v208, v208\n"
    "v_mfma_f32_16x16x32_bf16 a[172:175], v[200:203], v[212:215], a[172:175]\n"
    "ds_read_b64_tr_b16 v[184:185], v27 offset:24576\n"
    "v_add_f32 v221, v221, v208\n"
    "v_mfma_f32_16x16x32_bf16 v[204:207], a[224:227], v[64:67], v[152:155]\n"
    "ds_read_b64_tr_b16 v[186:187], v27 offset:28672\n"
    "v_exp_f32 v209, v209\n"
    "v_mfma_f32_16x16x32_bf16 v[204:207], a[228:231], v[68:71], v[204:207]\n"
    "v_cvt_pk_bf16_f32 v218, v208, v209\n"
    "v_add_f32 v221, v221, v209\n"
    "v_mfma_f32_16x16x32_bf16 v[204:207], a[232:235], v[72:75], v[204:207]\n"
    "s_memrealtime s[36:37]\n"
    "v_exp_f32 v210, v210\n"
    "v_mfma_f32_16x16x32_bf16 v[204:207], a[236:239], v[76:79], v[204:207]\n"
    "s_waitcnt lgkmcnt(6)\n"
    "v_add_f32 v221, v221, v210\n"
    "v_mfma_f32_16x16x32_bf16 a[0:3], v[172:175], v[212:215], a[0:3]\n"
    "s_waitcnt lgkmcnt(4)\n"
    "v_exp_f32 v211, v211\n"
    "v_mfma_f32_16x16x32_bf16 a[4:7], v[176:179], v[212:215], a[4:7]\n"
    "s_waitcnt lgkmcnt(2)\n"
    "v_cvt_pk_bf16_f32 v219, v210, v211\n"
    "v_add_f32 v221, v221, v211\n"
    "v_mfma_f32_16x16x32_bf16 a[56:59], v[180:183], v[212:215], a[56:59]\n"
    "s_waitcnt lgkmcnt(0)\n"
    "v_exp_f32 v204, v204\n"
    "v_mfma_f32_16x16x32_bf16 a[60:63], v[184:187], v[212:215], a[60:63]\n"
    "s_barrier\n"
    "v_add_f32 v222, v222, v204\n"
    "v_mfma_f32_16x16x32_bf16 v[208:211], a[240:243], v[64:67], v[152:155]\n"
    "v_exp_f32 v205, v205\n"
    "v_mfma_f32_16x16x32_bf16 v[208:211], a[244:247], v[68:71], v[208:211]\n"
    "v_cvt_pk_bf16_f32 v212, v204, v205\n"
    "v_add_f32 v222, v222, v205\n"
    "v_mfma_f32_16x16x32_bf16 v[208:211], a[248:251], v[72:75], v[208:211]\n"
    "s_add_u32 m0, s60, s73\n"
    "v_exp_f32 v206, v206\n"
    "v_mfma_f32_16x16x32_bf16 v[208:211], a[252:255], v[76:79], v[208:211]\n"
    "buffer_load_dwordx4 v5, s[48:51], s72 offen offset:0 nt lds\n"
    "v_mfma_f32_16x16x32_bf16 a[120:123], v[188:191], v[216:219], a[120:123]\n"
    "s_sub_u32 s36, s36, s77\n"
    "v_exp_f32 v207, v207\n"
    "v_mfma_f32_16x16x32_bf16 a[124:127], v[192:195], v[216:219], a[124:127]\n"
    "s_cmp_lt_i32 s36, 0\n"
    "v_cvt_pk_bf16_f32 v213, v206, v207\n"
    "v_add_f32 v222, v222, v207\n"
    "v_mfma_f32_16x16x32_bf16 a[176:179], v[196:199], v[216:219], a[176:179]\n"
    "s_cbranch_scc1 LPW3_%=\n"
    "LPR3_%=:\n"
    "v_exp_f32 v208, v208\n"
    "v_mfma_f32_16x16x32_bf16 a[180:183], v[200:203], v[216:219], a[180:183]\n"
    "v_add_f32 v222, v222, v206\n"
    "v_add_f32 v222, v222, v208\n"
    "v_mfma_f32_16x16x32_bf16 v[204:207], a[224:227], v[80:83], v[156:159]\n"
    "v_exp_f32 v209, v209\n"
    "v_mfma_f32_16x16x32_bf16 v[204:207], a[228:231], v[84:87], v[204:207]\n"
    "v_cvt_pk_bf16_f32 v214, v208, v209\n"
    "v_add_f32 v222, v222, v209\n"
    "v_mfma_f32_16x16x32_bf16 v[204:207], a[232:235], v[88:91], v[204:207]\n"
    "v_exp_f32 v210, v210\n"
    "v_mfma_f32_16x16x32_bf16 v[204:207], a[236:239], v[92:95], v[204:207]\n"
    "buffer_load_dwordx4 v6, s[48:51], s72 offen offset:1024 nt lds\n"
    "v_mfma_f32_16x16x32_bf16 a[8:11], v[172:175], v[216:219], a[8:11]\n"
    "v_exp_f32 v211, v211\n"
    "v_mfma_f32_16x16x32_bf16 a[12:15], v[176:179], v[216:219], a[12:15]\n"
    "v_cvt_pk_bf16_f32 v215, v210, v211\n"
    "v_add_f32 v222, v222, v211\n"
    "v_mfma_f32_16x16x32_bf16 a[64:67], v[180:183], v[216:219], a[64:67]\n"
    "v_exp_f32 v204, v204\n"
    "v_mfma_f32_16x16x32_bf16 a[68:71], v[184:187], v[216:219], a[68:71]\n"
    "v_add_f32 v222, v222, v210\n"
    "v_add_f32 v223, v223, v204\n"
    "v_mfma_f32_16x16x32_bf16 v[208:211], a[240:243], v[80:83], v[156:159]\n"
    "v_exp_f32 v205, v205\n"
    "v_mfma_f32_16x16x32_bf16 v[208:211], a[244:247], v[84:87], v[208:211]\n"
    "v_cvt_pk_bf16_f32 v216, v204, v205\n"
    "v_add_f32 v223, v223, v205\n"
    "v_mfma_f32_16x16x32_bf16 v[208:211], a[248:251], v[88:91], v[208:211]\n"
    "v_exp_f32 v206, v206\n"
    "v_mfma_f32_16x16x32_bf16 v[208:211], a[252:255], v[92:95], v[208:211]\n"
    "buffer_load_dwordx4 v7, s[48:51], s72 offen offset:2048 nt lds\n"
    "v_mfma_f32_16x16x32_bf16 a[128:131], v[188:191], v[212:215], a[128:131]\n"
    "v_exp_f32 v207, v207\n"
    "v_mfma_f32_16x16x32_bf16 a[132:135], v[192:195], v[212:215], a[132:135]\n"
    "v_cvt_pk_bf16_f32 v217, v206, v207\n"
    "v_add_f32 v223, v223, v207\n"
    "v_mfma_f32_16x16x32_bf16 a[184:187], v[196:199], v[212:215], a[184:187]\n"
    "v_exp_f32 v208, v208\n"
    "v_mfma_f32_16x16x32_bf16 a[188:191], v[200:203], v[212:215], a[188:191]\n"
    "v_add_f32 v223, v223, v206\n"
    "v_add_f32 v223, v223, v208\n"
    "v_mfma_f32_16x16x32_bf16 v[204:207], a[224:227], v[96:99], v[160:163]\n"
    "v_exp_f32 v209, v209\n"
    "v_mfma_f32_16x16x32_bf16 v[204:207], a[228:231], v[100:103], v[204:207]\n"
    "v_cvt_pk_bf16_f32 v218, v208, v209\n"
    "v_add_f32 v223, v223, v209\n"
    "v_mfma_f32_16x16x32_bf16 v[204:207], a[232:235], v[104:107], v[204:207]\n"
    "v_exp_f32 v210, v210\n"
    "v_mfma_f32_16x16x32_bf16 v[204:207], a[236:239], v[108:111], v[204:207]\n"
    "buffer_load_dwordx4 v8, s[48:51], s72 offen offset:3072 lds\n"
    "v_mfma_f32_16x16x32_bf16 a[16:19], v[172:175], v[212:215], a[16:19]\n"
    "s_add_u32 s72, s72, 4096\n"
    "v_exp_f32 v211, v211\n"
    "v_mfma_f32_16x16x32_bf16 a[20:23], v[176:179], v[212:215], a[20:23]\n"
    "s_and_b32 s72, s72, 131071\n"
    "v_cvt_pk_bf16_f32 v219, v210, v211\n"
    "v_add_f32 v223, v223, v211\n"
    "v_mfma_f32_16x16x32_bf16 a[72:75], v[180:183], v[212:215], a[72:75]\n"
    "s_add_u32 s73, s73, 4096\n"
    "v_exp_f32 v204, v204\n"
    "v_mfma_f32_16x16x32_bf16 a[76:79], v[184:187], v[212:215], a[76:79]\n"
    "s_sub_u32 s74, s73, 40960\n"
    "v_add_f32 v223, v223, v210\n"
    "v_add_f32 v224, v224, v204\n"
    "v_mfma_f32_16x16x32_bf16 v[208:211], a[240:243], v[96:99], v[160:163]\n"
    "s_min_u32 s73, s73, s74\n"
    "v_exp_f32 v205, v205\n"
    "v_mfma_f32_16x16x32_bf16 v[208:211], a[244:247], v[100:103], v[208:211]\n"
    "s_cmp_eq_u32 s76, 1\n"
    "s_cselect_b32 s50, 0, s50\n"
    "s_cselect_b32 s78, 0, s78\n"
    "v_cvt_pk_bf16_f32 v212, v204, v205\n"
    "v_add_f32 v224, v224, v205\n"
    "v_mfma_f32_16x16x32_bf16 v[208:211], a[248:251], v[104:107], v[208:211]\n"
    "s_add_u32 m0, s60, s73\n"
    "v_exp_f32 v206, v206\n"
    "v_mfma_f32_16x16x32_bf16 v[208:211], a[252:255], v[108:111], v[208:211]\n"
    "buffer_load_dwordx4 v5, s[48:51], s72 offen offset:0 nt lds\n"
    "v_mfma_f32_16x16x32_bf16 a[136:139], v[188:191], v[216:219], a[136:139]\n"
    "v_exp_f32 v207, v207\n"
    "v_mfma_f32_16x16x32_bf16 a[140:143], v[192:195], v[216:219], a[140:143]\n"
    "v_cvt_pk_bf16_f32 v213, v206, v207\n"
    "v_add_f32 v224, v224, v207\n"
    "v_mfma_f32_16x16x32_bf16 a[192:195], v[196:199], v[216:219], a[192:195]\n"
    "v_exp_f32 v208, v208\n"
    "v_mfma_f32_16x16x32_bf16 a[196:199], v[200:203], v[216:219], a[196:199]\n"
    "v_add_f32 v224, v224, v206\n"
    "v_add_f32 v224, v224, v208\n"
    "v_mfma_f32_16x16x32_bf16 v[204:207], a[224:227], v[112:115], v[164:167]\n"
    "v_exp_f32 v209, v209\n"
    "v_mfma_f32_16x16x32_bf16 v[204:207], a[228:231], v[116:119], v[204:207]\n"
    "v_cvt_pk_bf16_f32 v214, v208, v209\n"
    "v_add_f32 v224, v224, v209\n"
    "v_mfma_f32_16x16x32_bf16 v[204:207], a[232:235], v[120:123], v[204:207]\n"
    "v_exp_f32 v210, v210\n"
    "v_mfma_f32_16x16x32_bf16 v[204:207], a[236:239], v[124:127], v[204:207]\n"
    "buffer_load_dwordx4 v6, s[48:51], s72 offen offset:1024 nt lds\n"
    "v_mfma_f32_16x16x32_bf16 a[24:27], v[172:175], v[216:219], a[24:27]\n"
    "v_exp_f32 v211, v211\n"
    "v_mfma_f32_16x16x32_bf16 a[28:31], v[176:179], v[216:219], a[28:31]\n"
    "v_cvt_pk_bf16_f32 v215, v210, v211\n"
    "v_add_f32 v224, v224, v211\n"
    "v_mfma_f32_16x16x32_bf16 a[80:83], v[180:183], v[216:219], a[80:83]\n"
    "v_exp_f32 v204, v204\n"
    "v_mfma_f32_16x16x32_bf16 a[84:87], v[184:187], v[216:219], a[84:87]\n"
    "v_add_f32 v224, v224, v210\n"
    "v_add_f32 v225, v225, v204\n"
    "v_mfma_f32_16x16x32_bf16 v[208:211], a[240:243], v[112:115], v[164:167]\n"
    "v_exp_f32 v205, v205\n"
    "v_mfma_f32_16x16x32_bf16 v[208:211], a[244:247], v[116:119], v[208:211]\n"
    "v_cvt_pk_bf16_f32 v216, v204, v205\n"
    "v_add_f32 v225, v225, v205\n"
    "v_mfma_f32_16x16x32_bf16 v[208:211], a[248:251], v[120:123], v[208:211]\n"
    "s_waitcnt vmcnt(10)\n"
    "v_exp_f32 v206, v206\n"
    "v_mfma_f32_16x16x32_bf16 v[208:211], a[252:255], v[124:127], v[208:211]\n"
    "s_barrier\n"
    "v_mfma_f32_16x16x32_bf16 a[144:147], v[188:191], v[212:215], a[144:147]\n"
    "v_exp_f32 v207, v207\n"
    "v_mfma_f32_16x16x32_bf16 a[148:151], v[192:195], v[212:215], a[148:151]\n"
    "v_cvt_pk_bf16_f32 v217, v206, v207\n"
    "v_add_f32 v225, v225, v207\n"
    "v_mfma_f32_16x16x32_bf16 a[200:203], v[196:199], v[212:215], a[200:203]\n"
    "v_exp_f32 v208, v208\n"
    "v_mfma_f32_16x16x32_bf16 a[204:207], v[200:203], v[212:215], a[204:207]\n"
    "v_add_f32 v225, v225, v206\n"
    "v_add_f32 v225, v225, v208\n"
    "v_mfma_f32_16x16x32_bf16 v[204:207], a[224:227], v[128:131], v[168:171]\n"
    "v_exp_f32 v209, v209\n"
    "v_mfma_f32_16x16x32_bf16 v[204:207], a[228:231], v[132:135], v[204:207]\n"
    "v_cvt_pk_bf16_f32 v218, v208, v209\n"
    "v_add_f32 v225, v225, v209\n"
    "v_mfma_f32_16x16x32_bf16 v[204:207], a[232:235], v[136:139], v[204:207]\n"
    "v_exp_f32 v210, v210\n"
    "v_mfma_f32_16x16x32_bf16 v[204:207], a[236:239], v[140:143], v[204:207]\n"
    "ds_read_b128 a[224:227], v20 offset:32768\n"
    "v_add_f32 v225, v225, v210\n"
    "v_mfma_f32_16x16x32_bf16 a[32:35], v[172:175], v[212:215], a[32:35]\n"
    "ds_read_b128 a[228:231], v21 offset:32768\n"
    "v_exp_f32 v211, v211\n"
    "v_mfma_f32_16x16x32_bf16 a[36:39], v[176:179], v[212:215], a[36:39]\n"
    "v_cvt_pk_bf16_f32 v219, v210, v211\n"
    "v_add_f32 v225, v225, v211\n"
    "v_mfma_f32_16x16x32_bf16 a[88:91], v[180:183], v[212:215], a[88:91]\n"
    "ds_read_b128 a[232:235], v22 offset:32768\n"
    "v_exp_f32 v204, v204\n"
    "v_mfma_f32_16x16x32_bf16 a[92:95], v[184:187], v[212:215], a[92:95]\n"
    "ds_read_b128 a[236:239], v23 offset:32768\n"
    "v_add_f32 v226, v226, v204\n"
    "v_mfma_f32_16x16x32_bf16 v[208:211], a[240:243], v[128:131], v[168:171]\n"
    "v_exp_f32 v205, v205\n"
    "v_mfma_f32_16x16x32_bf16 v[208:211], a[244:247], v[132:135], v[208:211]\n"
    "v_cvt_pk_bf16_f32 v12, v204, v205\n"
    "v_add_f32 v226, v226, v205\n"
    "v_mfma_f32_16x16x32_bf16 v[208:211], a[248:251], v[136:139], v[208:211]\n"
    "v_exp_f32 v206, v206\n"
    "v_mfma_f32_16x16x32_bf16 v[208:211], a[252:255], v[140:143], v[208:211]\n"
    "ds_read_b128 a[240:243], v20 offset:36864\n"
    "v_add_f32 v226, v226, v206\n"
    "v_mfma_f32_16x16x32_bf16 a[152:155], v[188:191], v[216:219], a[152:155]\n"
    "ds_read_b128 a[244:247], v21 offset:36864\n"
    "v_exp_f32 v207, v207\n"
    "v_mfma_f32_16x16x32_bf16 a[156:159], v[192:195], v[216:219], a[156:159]\n"
    "v_cvt_pk_bf16_f32 v13, v206, v207\n"
    "v_add_f32 v226, v226, v207\n"
    "v_mfma_f32_16x16x32_bf16 a[208:211], v[196:199], v[216:219], a[208:211]\n"
    "v_exp_f32 v208, v208\n"
    "v_mfma_f32_16x16x32_bf16 a[212:215], v[200:203], v[216:219], a[212:215]\n"
    "s_waitcnt lgkmcnt(5)\n"
    "ds_read_b128 a[248:251], v22 offset:36864\n"
    "v_add_f32 v226, v226, v208\n"
    "v_mfma_f32_16x16x32_bf16 v[204:207], a[224:227], v[32:35], v[144:147]\n"
    "s_waitcnt lgkmcnt(5)\n"
    "v_exp_f32 v209, v209\n"
    "v_mfma_f32_16x16x32_bf16 v[204:207], a[228:231], v[36:39], v[204:207]\n"
    "s_waitcnt lgkmcnt(4)\n"
    "v_cvt_pk_bf16_f32 v14, v208, v209\n"
    "v_add_f32 v226, v226, v209\n"
    "v_mfma_f32_16x16x32_bf16 v[204:207], a[232:235], v[40:43], v[204:207]\n"
    "s_waitcnt lgkmcnt(3)\n"
    "v_exp_f32 v210, v210\n"
    "v_mfma_f32_16x16x32_bf16 v[204:207], a[236:239], v[44:47], v[204:207]\n"
    "ds_read_b128 a[252:255], v23 offset:36864\n"
    "v_add_f32 v226, v226, v210\n"
    "v_mfma_f32_16x16x32_bf16 a[40:43], v[172:175], v[216:219], a[40:43]\n"
    "v_exp_f32 v211, v211\n"
    "v_mfma_f32_16x16x32_bf16 a[44:47], v[176:179], v[216:219], a[44:47]\n"
    "v_cvt_pk_bf16_f32 v15, v210, v211\n"
    "v_add_f32 v226, v226, v211\n"
    "v_mfma_f32_16x16x32_bf16 a[96:99], v[180:183], v[216:219], a[96:99]\n"
    "v_exp_f32 v204, v204\n"
    "v_mfma_f32_16x16x32_bf16 a[100:103], v[184:187], v[216:219], a[100:103]\n"
    "s_waitcnt lgkmcnt(3)\n"
    "buffer_load_dwordx4 v7, s[48:51], s72 offen offset:2048 nt lds\n"
    "v_mfma_f32_16x16x32_bf16 v[208:211], a[240:243], v[32:35], v[144:147]\n"
    "s_waitcnt lgkmcnt(2)\n"
    "v_exp_f32 v205, v205\n"
    "v_mfma_f32_16x16x32_bf16 v[208:211], a[244:247], v[36:39], v[208:211]\n"
    "s_waitcnt lgkmcnt(1)\n"
    "v_cvt_pk_bf16_f32 v212, v204, v205\n"
    "v_add_f32 v220, v220, v205\n"
    "v_mfma_f32_16x16x32_bf16 v[208:211], a[248:251], v[40:43], v[208:211]\n"
    "s_waitcnt lgkmcnt(0)\n"
    "v_exp_f32 v206, v206\n"
    "v_mfma_f32_16x16x32_bf16 v[208:211], a[252:255], v[44:47], v[208:211]\n"
    "v_add_f32 v220, v220, v204\n"
    "v_add_f32 v220, v220, v206\n"
    "v_mfma_f32_16x16x32_bf16 a[160:163], v[188:191], v[12:15], a[160:163]\n"
    "ds_read_b64_tr_b16 v[188:189], v28 offset:32768\n"
    "v_exp_f32 v207, v207\n"
    "v_mfma_f32_16x16x32_bf16 a[164:167], v[192:195], v[12:15], a[164:167]\n"
    "v_cvt_pk_bf16_f32 v213, v206, v207\n"
    "v_add_f32 v220, v220, v207\n"
    "v_mfma_f32_16x16x32_bf16 a[216:219], v[196:199], v[12:15], a[216:219]\n"
    "ds_read_b64_tr_b16 v[190:191], v28 offset:36864\n"
    "v_exp_f32 v208, v208\n"
    "v_mfma_f32_16x16x32_bf16 a[220:223], v[200:203], v[12:15], a[220:223]\n"
    "ds_read_b64_tr_b16 v[192:193], v29 offset:32768\n"
    "v_add_f32 v220, v220, v208\n"
    "v_mfma_f32_16x16x32_bf16 v[204:207], a[224:227], v[48:51], v[148:151]\n"
    "ds_read_b64_tr_b16 v[194:195], v29 offset:36864\n"
    "v_exp_f32 v209, v209\n"
    "v_mfma_f32_16x16x32_bf16 v[204:207], a[228:231], v[52:55], v[204:207]\n"
    "v_cvt_pk_bf16_f32 v214, v208, v209\n"
    "v_add_f32 v220, v220, v209\n"
    "v_mfma_f32_16x16x32_bf16 v[204:207], a[232:235], v[56:59], v[204:207]\n"
    "ds_read_b64_tr_b16 v[196:197], v30 offset:32768\n"
    "v_exp_f32 v210, v210\n"
    "v_mfma_f32_16x16x32_bf16 v[204:207], a[236:239], v[60:63], v[204:207]\n"
    "ds_read_b64_tr_b16 v[198:199], v30 offset:36864\n"
    "v_add_f32 v220, v220, v210\n"
    "v_mfma_f32_16x16x32_bf16 a[48:51], v[172:175], v[12:15], a[48:51]\n"
    "ds_read_b64_tr_b16 v[200:201], v31 offset:32768\n"
    "v_exp_f32 v211, v211\n"
    "v_mfma_f32_16x16x32_bf16 a[52:55], v[176:179], v[12:15], a[52:55]\n"
    "s_add_u32 s77, s77, s78\n"
    "v_cvt_pk_bf16_f32 v215, v210, v211\n"
    "v_add_f32 v220, v220, v211\n"
    "v_mfma_f32_16x16x32_bf16 a[104:107], v[180:183], v[12:15], a[104:107]\n"
    "ds_read_b64_tr_b16 v[202:203], v31 offset:36864\n"
    "v_exp_f32 v204, v204\n"
    "v_mfma_f32_16x16x32_bf16 a[108:111], v[184:187], v[12:15], a[108:111]\n"
    "ds_read_b64_tr_b16 v[172:173], v24 offset:32768\n"
    "v_add_f32 v221, v221, v204\n"
    "v_mfma_f32_16x16x32_bf16 v[208:211], a[240:243], v[48:51], v[148:151]\n"
    "ds_read_b64_tr_b16 v[174:175], v24 offset:36864\n"
    "v_exp_f32 v205, v205\n"
    "v_mfma_f32_16x16x32_bf16 v[208:211], a[244:247], v[52:55], v[208:211]\n"
    "v_cvt_pk_bf16_f32 v216, v204, v205\n"
    "v_add_f32 v221, v221, v205\n"
    "v_mfma_f32_16x16x32_bf16 v[208:211], a[248:251], v[56:59], v[208:211]\n"
    "ds_read_b64_tr_b16 v[176:177], v25 offset:32768\n"
    "v_exp_f32 v206, v206\n"
    "v_mfma_f32_16x16x32_bf16 v[208:211], a[252:255], v[60:63], v[208:211]\n"
    "s_waitcnt lgkmcnt(9)\n"
    "ds_read_b64_tr_b16 v[178:179], v25 offset:36864\n"
    "v_add_f32 v221, v221, v206\n"
    "v_mfma_f32_16x16x32_bf16 a[112:115], v[188:191], v[212:215], a[112:115]\n"
    "s_waitcnt lgkmcnt(8)\n"
    "ds_read_b64_tr_b16 v[180:181], v26 offset:32768\n"
    "v_exp_f32 v207, v207\n"
    "v_mfma_f32_16x16x32_bf16 a[116:119], v[192:195], v[212:215], a[116:119]\n"
    "s_waitcnt lgkmcnt(7)\n"
    "v_cvt_pk_bf16_f32 v217, v206, v207\n"
    "v_add_f32 v221, v221, v207\n"
    "v_mfma_f32_16x16x32_bf16 a[168:171], v[196:199], v[212:215], a[168:171]\n"
    "s_waitcnt lgkmcnt(5)\n"
    "ds_read_b64_tr_b16 v[182:183], v26 offset:36864\n"
    "v_exp_f32 v208, v208\n"
    "v_mfma_f32_16x16x32_bf16 a[172:175], v[200:203], v[212:215], a[172:175]\n"
    "ds_read_b64_tr_b16 v[184:185], v27 offset:32768\n"
    "v_add_f32 v221, v221, v208\n"
    "v_mfma_f32_16x16x32_bf16 v[204:207], a[224:227], v[64:67], v[152:155]\n"
    "ds_read_b64_tr_b16 v[186:187], v27 offset:36864\n"
    "v_exp_f32 v209, v209\n"
    "v_mfma_f32_16x16x32_bf16 v[204:207], a[228:231], v[68:71], v[204:207]\n"
    "v_cvt_pk_bf16_f32 v218, v208, v209\n"
    "v_add_f32 v221, v221, v209\n"
    "v_mfma_f32_16x16x32_bf16 v[204:207], a[232:235], v[72:75], v[204:207]\n"
    "s_memrealtime s[36:37]\n"
    "v_exp_f32 v210, v210\n"
    "v_mfma_f32_16x16x32_bf16 v[204:207], a[236:239], v[76:79], v[204:207]\n"
    "s_waitcnt lgkmcnt(6)\n"
    "buffer_load_dword v254, v229, s[8:11], s71 offen offset:0\n"
    "v_mfma_f32_16x16x32_bf16 a[0:3], v[172:175], v[212:215], a[0:3]\n"
    "s_waitcnt lgkmcnt(4)\n"
    "v_exp_f32 v211, v211\n"
    "v_mfma_f32_16x16x32_bf16 a[4:7], v[176:179], v[212:215], a[4:7]\n"
    "s_waitcnt lgkmcnt(2)\n"
    "v_cvt_pk_bf16_f32 v219, v210, v211\n"
    "v_add_f32 v221, v221, v211\n"
    "v_mfma_f32_16x16x32_bf16 a[56:59], v[180:183], v[212:215], a[56:59]\n"
    "s_waitcnt lgkmcnt(0)\n"
    "v_exp_f32 v204, v204\n"
    "v_mfma_f32_16x16x32_bf16 a[60:63], v[184:187], v[212:215], a[60:63]\n"
    "s_add_u32 s71, s71, 4608\n"
    "v_add_f32 v221, v221, v210\n"
    "v_add_f32 v222, v222, v204\n"
    "v_mfma_f32_16x16x32_bf16 v[208:211], a[240:243], v[64:67], v[152:155]\n"
    "v_exp_f32 v205, v205\n"
    "v_mfma_f32_16x16x32_bf16 v[208:211], a[244:247], v[68:71], v[208:211]\n"
    "v_cvt_pk_bf16_f32 v212, v204, v205\n"
    "v_add_f32 v222, v222, v205\n"
    "v_mfma_f32_16x16x32_bf16 v[208:211], a[248:251], v[72:75], v[208:211]\n"
    "v_exp_f32 v206, v206\n"
    "v_mfma_f32_16x16x32_bf16 v[208:211], a[252:255], v[76:79], v[208:211]\n"
    "buffer_load_dwordx4 v8, s[48:51], s72 offen offset:3072 lds\n"
    "v_mfma_f32_16x16x32_bf16 a[120:123], v[188:191], v[216:219], a[120:123]\n"
    "s_sub_u32 s36, s36, s77\n"
    "v_exp_f32 v207, v207\n"
    "v_mfma_f32_16x16x32_bf16 a[124:127], v[192:195], v[216:219], a[124:127]\n"
    "s_cmp_lt_i32 s36, 0\n"
    "v_cvt_pk_bf16_f32 v213, v206, v207\n"
    "v_add_f32 v222, v222, v207\n"
    "v_mfma_f32_16x16x32_bf16 a[176:179], v[196:199], v[216:219], a[176:179]\n"
    "s_cbranch_scc1 LPW4_%=\n"
    "LPR4_%=:\n"
    "v_exp_f32 v208, v208\n"
    "v_mfma_f32_16x16x32_bf16 a[180:183], v[200:203], v[216:219], a[180:183]\n"
    "s_add_u32 s72, s72, 4096\n"
    "v_add_f32 v222, v222, v206\n"
    "v_add_f32 v222, v222, v208\n"
    "v_mfma_f32_16x16x32_bf16 v[204:207], a[224:227], v[80:83], v[156:159]\n"
    "s_and_b32 s72, s72, 131071\n"
    "v_exp_f32 v209, v209\n"
    "v_mfma_f32_16x16x32_bf16 v[204:207], a[228:231], v[84:87], v[204:207]\n"
    "s_add_u32 s73, s73, 4096\n"
    "v_cvt_pk_bf16_f32 v214, v208, v209\n"
    "v_add_f32 v222, v222, v209\n"
    "v_mfma_f32_16x16x32_bf16 v[204:207], a[232:235], v[88:91], v[204:207]\n"
    "s_sub_u32 s74, s73, 40960\n"
    "v_exp_f32 v210, v210\n"
    "v_mfma_f32_16x16x32_bf16 v[204:207], a[236:239], v[92:95], v[204:207]\n"
    "s_min_u32 s73, s73, s74\n"
    "buffer_load_dword v255, v229, s[8:11], s71 offen offset:0\n"
    "v_mfma_f32_16x16x32_bf16 a[8:11], v[172:175], v[216:219], a[8:11]\n"
    "v_exp_f32 v211, v211\n"
    "v_mfma_f32_16x16x32_bf16 a[12:15], v[176:179], v[216:219], a[12:15]\n"
    "v_cvt_pk_bf16_f32 v215, v210, v211\n"
    "v_add_f32 v222, v222, v211\n"
    "v_mfma_f32_16x16x32_bf16 a[64:67], v[180:183], v[216:219], a[64:67]\n"
    "v_exp_f32 v204, v204\n"
    "v_mfma_f32_16x16x32_bf16 a[68:71], v[184:187], v[216:219], a[68:71]\n"
    "v_add_f32 v222, v222, v210\n"
    "v_add_f32 v223, v223, v204\n"
    "v_mfma_f32_16x16x32_bf16 v[208:211], a[240:243], v[80:83], v[156:159]\n"
    "v_exp_f32 v205, v205\n"
    "v_mfma_f32_16x16x32_bf16 v[208:211], a[244:247], v[84:87], v[208:211]\n"
    "v_cvt_pk_bf16_f32 v216, v204, v205\n"
    "v_add_f32 v223, v223, v205\n"
    "v_mfma_f32_16x16x32_bf16 v[208:211], a[248:251], v[88:91], v[208:211]\n"
    "s_add_u32 m0, s60, s73\n"
    "v_exp_f32 v206, v206\n"
    "v_mfma_f32_16x16x32_bf16 v[208:211], a[252:255], v[92:95], v[208:211]\n"
    "buffer_load_dwordx4 v5, s[48:51], s72 offen offset:0 nt lds\n"
    "v_mfma_f32_16x16x32_bf16 a[128:131], v[188:191], v[212:215], a[128:131]\n"
    "v_exp_f32 v207, v207\n"
    "v_mfma_f32_16x16x32_bf16 a[132:135], v[192:195], v[212:215], a[132:135]\n"
    "v_cvt_pk_bf16_f32 v217, v206, v207\n"
    "v_add_f32 v223, v223, v207\n"
    "v_mfma_f32_16x16x32_bf16 a[184:187], v[196:199], v[212:215], a[184:187]\n"
    "v_exp_f32 v208, v208\n"
    "v_mfma_f32_16x16x32_bf16 a[188:191], v[200:203], v[212:215], a[188:191]\n"
    "v_add_f32 v223, v223, v206\n"
    "v_add_f32 v223, v223, v208\n"
    "v_mfma_f32_16x16x32_bf16 v[204:207], a[224:227], v[96:99], v[160:163]\n"
    "v_exp_f32 v209, v209\n"
    "v_mfma_f32_16x16x32_bf16 v[204:207], a[228:231], v[100:103], v[204:207]\n"
    "v_cvt_pk_bf16_f32 v218, v208, v209\n"
    "v_add_f32 v223, v223, v209\n"
    "v_mfma_f32_16x16x32_bf16 v[204:207], a[232:235], v[104:107], v[204:207]\n"
    "v_exp_f32 v210, v210\n"
    "v_mfma_f32_16x16x32_bf16 v[204:207], a[236:239], v[108:111], v[204:207]\n"
    "buffer_load_dwordx4 v6, s[48:51], s72 offen offset:1024 nt lds\n"
    "v_mfma_f32_16x16x32_bf16 a[16:19], v[172:175], v[212:215], a[16:19]\n"
    "v_exp_f32 v211, v211\n"
    "v_mfma_f32_16x16x32_bf16 a[20:23], v[176:179], v[212:215], a[20:23]\n"
    "v_cvt_pk_bf16_f32 v219, v210, v211\n"
    "v_add_f32 v223, v223, v211\n"
    "v_mfma_f32_16x16x32_bf16 a[72:75], v[180:183], v[212:215], a[72:75]\n"
    "v_exp_f32 v204, v204\n"
    "v_mfma_f32_16x16x32_bf16 a[76:79], v[184:187], v[212:215], a[76:79]\n"
    "v_add_f32 v223, v223, v210\n"
    "v_add_f32 v224, v224, v204\n"
    "v_mfma_f32_16x16x32_bf16 v[208:211], a[240:243], v[96:99], v[160:163]\n"
    "v_exp_f32 v205, v205\n"
    "v_mfma_f32_16x16x32_bf16 v[208:211], a[244:247], v[100:103], v[208:211]\n"
    "v_cvt_pk_bf16_f32 v212, v204, v205\n"
    "v_add_f32 v224, v224, v205\n"
    "v_mfma_f32_16x16x32_bf16 v[208:211], a[248:251], v[104:107], v[208:211]\n"
    "v_exp_f32 v206, v206\n"
    "v_mfma_f32_16x16x32_bf16 v[208:211], a[252:255], v[108:111], v[208:211]\n"
    "buffer_load_dwordx4 v7, s[48:51], s72 offen offset:2048 nt lds\n"
    "v_mfma_f32_16x16x32_bf16 a[136:139], v[188:191], v[216:219], a[136:139]\n"
    "v_exp_f32 v207, v207\n"
    "v_mfma_f32_16x16x32_bf16 a[140:143], v[192:195], v[216:219], a[140:143]\n"
    "v_cvt_pk_bf16_f32 v213, v206, v207\n"
    "v_add_f32 v224, v224, v207\n"
    "v_mfma_f32_16x16x32_bf16 a[192:195], v[196:199], v[216:219], a[192:195]\n"
    "v_exp_f32 v208, v208\n"
    "v_mfma_f32_16x16x32_bf16 a[196:199], v[200:203], v[216:219], a[196:199]\n"
    "v_add_f32 v224, v224, v206\n"
    "v_add_f32 v224, v224, v208\n"
    "v_mfma_f32_16x16x32_bf16 v[204:207], a[224:227], v[112:115], v[164:167]\n"
    "v_exp_f32 v209, v209\n"
    "v_mfma_f32_16x16x32_bf16 v[204:207], a[228:231], v[116:119], v[204:207]\n"
    "v_cvt_pk_bf16_f32 v214, v208, v209\n"
    "v_add_f32 v224, v224, v209\n"
    "v_mfma_f32_16x16x32_bf16 v[204:207], a[232:235], v[120:123], v[204:207]\n"
    "v_exp_f32 v210, v210\n"
    "v_mfma_f32_16x16x32_bf16 v[204:207], a[236:239], v[124:127], v[204:207]\n"
    "buffer_load_dwordx4 v8, s[48:51], s72 offen offset:3072 lds\n"
    "v_mfma_f32_16x16x32_bf16 a[24:27], v[172:175], v[216:219], a[24:27]\n"
    "s_add_u32 s72, s72, 4096\n"
    "v_exp_f32 v211, v211\n"
    "v_mfma_f32_16x16x32_bf16 a[28:31], v[176:179], v[216:219], a[28:31]\n"
    "s_and_b32 s72, s72, 131071\n"
    "v_cvt_pk_bf16_f32 v215, v210, v211\n"
    "v_add_f32 v224, v224, v211\n"
    "v_mfma_f32_16x16x32_bf16 a[80:83], v[180:183], v[216:219], a[80:83]\n"
    "s_add_u32 s73, s73, 4096\n"
    "v_exp_f32 v204, v204\n"
    "v_mfma_f32_16x16x32_bf16 a[84:87], v[184:187], v[216:219], a[84:87]\n"
    "s_sub_u32 s74, s73, 40960\n"
    "v_add_f32 v224, v224, v210\n"
    "v_add_f32 v225, v225, v204\n"
    "v_mfma_f32_16x16x32_bf16 v[208:211], a[240:243], v[112:115], v[164:167]\n"
    "s_min_u32 s73, s73, s74\n"
    "v_exp_f32 v205, v205\n"
    "v_mfma_f32_16x16x32_bf16 v[208:211], a[244:247], v[116:119], v[208:211]\n"
    "v_cvt_pk_bf16_f32 v216, v204, v205\n"
    "v_add_f32 v225, v225, v205\n"
    "v_mfma_f32_16x16x32_bf16 v[208:211], a[248:251], v[120:123], v[208:211]\n"
    "s_waitcnt vmcnt(10)\n"
    "v_exp_f32 v206, v206\n"
    "v_mfma_f32_16x16x32_bf16 v[208:211], a[252:255], v[124:127], v[208:211]\n"
    "s_barrier\n"
    "v_mfma_f32_16x16x32_bf16 a[144:147], v[188:191], v[212:215], a[144:147]\n"
    "v_exp_f32 v207, v207\n"
    "v_mfma_f32_16x16x32_bf16 a[148:151], v[192:195], v[212:215], a[148:151]\n"
    "v_cvt_pk_bf16_f32 v217, v206, v207\n"
    "v_add_f32 v225, v225, v207\n"
    "v_mfma_f32_16x16x32_bf16 a[200:203], v[196:199], v[212:215], a[200:203]\n"
    "v_exp_f32 v208, v208\n"
    "v_mfma_f32_16x16x32_bf16 a[204:207], v[200:203], v[212:215], a[204:207]\n"
    "v_add_f32 v225, v225, v206\n"
    "v_add_f32 v225, v225, v208\n"
    "v_mfma_f32_16x16x32_bf16 v[204:207], a[224:227], v[128:131], v[168:171]\n"
    "v_exp_f32 v209, v209\n"
    "v_mfma_f32_16x16x32_bf16 v[204:207], a[228:231], v[132:135], v[204:207]\n"
    "v_cvt_pk_bf16_f32 v218, v208, v209\n"
    "v_add_f32 v225, v225, v209\n"
    "v_mfma_f32_16x16x32_bf16 v[204:207], a[232:235], v[136:139], v[204:207]\n"
    "v_exp_f32 v210, v210\n"
    "v_mfma_f32_16x16x32_bf16 v[204:207], a[236:239], v[140:143], v[204:207]\n"
    "ds_read_b128 a[224:227], v20 offset:0\n"
    "v_add_f32 v225, v225, v210\n"
    "v_mfma_f32_16x16x32_bf16 a[32:35], v[172:175], v[212:215], a[32:35]\n"
    "ds_read_b128 a[228:231], v21 offset:0\n"
    "v_exp_f32 v211, v211\n"
    "v_mfma_f32_16x16x32_bf16 a[36:39], v[176:179], v[212:215], a[36:39]\n"
    "v_cvt_pk_bf16_f32 v219, v210, v211\n"
    "v_add_f32 v225, v225, v211\n"
    "v_mfma_f32_16x16x32_bf16 a[88:91], v[180:183], v[212:215], a[88:91]\n"
    "ds_read_b128 a[232:235], v22 offset:0\n"
    "v_exp_f32 v204, v204\n"
    "v_mfma_f32_16x16x32_bf16 a[92:95], v[184:187], v[212:215], a[92:95]\n"
    "ds_read_b128 a[236:239], v23 offset:0\n"
    "v_add_f32 v226, v226, v204\n"
    "v_mfma_f32_16x16x32_bf16 v[208:211], a[240:243], v[128:131], v[168:171]\n"
    "v_exp_f32 v205, v205\n"
    "v_mfma_f32_16x16x32_bf16 v[208:211], a[244:247], v[132:135], v[208:211]\n"
    "v_cvt_pk_bf16_f32 v12, v204, v205\n"
    "v_add_f32 v226, v226, v205\n"
    "v_mfma_f32_16x16x32_bf16 v[208:211], a[248:251], v[136:139], v[208:211]\n"
    "v_exp_f32 v206, v206\n"
    "v_mfma_f32_16x16x32_bf16 v[208:211], a[252:255], v[140:143], v[208:211]\n"
    "ds_read_b128 a[240:243], v20 offset:4096\n"
    "v_add_f32 v226, v226, v206\n"
    "v_mfma_f32_16x16x32_bf16 a[152:155], v[188:191], v[216:219], a[152:155]\n"
    "ds_read_b128 a[244:247], v21 offset:4096\n"
    "v_exp_f32 v207, v207\n"
    "v_mfma_f32_16x16x32_bf16 a[156:159], v[192:195], v[216:219], a[156:159]\n"
    "v_cvt_pk_bf16_f32 v13, v206, v207\n"
    "v_add_f32 v226, v226, v207\n"
    "v_mfma_f32_16x16x32_bf16 a[208:211], v[196:199], v[216:219], a[208:211]\n"
    "v_exp_f32 v208, v208\n"
    "v_mfma_f32_16x16x32_bf16 a[212:215], v[200:203], v[216:219], a[212:215]\n"
    "s_waitcnt lgkmcnt(5)\n"
    "ds_read_b128 a[248:251], v22 offset:4096\n"
    "v_add_f32 v226, v226, v208\n"
    "v_mfma_f32_16x16x32_bf16 v[204:207], a[224:227], v[32:35], v[144:147]\n"
    "s_waitcnt lgkmcnt(5)\n"
    "v_exp_f32 v209, v209\n"
    "v_mfma_f32_16x16x32_bf16 v[204:207], a[228:231], v[36:39], v[204:207]\n"
    "s_waitcnt lgkmcnt(4)\n"
    "v_cvt_pk_bf16_f32 v14, v208, v209\n"
    "v_add_f32 v226, v226, v209\n"
    "v_mfma_f32_16x16x32_bf16 v[204:207], a[232:235], v[40:43], v[204:207]\n"
    "s_waitcnt lgkmcnt(3)\n"
    "v_exp_f32 v210, v210\n"
    "v_mfma_f32_16x16x32_bf16 v[204:207], a[236:239], v[44:47], v[204:207]\n"
    "ds_read_b128 a[252:255], v23 offset:4096\n"
    "v_add_f32 v226, v226, v210\n"
    "v_mfma_f32_16x16x32_bf16 a[40:43], v[172:175], v[216:219], a[40:43]\n"
    "v_exp_f32 v211, v211\n"
    "v_mfma_f32_16x16x32_bf16 a[44:47], v[176:179], v[216:219], a[44:47]\n"
    "v_cvt_pk_bf16_f32 v15, v210, v211\n"
    "v_add_f32 v226, v226, v211\n"
    "v_mfma_f32_16x16x32_bf16 a[96:99], v[180:183], v[216:219], a[96:99]\n"
    "s_add_u32 m0, s60, s73\n"
    "v_exp_f32 v204, v204\n"
    "v_mfma_f32_16x16x32_bf16 a[100:103], v[184:187], v[216:219], a[100:103]\n"
    "s_waitcnt lgkmcnt(3)\n"
    "buffer_load_dwordx4 v5, s[48:51], s72 offen offset:0 nt lds\n"
    "v_mfma_f32_16x16x32_bf16 v[208:211], a[240:243], v[32:35], v[144:147]\n"
    "s_waitcnt lgkmcnt(2)\n"
    "v_exp_f32 v205, v205\n"
    "v_mfma_f32_16x16x32_bf16 v[208:211], a[244:247], v[36:39], v[208:211]\n"
    "s_waitcnt lgkmcnt(1)\n"
    "v_cvt_pk_bf16_f32 v212, v204, v205\n"
    "v_add_f32 v220, v220, v205\n"
    "v_mfma_f32_16x16x32_bf16 v[208:211], a[248:251], v[40:43], v[208:211]\n"
    "s_waitcnt lgkmcnt(0)\n"
    "v_exp_f32 v206, v206\n"
    "v_mfma_f32_16x16x32_bf16 v[208:211], a[252:255], v[44:47], v[208:211]\n"
    "v_add_f32 v220, v220, v204\n"
    "v_add_f32 v220, v220, v206\n"
    "v_mfma_f32_16x16x32_bf16 a[160:163], v[188:191], v[12:15], a[160:163]\n"
    "ds_read_b64_tr_b16 v[188:189], v28 offset:0\n"
    "v_exp_f32 v207, v207\n"
    "v_mfma_f32_16x16x32_bf16 a[164:167], v[192:195], v[12:15], a[164:167]\n"
    "v_cvt_pk_bf16_f32 v213, v206, v207\n"
    "v_add_f32 v220, v220, v207\n"
    "v_mfma_f32_16x16x32_bf16 a[216:219], v[196:199], v[12:15], a[216:219]\n"
    "ds_read_b64_tr_b16 v[190:191], v28 offset:4096\n"
    "v_exp_f32 v208, v208\n"
    "v_mfma_f32_16x16x32_bf16 a[220:223], v[200:203], v[12:15], a[220:223]\n"
    "ds_read_b64_tr_b16 v[192:193], v29 offset:0\n"
    "v_add_f32 v220, v220, v208\n"
    "v_mfma_f32_16x16x32_bf16 v[204:207], a[224:227], v[48:51], v[148:151]\n"
    "ds_read_b64_tr_b16 v[194:195], v29 offset:4096\n"
    "v_exp_f32 v209, v209\n"
    "v_mfma_f32_16x16x32_bf16 v[204:207], a[228:231], v[52:55], v[204:207]\n"
    "v_cvt_pk_bf16_f32 v214, v208, v209\n"
    "v_add_f32 v220, v220, v209\n"
    "v_mfma_f32_16x16x32_bf16 v[204:207], a[232:235], v[56:59], v[204:207]\n"
    "ds_read_b64_tr_b16 v[196:197], v30 offset:0\n"
    "v_exp_f32 v210, v210\n"
    "v_mfma_f32_16x16x32_bf16 v[204:207], a[236:239], v[60:63], v[204:207]\n"
    "ds_read_b64_tr_b16 v[198:199], v30 offset:4096\n"
    "v_add_f32 v220, v220, v210\n"
    "v_mfma_f32_16x16x32_bf16 a[48:51], v[172:175], v[12:15], a[48:51]\n"
    "ds_read_b64_tr_b16 v[200:201], v31 offset:0\n"
    "v_exp_f32 v211, v211\n"
    "v_mfma_f32_16x16x32_bf16 a[52:55], v[176:179], v[12:15], a[52:55]\n"
    "s_add_u32 s77, s77, s78\n"
    "v_cvt_pk_bf16_f32 v215, v210, v211\n"
    "v_add_f32 v220, v220, v211\n"
    "v_mfma_f32_16x16x32_bf16 a[104:107], v[180:183], v[12:15], a[104:107]\n"
    "ds_read_b64_tr_b16 v[202:203], v31 offset:4096\n"
    "v_exp_f32 v204, v204\n"
    "v_mfma_f32_16x16x32_bf16 a[108:111], v[184:187], v[12:15], a[108:111]\n"
    "ds_read_b64_tr_b16 v[172:173], v24 offset:0\n"
    "v_add_f32 v221, v221, v204\n"
    "v_mfma_f32_16x16x32_bf16 v[208:211], a[240:243], v[48:51], v[148:151]\n"
    "ds_read_b64_tr_b16 v[174:175], v24 offset:4096\n"
    "v_exp_f32 v205, v205\n"
    "v_mfma_f32_16x16x32_bf16 v[208:211], a[244:247], v[52:55], v[208:211]\n"
    "v_cvt_pk_bf16_f32 v216, v204, v205\n"
    "v_add_f32 v221, v221, v205\n"
    "v_mfma_f32_16x16x32_bf16 v[208:211], a[248:251], v[56:59], v[208:211]\n"
    "ds_read_b64_tr_b16 v[176:177], v25 offset:0\n"
    "v_exp_f32 v206, v206\n"
    "v_mfma_f32_16x16x32_bf16 v[208:211], a[252:255], v[60:63], v[208:211]\n"
    "s_waitcnt lgkmcnt(9)\n"
    "ds_read_b64_tr_b16 v[178:179], v25 offset:4096\n"
    "v_add_f32 v221, v221, v206\n"
    "v_mfma_f32_16x16x32_bf16 a[112:115], v[188:191], v[212:215], a[112:115]\n"
    "s_waitcnt lgkmcnt(8)\n"
    "ds_read_b64_tr_b16 v[180:181], v26 offset:0\n"
    "v_exp_f32 v207, v207\n"
    "v_mfma_f32_16x16x32_bf16 a[116:119], v[192:195], v[212:215], a[116:119]\n"
    "s_waitcnt lgkmcnt(7)\n"
    "v_cvt_pk_bf16_f32 v217, v206, v207\n"
    "v_add_f32 v221, v221, v207\n"
    "v_mfma_f32_16x16x32_bf16 a[168:171], v[196:199], v[212:215], a[168:171]\n"
    "s_waitcnt lgkmcnt(5)\n"
    "ds_read_b64_tr_b16 v[182:183], v26 offset:4096\n"
    "v_exp_f32 v208, v208\n"
    "v_mfma_f32_16x16x32_bf16 a[172:175], v[200:203], v[212:215], a[172:175]\n"
    "ds_read_b64_tr_b16 v[184:185], v27 offset:0\n"
    "v_add_f32 v221, v221, v208\n"
    "v_mfma_f32_16x16x32_bf16 v[204:207], a[224:227], v[64:67], v[152:155]\n"
    "ds_read_b64_tr_b16 v[186:187], v27 offset:4096\n"
    "v_exp_f32 v209, v209\n"
    "v_mfma_f32_16x16x32_bf16 v[204:207], a[228:231], v[68:71], v[204:207]\n"
    "v_cvt_pk_bf16_f32 v218, v208, v209\n"
    "v_add_f32 v221, v221, v209\n"
    "v_mfma_f32_16x16x32_bf16 v[204:207], a[232:235], v[72:75], v[204:207]\n"
    "s_memrealtime s[36:37]\n"
    "v_exp_f32 v210, v210\n"
    "v_mfma_f32_16x16x32_bf16 v[204:207], a[236:239], v[76:79], v[204:207]\n"
    "s_waitcnt lgkmcnt(6)\n"
    "buffer_load_dword v253, v228, s[84:87], s45 offen offset:0\n"
    "v_mfma_f32_16x16x32_bf16 a[0:3], v[172:175], v[212:215], a[0:3]\n"
    "s_waitcnt lgkmcnt(4)\n"
    "v_exp_f32 v211, v211\n"
    "v_mfma_f32_16x16x32_bf16 a[4:7], v[176:179], v[212:215], a[4:7]\n"
    "s_waitcnt lgkmcnt(2)\n"
    "v_cvt_pk_bf16_f32 v219, v210, v211\n"
    "v_add_f32 v221, v221, v211\n"
    "v_mfma_f32_16x16x32_bf16 a[56:59], v[180:183], v[212:215], a[56:59]\n"
    "s_waitcnt lgkmcnt(0)\n"
    "v_exp_f32 v204, v204\n"
    "v_mfma_f32_16x16x32_bf16 a[60:63], v[184:187], v[212:215], a[60:63]\n"
    "v_add_f32 v221, v221, v210\n"
    "v_add_f32 v222, v222, v204\n"
    "v_mfma_f32_16x16x32_bf16 v[208:211], a[240:243], v[64:67], v[152:155]\n"
    "v_exp_f32 v205, v205\n"
    "v_mfma_f32_16x16x32_bf16 v[208:211], a[244:247], v[68:71], v[208:211]\n"
    "v_cvt_pk_bf16_f32 v212, v204, v205\n"
    "v_add_f32 v222, v222, v205\n"
    "v_mfma_f32_16x16x32_bf16 v[208:211], a[248:251], v[72:75], v[208:211]\n"
    "v_exp_f32 v206, v206\n"
    "v_mfma_f32_16x16x32_bf16 v[208:211], a[252:255], v[76:79], v[208:211]\n"
    "buffer_load_dwordx4 v6, s[48:51], s72 offen offset:1024 nt lds\n"
    "v_mfma_f32_16x16x32_bf16 a[120:123], v[188:191], v[216:219], a[120:123]\n"
    "s_sub_u32 s36, s36, s77\n"
    "v_exp_f32 v207, v207\n"
    "v_mfma_f32_16x16x32_bf16 a[124:127], v[192:195], v[216:219], a[124:127]\n"
    "s_cmp_lt_i32 s36, 0\n"
    "v_cvt_pk_bf16_f32 v213, v206, v207\n"
    "v_add_f32 v222, v222, v207\n"
    "v_mfma_f32_16x16x32_bf16 a[176:179], v[196:199], v[216:219], a[176:179]\n"
    "s_cbranch_scc1 LPW0_%=\n"
    "LPR0_%=:\n"
    "v_exp_f32 v208, v208\n"
    "v_mfma_f32_16x16x32_bf16 a[180:183], v[200:203], v[216:219], a[180:183]\n"
    "v_add_f32 v222, v222, v206\n"
    "v_add_f32 v222, v222, v208\n"
    "v_mfma_f32_16x16x32_bf16 v[204:207], a[224:227], v[80:83], v[156:159]\n"
    "v_exp_f32 v209, v209\n"
    "v_mfma_f32_16x16x32_bf16 v[204:207], a[228:231], v[84:87], v[204:207]\n"
    "v_cvt_pk_bf16_f32 v214, v208, v209\n"
    "v_add_f32 v222, v222, v209\n"
    "v_mfma_f32_16x16x32_bf16 v[204:207], a[232:235], v[88:91], v[204:207]\n"
    "v_exp_f32 v210, v210\n"
    "v_mfma_f32_16x16x32_bf16 v[204:207], a[236:239], v[92:95], v[204:207]\n"
    "buffer_load_dwordx4 v7, s[48:51], s72 offen offset:2048 nt lds\n"
    "v_mfma_f32_16x16x32_bf16 a[8:11], v[172:175], v[216:219], a[8:11]\n"
    "v_exp_f32 v211, v211\n"
    "v_mfma_f32_16x16x32_bf16 a[12:15], v[176:179], v[216:219], a[12:15]\n"
    "v_cvt_pk_bf16_f32 v215, v210, v211\n"
    "v_add_f32 v222, v222, v211\n"
    "v_mfma_f32_16x16x32_bf16 a[64:67], v[180:183], v[216:219], a[64:67]\n"
    "v_exp_f32 v204, v204\n"
    "v_mfma_f32_16x16x32_bf16 a[68:71], v[184:187], v[216:219], a[68:71]\n"
    "v_add_f32 v222, v222, v210\n"
    "v_add_f32 v223, v223, v204\n"
    "v_mfma_f32_16x16x32_bf16 v[208:211], a[240:243], v[80:83], v[156:159]\n"
    "v_exp_f32 v205, v205\n"
    "v_mfma_f32_16x16x32_bf16 v[208:211], a[244:247], v[84:87], v[208:211]\n"
    "v_cvt_pk_bf16_f32 v216, v204, v205\n"
    "v_add_f32 v223, v223, v205\n"
    "v_mfma_f32_16x16x32_bf16 v[208:211], a[248:251], v[88:91], v[208:211]\n"
    "v_exp_f32 v206, v206\n"
    "v_mfma_f32_16x16x32_bf16 v[208:211], a[252:255], v[92:95], v[208:211]\n"
    "buffer_load_dwordx4 v8, s[48:51], s72 offen offset:3072 lds\n"
    "v_mfma_f32_16x16x32_bf16 a[128:131], v[188:191], v[212:215], a[128:131]\n"
    "s_add_u32 s72, s72, 4096\n"
    "v_exp_f32 v207, v207\n"
    "v_mfma_f32_16x16x32_bf16 a[132:135], v[192:195], v[212:215], a[132:135]\n"
    "s_and_b32 s72, s72, 131071\n"
    "v_cvt_pk_bf16_f32 v217, v206, v207\n"
    "v_add_f32 v223, v223, v207\n"
    "v_mfma_f32_16x16x32_bf16 a[184:187], v[196:199], v[212:215], a[184:187]\n"
    "s_add_u32 s73, s73, 4096\n"
    "v_exp_f32 v208, v208\n"
    "v_mfma_f32_16x16x32_bf16 a[188:191], v[200:203], v[212:215], a[188:191]\n"
    "s_sub_u32 s74, s73, 40960\n"
    "v_add_f32 v223, v223, v206\n"
    "v_add_f32 v223, v223, v208\n"
    "v_mfma_f32_16x16x32_bf16 v[204:207], a[224:227], v[96:99], v[160:163]\n"
    "s_min_u32 s73, s73, s74\n"
    "v_exp_f32 v209, v209\n"
    "v_mfma_f32_16x16x32_bf16 v[204:207], a[228:231], v[100:103], v[204:207]\n"
    "v_cvt_pk_bf16_f32 v218, v208, v209\n"
    "v_add_f32 v223, v223, v209\n"
    "v_mfma_f32_16x16x32_bf16 v[204:207], a[232:235], v[104:107], v[204:207]\n"
    "s_add_u32 m0, s60, s73\n"
    "v_exp_f32 v210, v210\n"
    "v_mfma_f32_16x16x32_bf16 v[204:207], a[236:239], v[108:111], v[204:207]\n"
    "buffer_load_dwordx4 v5, s[48:51], s72 offen offset:0 nt lds\n"
    "v_mfma_f32_16x16x32_bf16 a[16:19], v[172:175], v[212:215], a[16:19]\n"
    "v_exp_f32 v211, v211\n"
    "v_mfma_f32_16x16x32_bf16 a[20:23], v[176:179], v[212:215], a[20:23]\n"
    "v_cvt_pk_bf16_f32 v219, v210, v211\n"
    "v_add_f32 v223, v223, v211\n"
    "v_mfma_f32_16x16x32_bf16 a[72:75], v[180:183], v[212:215], a[72:75]\n"
    "v_exp_f32 v204, v204\n"
    "v_mfma_f32_16x16x32_bf16 a[76:79], v[184:187], v[212:215], a[76:79]\n"
    "v_add_f32 v223, v223, v210\n"
    "v_add_f32 v224, v224, v204\n"
    "v_mfma_f32_16x16x32_bf16 v[208:211], a[240:243], v[96:99], v[160:163]\n"
    "v_exp_f32 v205, v205\n"
    "v_mfma_f32_16x16x32_bf16 v[208:211], a[244:247], v[100:103], v[208:211]\n"
    "v_cvt_pk_bf16_f32 v212, v204, v205\n"
    "v_add_f32 v224, v224, v205\n"
    "v_mfma_f32_16x16x32_bf16 v[208:211], a[248:251], v[104:107], v[208:211]\n"
    "v_exp_f32 v206, v206\n"
    "v_mfma_f32_16x16x32_bf16 v[208:211], a[252:255], v[108:111], v[208:211]\n"
    "buffer_load_dwordx4 v6, s[48:51], s72 offen offset:1024 nt lds\n"
    "v_mfma_f32_16x16x32_bf16 a[136:139], v[188:191], v[216:219], a[136:139]\n"
    "v_exp_f32 v207, v207\n"
    "v_mfma_f32_16x16x32_bf16 a[140:143], v[192:195], v[216:219], a[140:143]\n"
    "v_cvt_pk_bf16_f32 v213, v206, v207\n"
    "v_add_f32 v224, v224, v207\n"
    "v_mfma_f32_16x16x32_bf16 a[192:195], v[196:199], v[216:219], a[192:195]\n"
    "v_exp_f32 v208, v208\n"
    "v_mfma_f32_16x16x32_bf16 a[196:199], v[200:203], v[216:219], a[196:199]\n"
    "v_add_f32 v224, v224, v206\n"
    "v_add_f32 v224, v224, v208\n"
    "v_mfma_f32_16x16x32_bf16 v[204:207], a[224:227], v[112:115], v[164:167]\n"
    "v_exp_f32 v209, v209\n"
    "v_mfma_f32_16x16x32_bf16 v[204:207], a[228:231], v[116:119], v[204:207]\n"
    "v_cvt_pk_bf16_f32 v214, v208, v209\n"
    "v_add_f32 v224, v224, v209\n"
    "v_mfma_f32_16x16x32_bf16 v[204:207], a[232:235], v[120:123], v[204:207]\n"
    "v_exp_f32 v210, v210\n"
    "v_mfma_f32_16x16x32_bf16 v[204:207], a[236:239], v[124:127], v[204:207]\n"
    "buffer_load_dwordx4 v7, s[48:51], s72 offen offset:2048 nt lds\n"
    "v_mfma_f32_16x16x32_bf16 a[24:27], v[172:175], v[216:219], a[24:27]\n"
    "v_exp_f32 v211, v211\n"
    "v_mfma_f32_16x16x32_bf16 a[28:31], v[176:179], v[216:219], a[28:31]\n"
    "v_cvt_pk_bf16_f32 v215, v210, v211\n"
    "v_add_f32 v224, v224, v211\n"
    "v_mfma_f32_16x16x32_bf16 a[80:83], v[180:183], v[216:219], a[80:83]\n"
    "v_exp_f32 v204, v204\n"
    "v_mfma_f32_16x16x32_bf16 a[84:87], v[184:187], v[216:219], a[84:87]\n"
    "v_add_f32 v224, v224, v210\n"
    "v_add_f32 v225, v225, v204\n"
    "v_mfma_f32_16x16x32_bf16 v[208:211], a[240:243], v[112:115], v[164:167]\n"
    "v_exp_f32 v205, v205\n"
    "v_mfma_f32_16x16x32_bf16 v[208:211], a[244:247], v[116:119], v[208:211]\n"
    "v_cvt_pk_bf16_f32 v216, v204, v205\n"
    "v_add_f32 v225, v225, v205\n"
    "v_mfma_f32_16x16x32_bf16 v[208:211], a[248:251], v[120:123], v[208:211]\n"
    "s_waitcnt vmcnt(18)\n"
    "v_exp_f32 v206, v206\n"
    "v_mfma_f32_16x16x32_bf16 v[208:211], a[252:255], v[124:127], v[208:211]\n"
    "s_barrier\n"
    "v_mfma_f32_16x16x32_bf16 a[144:147], v[188:191], v[212:215], a[144:147]\n"
    "v_exp_f32 v207, v207\n"
    "v_mfma_f32_16x16x32_bf16 a[148:151], v[192:195], v[212:215], a[148:151]\n"
    "v_cvt_pk_bf16_f32 v217, v206, v207\n"
    "v_add_f32 v225, v225, v207\n"
    "v_mfma_f32_16x16x32_bf16 a[200:203], v[196:199], v[212:215], a[200:203]\n"
    "v_exp_f32 v208, v208\n"
    "v_mfma_f32_16x16x32_bf16 a[204:207], v[200:203], v[212:215], a[204:207]\n"
    "v_add_f32 v225, v225, v206\n"
    "v_add_f32 v225, v225, v208\n"
    "v_mfma_f32_16x16x32_bf16 v[204:207], a[224:227], v[128:131], v[168:171]\n"
    "v_exp_f32 v209, v209\n"
    "v_mfma_f32_16x16x32_bf16 v[204:207], a[228:231], v[132:135], v[204:207]\n"
    "v_cvt_pk_bf16_f32 v218, v208, v209\n"
    "v_add_f32 v225, v225, v209\n"
    "v_mfma_f32_16x16x32_bf16 v[204:207], a[232:235], v[136:139], v[204:207]\n"
    "v_exp_f32 v210, v210\n"
    "v_mfma_f32_16x16x32_bf16 v[204:207], a[236:239], v[140:143], v[204:207]\n"
    "ds_read_b128 a[224:227], v20 offset:8192\n"
    "v_add_f32 v225, v225, v210\n"
    "v_mfma_f32_16x16x32_bf16 a[32:35], v[172:175], v[212:215], a[32:35]\n"
    "ds_read_b128 a[228:231], v21 offset:8192\n"
    "v_exp_f32 v211, v211\n"
    "v_mfma_f32_16x16x32_bf16 a[36:39], v[176:179], v[212:215], a[36:39]\n"
    "v_cvt_pk_bf16_f32 v219, v210, v211\n"
    "v_add_f32 v225, v225, v211\n"
    "v_mfma_f32_16x16x32_bf16 a[88:91], v[180:183], v[212:215], a[88:91]\n"
    "ds_read_b128 a[232:235], v22 offset:8192\n"
    "v_exp_f32 v204, v204\n"
    "v_mfma_f32_16x16x32_bf16 a[92:95], v[184:187], v[212:215], a[92:95]\n"
    "ds_read_b128 a[236:239], v23 offset:8192\n"
    "v_add_f32 v226, v226, v204\n"
    "v_mfma_f32_16x16x32_bf16 v[208:211], a[240:243], v[128:131], v[168:171]\n"
    "v_exp_f32 v205, v205\n"
    "v_mfma_f32_16x16x32_bf16 v[208:211], a[244:247], v[132:135], v[208:211]\n"
    "v_cvt_pk_bf16_f32 v12, v204, v205\n"
    "v_add_f32 v226, v226, v205\n"
    "v_mfma_f32_16x16x32_bf16 v[208:211], a[248:251], v[136:139], v[208:211]\n"
    "v_exp_f32 v206, v206\n"
    "v_mfma_f32_16x16x32_bf16 v[208:211], a[252:255], v[140:143], v[208:211]\n"
    "ds_read_b128 a[240:243], v20 offset:12288\n"
    "v_add_f32 v226, v226, v206\n"
    "v_mfma_f32_16x16x32_bf16 a[152:155], v[188:191], v[216:219], a[152:155]\n"
    "ds_read_b128 a[244:247], v21 offset:12288\n"
    "v_exp_f32 v207, v207\n"
    "v_mfma_f32_16x16x32_bf16 a[156:159], v[192:195], v[216:219], a[156:159]\n"
    "s_sub_u32 s76, s76, 1\n"
    "v_cvt_pk_bf16_f32 v13, v206, v207\n"
    "v_add_f32 v226, v226, v207\n"
    "v_mfma_f32_16x16x32_bf16 a[208:211], v[196:199], v[216:219], a[208:211]\n"
    "s_cmp_lg_u32 s76, 0\n"
    "v_exp_f32 v208, v208\n"
    "v_mfma_f32_16x16x32_bf16 a[212:215], v[200:203], v[216:219], a[212:215]\n"
    "s_waitcnt lgkmcnt(5)\n"
    "ds_read_b128 a[248:251], v22 offset:12288\n"
    "v_add_f32 v226, v226, v208\n"
    "s_cbranch_scc1 LA_%=\n"
    "s_mov_b32 s79, 1602224128\n"
    "s_mov_b64 s[30:31], 0\n"
    "v_mov_b32 v228, v220\n"
    "v_exp_f32 v209, v209\n"
    "v_mov_b32 v229, v221\n"
    "v_mov_b32 v230, v222\n"
    "v_mov_b32 v231, v223\n"
    "v_cvt_pk_bf16_f32 v14, v208, v209\n"
    "v_add_f32 v226, v226, v209\n"
    "v_mov_b32 v232, v224\n"
    "v_mov_b32 v233, v225\n"
    "v_permlane32_swap_b32 v220, v228\n"
    "v_exp_f32 v210, v210\n"
    "v_permlane32_swap_b32 v221, v229\n"
    "v_permlane32_swap_b32 v222, v230\n"
    "v_permlane32_swap_b32 v223, v231\n"
    "v_add_f32 v226, v226, v210\n"
    "v_mfma_f32_16x16x32_bf16 a[40:43], v[172:175], v[216:219], a[40:43]\n"
    "v_exp_f32 v211, v211\n"
    "v_permlane32_swap_b32 v224, v232\n"
    "v_permlane32_swap_b32 v225, v233\n"
    "v_mfma_f32_16x16x32_bf16 a[44:47], v[176:179], v[216:219], a[44:47]\n"
    "v_cvt_pk_bf16_f32 v15, v210, v211\n"
    "v_add_f32 v226, v226, v211\n"
    "v_add_f32 v220, v220, v228\n"
    "v_mfma_f32_16x16x32_bf16 a[96:99], v[180:183], v[216:219], a[96:99]\n"
    "v_add_f32 v221, v221, v229\n"
    "v_add_f32 v222, v222, v230\n"
    "v_add_f32 v223, v223, v231\n"
    "v_mfma_f32_16x16x32_bf16 a[100:103], v[184:187], v[216:219], a[100:103]\n"
    "v_add_f32 v224, v224, v232\n"
    "v_add_f32 v225, v225, v233\n"
    "v_mov_b32 v228, v220\n"
    "v_mfma_f32_16x16x32_bf16 a[160:163], v[188:191], v[12:15], a[160:163]\n"
    "v_mov_b32 v229, v221\n"
    "v_mov_b32 v230, v222\n"
    "v_mov_b32 v231, v223\n"
    "v_mfma_f32_16x16x32_bf16 a[164:167], v[192:195], v[12:15], a[164:167]\n"
    "v_mov_b32 v232, v224\n"
    "v_mov_b32 v233, v225\n"
    "v_permlane16_swap_b32 v220, v228\n"
    "v_mfma_f32_16x16x32_bf16 a[216:219], v[196:199], v[12:15], a[216:219]\n"
    "v_permlane16_swap_b32 v221, v229\n"
    "v_permlane16_swap_b32 v222, v230\n"
    "v_permlane16_swap_b32 v223, v231\n"
    "v_mfma_f32_16x16x32_bf16 a[220:223], v[200:203], v[12:15], a[220:223]\n"
    "v_permlane16_swap_b32 v224, v232\n"
    "v_permlane16_swap_b32 v225, v233\n"
    "v_add_f32 v220, v220, v228\n"
    "v_mfma_f32_16x16x32_bf16 a[48:51], v[172:175], v[12:15], a[48:51]\n"
    "v_add_f32 v221, v221, v229\n"
    "v_add_f32 v222, v222, v230\n"
    "v_add_f32 v223, v223, v231\n"
    "v_mfma_f32_16x16x32_bf16 a[52:55], v[176:179], v[12:15], a[52:55]\n"
    "v_add_f32 v224, v224, v232\n"
    "v_add_f32 v225, v225, v233\n"
    "v_cmp_nge_f32 vcc, s79, v220\n"
    "v_mfma_f32_16x16x32_bf16 a[104:107], v[180:183], v[12:15], a[104:107]\n"
    "v_rcp_f32 v220, v220\n"
    "s_or_b64 s[30:31], s[30:31], vcc\n"
    "v_cmp_nge_f32 vcc, s79, v221\n"
    "v_mfma_f32_16x16x32_bf16 a[108:111], v[184:187], v[12:15], a[108:111]\n"
    "v_rcp_f32 v221, v221\n"
    "s_or_b64 s[30:31], s[30:31], vcc\n"
    "v_cmp_nge_f32 vcc, s79, v222\n"
    "LE_%=:\n"
    "s_waitcnt lgkmcnt(0)\n"
    "s_add_u32 s70, s21, 0\n"
    "s_add_u32 m0, s60, 8192\n"
    "v_rcp_f32 v222, v222\n"
    "buffer_load_dwordx4 v1, s[8:11], s70 offen offset:0 lds\n"
    "s_add_u32 s70, s70, 8192\n"
    "s_or_b64 s[30:31], s[30:31], vcc\n"
    "v_cmp_nge_f32 vcc, s79, v223\n"
    "v_rcp_f32 v223, v223\n"
    "s_or_b64 s[30:31], s[30:31], vcc\n"
    "v_cmp_nge_f32 vcc, s79, v224\n"
    "v_rcp_f32 v224, v224\n"
    "s_or_b64 s[30:31], s[30:31], vcc\n"
    "v_cmp_nge_f32 vcc, s79, v225\n"
    "v_rcp_f32 v225, v225\n"
    "s_or_b64 s[30:31], s[30:31], vcc\n"
    "v_accvgpr_read_b32 v228, a0\n"
    "v_mov_b32 v252, v226\n"
    "v_accvgpr_read_b32 v229, a1\n"
    "v_accvgpr_read_b32 v230, a2\n"
    "buffer_load_dwordx4 v1, s[8:11], s70 offen offset:1024 lds\n"
    "s_add_u32 s70, s70, 8192\n"
    "v_permlane32_swap_b32 v226, v252\n"
    "v_accvgpr_read_b32 v231, a3\n"
    "v_add_f32 v226, v226, v252\n"
    "v_accvgpr_read_b32 v232, a4\n"
    "v_mov_b32 v252, v226\n"
    "v_accvgpr_read_b32 v233, a5\n"
    "v_accvgpr_read_b32 v234, a6\n"
    "v_permlane16_swap_b32 v226, v252\n"
    "v_accvgpr_read_b32 v235, a7\n"
    "v_add_f32 v226, v226, v252\n"
    "v_pk_mul_f32 v[228:229], v[228:229], v[220:221] op_sel:[0,0] op_sel_hi:[1,0]\n"
    "v_cmp_nge_f32 vcc, s79, v226\n"
    "v_pk_mul_f32 v[230:231], v[230:231], v[220:221] op_sel:[0,0] op_sel_hi:[1,0]\n"
    "v_rcp_f32 v226, v226\n"
    "buffer_load_dwordx4 v1, s[8:11], s70 offen offset:2048 lds\n"
    "s_add_u32 s70, s70, 8192\n"
    "v_pk_mul_f32 v[232:233], v[232:233], v[220:221] op_sel:[0,0] op_sel_hi:[1,0]\n"
    "s_or_b64 s[30:31], s[30:31], vcc\n"
    "v_pk_mul_f32 v[234:235], v[234:235], v[220:221] op_sel:[0,0] op_sel_hi:[1,0]\n"
    "s_or_b32 s83, s30, s31\n"
    "v_cvt_pk_bf16_f32 v244, v228, v229\n"
    "v_mov_b32 v252, s83\n"
    "v_cvt_pk_bf16_f32 v245, v230, v231\n"
    "ds_write_b32 v9, v252 offset:39936\n"
    "v_cvt_pk_bf16_f32 v246, v232, v233\n"
    "v_cvt_pk_bf16_f32 v247, v234, v235\n"
    "ds_write_b128 v9, v[244:247] offset:32768\n"
    "v_accvgpr_read_b32 v236, a8\n"
    "v_accvgpr_read_b32 v237, a9\n"
    "v_accvgpr_read_b32 v238, a10\n"
    "buffer_load_dwordx4 v1, s[8:11], s70 offen offset:3072 lds\n"
    "v_accvgpr_read_b32 v239, a11\n"
    "v_accvgpr_read_b32 v240, a12\n"
    "v_accvgpr_read_b32 v241, a13\n"
    "v_accvgpr_read_b32 v242, a14\n"
    "v_accvgpr_read_b32 v243, a15\n"
    "v_pk_mul_f32 v[236:237], v[236:237], v[220:221] op_sel:[0,1] op_sel_hi:[1,1]\n"
    "v_pk_mul_f32 v[238:239], v[238:239], v[220:221] op_sel:[0,1] op_sel_hi:[1,1]\n"
    "v_pk_mul_f32 v[240:241], v[240:241], v[220:221] op_sel:[0,1] op_sel_hi:[1,1]\n"
    "v_pk_mul_f32 v[242:243], v[242:243], v[220:221] op_sel:[0,1] op_sel_hi:[1,1]\n"
    "v_cvt_pk_bf16_f32 v248, v236, v237\n"
    "v_cvt_pk_bf16_f32 v249, v238, v239\n"
    "v_cvt_pk_bf16_f32 v250, v240, v241\n"
    "v_cvt_pk_bf16_f32 v251, v242, v243\n"
    "s_add_u32 s70, s21, 294912\n"
    "s_add_u32 m0, s60, 12288\n"
    "ds_write_b128 v9, v[248:251] offset:33792\n"
    "buffer_load_dwordx4 v1, s[8:11], s70 offen offset:0 lds\n"
    "s_add_u32 s70, s70, 8192\n"
    "v_accvgpr_read_b32 v228, a16\n"
    "v_accvgpr_read_b32 v229, a17\n"
    "v_accvgpr_read_b32 v230, a18\n"
    "v_accvgpr_read_b32 v231, a19\n"
    "v_accvgpr_read_b32 v232, a20\n"
    "v_accvgpr_read_b32 v233, a21\n"
    "v_accvgpr_read_b32 v234, a22\n"
    "v_accvgpr_read_b32 v235, a23\n"
    "v_pk_mul_f32 v[228:229], v[228:229], v[222:223] op_sel:[0,0] op_sel_hi:[1,0]\n"
    "v_pk_mul_f32 v[230:231], v[230:231], v[222:223] op_sel:[0,0] op_sel_hi:[1,0]\n"
    "v_pk_mul_f32 v[232:233], v[232:233], v[222:223] op_sel:[0,0] op_sel_hi:[1,0]\n"
    "v_pk_mul_f32 v[234:235], v[234:235], v[222:223] op_sel:[0,0] op_sel_hi:[1,0]\n"
    "v_cvt_pk_bf16_f32 v244, v228, v229\n"
    "v_cvt_pk_bf16_f32 v245, v230, v231\n"
    "buffer_load_dwordx4 v1, s[8:11], s70 offen offset:1024 lds\n"
    "s_add_u32 s70, s70, 8192\n"
    "v_cvt_pk_bf16_f32 v246, v232, v233\n"
    "v_cvt_pk_bf16_f32 v247, v234, v235\n"
    "ds_write_b128 v9, v[244:247] offset:34816\n"
    "v_accvgpr_read_b32 v236, a24\n"
    "v_accvgpr_read_b32 v237, a25\n"
    "v_accvgpr_read_b32 v238, a26\n"
    "v_accvgpr_read_b32 v239, a27\n"
    "v_accvgpr_read_b32 v240, a28\n"
    "v_accvgpr_read_b32 v241, a29\n"
    "v_accvgpr_read_b32 v242, a30\n"
    "v_accvgpr_read_b32 v243, a31\n"
    "v_pk_mul_f32 v[236:237], v[236:237], v[222:223] op_sel:[0,1] op_sel_hi:[1,1]\n"
    "v_pk_mul_f32 v[238:239], v[238:239], v[222:223] op_sel:[0,1] op_sel_hi:[1,1]\n"
    "v_pk_mul_f32 v[240:241], v[240:241], v[222:223] op_sel:[0,1] op_sel_hi:[1,1]\n"
    "buffer_load_dwordx4 v1, s[8:11], s70 offen offset:2048 lds\n"
    "s_add_u32 s70, s70, 8192\n"
    "v_pk_mul_f32 v[242:243], v[242:243], v[222:223] op_sel:[0,1] op_sel_hi:[1,1]\n"
    "v_cvt_pk_bf16_f32 v248, v236, v237\n"
    "v_cvt_pk_bf16_f32 v249, v238, v239\n"
    "v_cvt_pk_bf16_f32 v250, v240, v241\n"
    "v_cvt_pk_bf16_f32 v251, v242, v243\n"
    "ds_write_b128 v9, v[248:251] offset:35840\n"
    "v_accvgpr_read_b32 v228, a32\n"
    "v_accvgpr_read_b32 v229, a33\n"
    "v_accvgpr_read_b32 v230, a34\n"
    "v_accvgpr_read_b32 v231, a35\n"
    "v_accvgpr_read_b32 v232, a36\n"
    "v_accvgpr_read_b32 v233, a37\n"
    "v_accvgpr_read_b32 v234, a38\n"
    "v_accvgpr_read_b32 v235, a39\n"
    "buffer_load_dwordx4 v1, s[8:11], s70 offen offset:3072 lds\n"
    "v_pk_mul_f32 v[228:229], v[228:229], v[224:225] op_sel:[0,0] op_sel_hi:[1,0]\n"
    "v_pk_mul_f32 v[230:231], v[230:231], v[224:225] op_sel:[0,0] op_sel_hi:[1,0]\n"
    "v_pk_mul_f32 v[232:233], v[232:233], v[224:225] op_sel:[0,0] op_sel_hi:[1,0]\n"
    "v_pk_mul_f32 v[234:235], v[234:235], v[224:225] op_sel:[0,0] op_sel_hi:[1,0]\n"
    "v_cvt_pk_bf16_f32 v244, v228, v229\n"
    "v_cvt_pk_bf16_f32 v245, v230, v231\n"
    "v_cvt_pk_bf16_f32 v246, v232, v233\n"
    "v_cvt_pk_bf16_f32 v247, v234, v235\n"
    "ds_write_b128 v9, v[244:247] offset:36864\n"
    "v_accvgpr_read_b32 v236, a40\n"
    "v_accvgpr_read_b32 v237, a41\n"
    "v_accvgpr_read_b32 v238, a42\n"
    "v_accvgpr_read_b32 v239, a43\n"
    "s_add_u32 s70, s21, 36864\n"
    "s_add_u32 m0, s60, 16384\n"
    "v_accvgpr_read_b32 v240, a44\n"
    "buffer_load_dwordx4 v1, s[8:11], s70 offen offset:0 lds\n"
    "s_add_u32 s70, s70, 8192\n"
    "v_accvgpr_read_b32 v241, a45\n"
    "v_accvgpr_read_b32 v242, a46\n"
    "v_accvgpr_read_b32 v243, a47\n"
    "v_pk_mul_f32 v[236:237], v[236:237], v[224:225] op_sel:[0,1] op_sel_hi:[1,1]\n"
    "v_pk_mul_f32 v[238:239], v[238:239], v[224:225] op_sel:[0,1] op_sel_hi:[1,1]\n"
    "v_pk_mul_f32 v[240:241], v[240:241], v[224:225] op_sel:[0,1] op_sel_hi:[1,1]\n"
    "v_pk_mul_f32 v[242:243], v[242:243], v[224:225] op_sel:[0,1] op_sel_hi:[1,1]\n"
    "v_cvt_pk_bf16_f32 v248, v236, v237\n"
    "v_cvt_pk_bf16_f32 v249, v238, v239\n"
    "v_cvt_pk_bf16_f32 v250, v240, v241\n"
    "v_cvt_pk_bf16_f32 v251, v242, v243\n"
    "ds_write_b128 v9, v[248:251] offset:37888\n"
    "v_accvgpr_read_b32 v228, a48\n"
    "v_accvgpr_read_b32 v229, a49\n"
    "buffer_load_dwordx4 v1, s[8:11], s70 offen offset:1024 lds\n"
    "s_add_u32 s70, s70, 8192\n"
    "v_accvgpr_read_b32 v230, a50\n"
    "v_accvgpr_read_b32 v231, a51\n"
    "v_accvgpr_read_b32 v232, a52\n"
    "v_accvgpr_read_b32 v233, a53\n"
    "v_accvgpr_read_b32 v234, a54\n"
    "v_accvgpr_read_b32 v235, a55\n"
    "v_pk_mul_f32 v[228:229], v[228:229], v[226:227] op_sel:[0,0] op_sel_hi:[1,0]\n"
    "v_pk_mul_f32 v[230:231], v[230:231], v[226:227] op_sel:[0,0] op_sel_hi:[1,0]\n"
    "v_pk_mul_f32 v[232:233], v[232:233], v[226:227] op_sel:[0,0] op_sel_hi:[1,0]\n"
    "v_pk_mul_f32 v[234:235], v[234:235], v[226:227] op_sel:[0,0] op_sel_hi:[1,0]\n"
    "v_cvt_pk_bf16_f32 v244, v228, v229\n"
    "v_cvt_pk_bf16_f32 v245, v230, v231\n"
    "v_cvt_pk_bf16_f32 v246, v232, v233\n"
    "v_cvt_pk_bf16_f32 v247, v234, v235\n"
    "buffer_load_dwordx4 v1, s[8:11], s70 offen offset:2048 lds\n"
    "s_add_u32 s70, s70, 8192\n"
    "ds_write_b128 v9, v[244:247] offset:38912\n"
    "s_waitcnt vmcnt(3) lgkmcnt(0)\n"
    "s_barrier\n"
    "ds_read_b32 v200, v4 offset:39936\n"
    "ds_read_b32 v201, v10 offset:7168\n"
    "ds_read_b32 v202, v10 offset:48128\n"
    "ds_read_b32 v203, v18 offset:15360\n"
    "ds_read_b64_tr_b16 a[224:225], v2 offset:0\n"
    "ds_read_b64_tr_b16 a[226:227], v2 offset:2048\n"
    "ds_read_b64_tr_b16 a[232:233], v3 offset:0\n"
    "ds_read_b64_tr_b16 a[234:235], v3 offset:2048\n"
    "ds_read_b128 a[0:3], v4 offset:32768\n"
    "ds_read_b128 a[28:31], v10 offset:0\n"
    "ds_read_b128 a[4:7], v4 offset:33792\n"
    "buffer_load_dwordx4 v1, s[8:11], s70 offen offset:3072 lds\n"
    "ds_read_b128 a[32:35], v10 offset:1024\n"
    "ds_read_b128 a[8:11], v4 offset:34816\n"
    "ds_read_b128 a[36:39], v10 offset:2048\n"
    "ds_read_b128 a[12:15], v4 offset:35840\n"
    "ds_read_b128 a[40:43], v10 offset:3072\n"
    "ds_read_b128 a[16:19], v4 offset:36864\n"
    "ds_read_b128 a[44:47], v10 offset:4096\n"
    "ds_read_b128 a[20:23], v4 offset:37888\n"
    "ds_read_b128 a[48:51], v10 offset:5120\n"
    "ds_read_b128 a[24:27], v4 offset:38912\n"
    "ds_read_b128 a[52:55], v10 offset:6144\n"
    "ds_read_b64_tr_b16 a[228:229], v2 offset:40960\n"
    "ds_read_b64_tr_b16 a[230:231], v2 offset:43008\n"
    "ds_read_b64_tr_b16 a[236:237], v3 offset:40960\n"
    "ds_read_b64_tr_b16 a[238:239], v3 offset:43008\n"
    "v_accvgpr_read_b32 v236, a56\n"
    "v_accvgpr_read_b32 v237, a57\n"
    "v_accvgpr_read_b32 v238, a58\n"
    "v_accvgpr_read_b32 v239, a59\n"
    "v_accvgpr_read_b32 v240, a60\n"
    "v_accvgpr_read_b32 v241, a61\n"
    "v_accvgpr_read_b32 v242, a62\n"
    "v_accvgpr_read_b32 v243, a63\n"
    "v_pk_mul_f32 v[236:237], v[236:237], v[220:221] op_sel:[0,0] op_sel_hi:[1,0]\n"
    "v_pk_mul_f32 v[238:239], v[238:239], v[220:221] op_sel:[0,0] op_sel_hi:[1,0]\n"
    "v_pk_mul_f32 v[240:241], v[240:241], v[220:221] op_sel:[0,0] op_sel_hi:[1,0]\n"
    "v_pk_mul_f32 v[242:243], v[242:243], v[220:221] op_sel:[0,0] op_sel_hi:[1,0]\n"
    "v_cvt_pk_bf16_f32 v248, v236, v237\n"
    "v_cvt_pk_bf16_f32 v249, v238, v239\n"
    "v_cvt_pk_bf16_f32 v250, v240, v241\n"
    "v_cvt_pk_bf16_f32 v251, v242, v243\n"
    "ds_write_b128 v9, v[248:251] offset:0\n"
    "v_accvgpr_read_b32 v228, a64\n"
    "v_accvgpr_read_b32 v229, a65\n"
    "v_accvgpr_read_b32 v230, a66\n"
    "v_accvgpr_read_b32 v231, a67\n"
    "v_accvgpr_read_b32 v232, a68\n"
    "v_accvgpr_read_b32 v233, a69\n"
    "v_accvgpr_read_b32 v234, a70\n"
    "v_accvgpr_read_b32 v235, a71\n"
    "v_pk_mul_f32 v[228:229], v[228:229], v[220:221] op_sel:[0,1] op_sel_hi:[1,1]\n"
    "v_pk_mul_f32 v[230:231], v[230:231], v[220:221] op_sel:[0,1] op_sel_hi:[1,1]\n"
    "v_pk_mul_f32 v[232:233], v[232:233], v[220:221] op_sel:[0,1] op_sel_hi:[1,1]\n"
    "v_pk_mul_f32 v[234:235], v[234:235], v[220:221] op_sel:[0,1] op_sel_hi:[1,1]\n"
    "v_cvt_pk_bf16_f32 v244, v228, v229\n"
    "v_cvt_pk_bf16_f32 v245, v230, v231\n"
    "v_cvt_pk_bf16_f32 v246, v232, v233\n"
    "v_cvt_pk_bf16_f32 v247, v234, v235\n"
    "ds_write_b128 v9, v[244:247] offset:1024\n"
    "s_waitcnt lgkmcnt(2)\n"
    "v_or_b32 v200, v200, v201\n"
    "v_or3_b32 v200, v200, v202, v203\n"
    "v_cmp_lt_u32 vcc, s81, v200\n"
    "s_nop 1\n"
    "s_cbranch_vccnz LFB_%=\n"
    "s_add_u32 s70, s21, 331776\n"
    "s_add_u32 m0, s60, 20480\n"
    "v_mfma_f32_16x16x32_bf16 v[88:91], a[224:227], a[0:3], 0\n"
    "v_accvgpr_read_b32 v236, a72\n"
    "v_accvgpr_read_b32 v237, a73\n"
    "v_accvgpr_read_b32 v238, a74\n"
    "v_accvgpr_read_b32 v239, a75\n"
    "v_accvgpr_read_b32 v240, a76\n"
    "v_mfma_f32_16x16x32_bf16 v[88:91], a[232:235], a[28:31], v[88:91]\n"
    "buffer_load_dwordx4 v1, s[8:11], s70 offen offset:0 lds\n"
    "v_mfma_f32_16x16x32_bf16 v[92:95], a[224:227], a[4:7], 0\n"
    "s_add_u32 s70, s70, 8192\n"
    "ds_read_b64_tr_b16 a[240:241], v2 offset:4096\n"
    "ds_read_b64_tr_b16 a[242:243], v2 offset:6144\n"
    "v_accvgpr_read_b32 v241, a77\n"
    "v_accvgpr_read_b32 v242, a78\n"
    "v_accvgpr_read_b32 v243, a79\n"
    "v_mul_f32 v236, v222, v236\n"
    "v_mul_f32 v237, v222, v237\n"
    "v_mul_f32 v238, v222, v238\n"
    "v_mul_f32 v239, v222, v239\n"
    "v_mul_f32 v240, v222, v240\n"
    "v_mfma_f32_16x16x32_bf16 v[92:95], a[232:235], a[32:35], v[92:95]\n"
    "ds_read_b128 a[56:59], v10 offset:40960\n"
    "ds_read_b128 a[60:63], v10 offset:41984\n"
    "v_mul_f32 v241, v222, v241\n"
    "v_mul_f32 v242, v222, v242\n"
    "v_mul_f32 v243, v222, v243\n"
    "v_cvt_pk_bf16_f32 v248, v236, v237\n"
    "v_mfma_f32_16x16x32_bf16 v[96:99], a[224:227], a[8:11], 0\n"
    "ds_read_b64_tr_b16 a[244:245], v2 offset:45056\n"
    "ds_read_b64_tr_b16 a[246:247], v2 offset:47104\n"
    "v_cvt_pk_bf16_f32 v249, v238, v239\n"
    "v_cvt_pk_bf16_f32 v250, v240, v241\n"
    "v_cvt_pk_bf16_f32 v251, v242, v243\n"
    "v_accvgpr_read_b32 v228, a80\n"
    "v_mfma_f32_16x16x32_bf16 v[96:99], a[232:235], a[36:39], v[96:99]\n"
    "ds_read_b128 a[64:67], v10 offset:43008\n"
    "ds_read_b128 a[68:71], v10 offset:44032\n"
    "v_accvgpr_read_b32 v229, a81\n"
    "v_accvgpr_read_b32 v230, a82\n"
    "v_accvgpr_read_b32 v231, a83\n"
    "v_accvgpr_read_b32 v232, a84\n"
    "v_mfma_f32_16x16x32_bf16 v[100:103], a[224:227], a[12:15], 0\n"
    "buffer_load_dwordx4 v1, s[8:11], s70 offen offset:1024 lds\n"
    "v_mfma_f32_16x16x32_bf16 v[100:103], a[232:235], a[40:43], v[100:103]\n"
    "s_add_u32 s70, s70, 8192\n"
    "ds_read_b64_tr_b16 a[248:249], v3 offset:4096\n"
    "ds_read_b64_tr_b16 a[250:251], v3 offset:6144\n"
    "v_accvgpr_read_b32 v233, a85\n"
    "v_accvgpr_read_b32 v234, a86\n"
    "v_accvgpr_read_b32 v235, a87\n"
    "v_mul_f32 v228, v223, v228\n"
    "v_mul_f32 v229, v223, v229\n"
    "v_mul_f32 v230, v223, v230\n"
    "v_mul_f32 v231, v223, v231\n"
    "v_mul_f32 v232, v223, v232\n"
    "v_mul_f32 v233, v223, v233\n"
    "v_mfma_f32_16x16x32_bf16 v[104:107], a[224:227], a[16:19], 0\n"
    "ds_write_b128 v9, v[248:251] offset:2048\n"
    "ds_read_b128 a[72:75], v10 offset:45056\n"
    "v_mul_f32 v234, v223, v234\n"
    "v_mul_f32 v235, v223, v235\n"
    "v_cvt_pk_bf16_f32 v244, v228, v229\n"
    "v_cvt_pk_bf16_f32 v245, v230, v231\n"
    "v_mfma_f32_16x16x32_bf16 v[104:107], a[232:235], a[44:47], v[104:107]\n"
    "ds_read_b64_tr_b16 a[252:253], v3 offset:45056\n"
    "ds_read_b64_tr_b16 a[254:255], v3 offset:47104\n"
    "v_cvt_pk_bf16_f32 v246, v232, v233\n"
    "v_cvt_pk_bf16_f32 v247, v234, v235\n"
    "v_accvgpr_read_b32 v236, a88\n"
    "v_accvgpr_read_b32 v237, a89\n"
    "v_mfma_f32_16x16x32_bf16 v[108:111], a[224:227], a[20:23], 0\n"
    "ds_read_b128 a[76:79], v10 offset:46080\n"
    "ds_write_b128 v9, v[244:247] offset:3072\n"
    "v_accvgpr_read_b32 v238, a90\n"
    "v_accvgpr_read_b32 v239, a91\n"
    "v_accvgpr_read_b32 v240, a92\n"
    "v_accvgpr_read_b32 v241, a93\n"
    "v_mfma_f32_16x16x32_bf16 v[108:111], a[232:235], a[48:51], v[108:111]\n"
    "buffer_load_dwordx4 v1, s[8:11], s70 offen offset:2048 lds\n"
    "v_mfma_f32_16x16x32_bf16 v[112:115], a[224:227], a[24:27], 0\n"
    "s_add_u32 s70, s70, 8192\n"
    "ds_read_b128 a[80:83], v10 offset:47104\n"
    "ds_read_b128 a[84:87], v18 offset:8192\n"
    "v_accvgpr_read_b32 v242, a94\n"
    "v_accvgpr_read_b32 v243, a95\n"
    "v_mul_f32 v236, v224, v236\n"
    "v_mul_f32 v237, v224, v237\n"
    "v_mul_f32 v238, v224, v238\n"
    "v_mul_f32 v239, v224, v239\n"
    "v_mul_f32 v240, v224, v240\n"
    "v_mul_f32 v241, v224, v241\n"
    "v_mul_f32 v242, v224, v242\n"
    "v_mfma_f32_16x16x32_bf16 v[112:115], a[232:235], a[52:55], v[112:115]\n"
    "ds_read_b128 a[88:91], v18 offset:9216\n"
    "ds_read_b128 a[92:95], v18 offset:10240\n"
    "v_mul_f32 v243, v224, v243\n"
    "v_cvt_pk_bf16_f32 v248, v236, v237\n"
    "v_cvt_pk_bf16_f32 v249, v238, v239\n"
    "v_cvt_pk_bf16_f32 v250, v240, v241\n"
    "v_mfma_f32_16x16x32_bf16 v[116:119], a[228:231], a[0:3], 0\n"
    "v_cvt_pk_bf16_f32 v251, v242, v243\n"
    "v_accvgpr_read_b32 v228, a96\n"
    "v_accvgpr_read_b32 v229, a97\n"
    "v_accvgpr_read_b32 v230, a98\n"
    "v_mfma_f32_16x16x32_bf16 v[116:119], a[236:239], a[28:31], v[116:119]\n"
    "ds_write_b128 v9, v[248:251] offset:4096\n"
    "v_accvgpr_read_b32 v231, a99\n"
    "v_accvgpr_read_b32 v232, a100\n"
    "v_accvgpr_read_b32 v233, a101\n"
    "v_accvgpr_read_b32 v234, a102\n"
    "v_mfma_f32_16x16x32_bf16 v[120:123], a[228:231], a[4:7], 0\n"
    "buffer_load_dwordx4 v1, s[8:11], s70 offen offset:3072 lds\n"
    "v_mfma_f32_16x16x32_bf16 v[120:123], a[236:239], a[32:35], v[120:123]\n"
    "s_add_u32 s70, s21, 73728\n"
    "v_accvgpr_read_b32 v235, a103\n"
    "v_mul_f32 v228, v225, v228\n"
    "v_mul_f32 v229, v225, v229\n"
    "v_mul_f32 v230, v225, v230\n"
    "v_mul_f32 v231, v225, v231\n"
    "v_mul_f32 v232, v225, v232\n"
    "v_mul_f32 v233, v225, v233\n"
    "v_mul_f32 v234, v225, v234\n"
    "v_mfma_f32_16x16x32_bf16 v[124:127], a[228:231], a[8:11], 0\n"
    "s_add_u32 m0, s60, 24576\n"
    "ds_read_b128 a[96:99], v18 offset:11264\n"
    "ds_read_b128 a[100:103], v18 offset:12288\n"
    "v_mul_f32 v235, v225, v235\n"
    "v_cvt_pk_bf16_f32 v244, v228, v229\n"
    "v_cvt_pk_bf16_f32 v245, v230, v231\n"
    "v_cvt_pk_bf16_f32 v246, v232, v233\n"
    "v_cvt_pk_bf16_f32 v247, v234, v235\n"
    "v_mfma_f32_16x16x32_bf16 v[124:127], a[236:239], a[36:39], v[124:127]\n"
    "ds_write_b128 v9, v[244:247] offset:5120\n"
    "v_accvgpr_read_b32 v236, a104\n"
    "v_accvgpr_read_b32 v237, a105\n"
    "v_accvgpr_read_b32 v238, a106\n"
    "v_accvgpr_read_b32 v239, a107\n"
    "v_mfma_f32_16x16x32_bf16 v[128:131], a[228:231], a[12:15], 0\n"
    "v_accvgpr_read_b32 v240, a108\n"
    "v_accvgpr_read_b32 v241, a109\n"
    "v_accvgpr_read_b32 v242, a110\n"
    "v_accvgpr_read_b32 v243, a111\n"
    "v_mfma_f32_16x16x32_bf16 v[128:131], a[236:239], a[40:43], v[128:131]\n"
    "buffer_load_dwordx4 v1, s[8:11], s70 offen offset:0 lds\n"
    "v_mfma_f32_16x16x32_bf16 v[132:135], a[228:231], a[16:19], 0\n"
    "s_add_u32 s70, s70, 8192\n"
    "ds_read_b128 a[104:107], v18 offset:13312\n"
    "ds_read_b128 a[108:111], v18 offset:14336\n"
    "v_mul_f32 v236, v226, v236\n"
    "v_mul_f32 v237, v226, v237\n"
    "v_mul_f32 v238, v226, v238\n"
    "v_mul_f32 v239, v226, v239\n"
    "v_mul_f32 v240, v226, v240\n"
    "v_mul_f32 v241, v226, v241\n"
    "v_mul_f32 v242, v226, v242\n"
    "v_mul_f32 v243, v226, v243\n"
    "v_mfma_f32_16x16x32_bf16 v[132:135], a[236:239], a[44:47], v[132:135]\n"
    "v_cvt_pk_bf16_f32 v248, v236, v237\n"
    "v_cvt_pk_bf16_f32 v249, v238, v239\n"
    "v_cvt_pk_bf16_f32 v250, v240, v241\n"
    "v_cvt_pk_bf16_f32 v251, v242, v243\n"
    "v_mfma_f32_16x16x32_bf16 v[136:139], a[228:231], a[20:23], 0\n"
    "ds_write_b128 v9, v[248:251] offset:6144\n"
    "v_mfma_f32_16x16x32_bf16 v[136:139], a[236:239], a[48:51], v[136:139]\n"
    "v_mfma_f32_16x16x32_bf16 v[140:143], a[228:231], a[24:27], 0\n"
    "buffer_load_dwordx4 v1, s[8:11], s70 offen offset:1024 lds\n"
    "v_mfma_f32_16x16x32_bf16 v[140:143], a[236:239], a[52:55], v[140:143]\n"
    "s_waitcnt vmcnt(6) lgkmcnt(0)\n"
    "v_mfma_f32_16x16x32_bf16 v[88:91], a[240:243], a[56:59], v[88:91]\n"
    "s_barrier\n"
    "v_accvgpr_read_b32 v228, a112\n"
    "v_accvgpr_read_b32 v229, a113\n"
    "v_accvgpr_read_b32 v230, a114\n"
    "v_accvgpr_read_b32 v231, a115\n"
    "v_accvgpr_read_b32 v232, a116\n"
    "v_accvgpr_read_b32 v233, a117\n"
    "v_mfma_f32_16x16x32_bf16 v[88:91], a[248:251], a[84:87], v[88:91]\n"
    "s_add_u32 s70, s70, 8192\n"
    "ds_read_b64_tr_b16 a[224:225], v2 offset:8192\n"
    "ds_read_b64_tr_b16 a[226:227], v2 offset:10240\n"
    "v_accvgpr_read_b32 v234, a118\n"
    "v_accvgpr_read_b32 v235, a119\n"
    "v_mul_f32 v228, v220, v228\n"
    "v_mul_f32 v229, v220, v229\n"
    "v_mul_f32 v230, v220, v230\n"
    "v_mul_f32 v231, v220, v231\n"
    "v_mfma_f32_16x16x32_bf16 v[92:95], a[240:243], a[60:63], v[92:95]\n"
    "ds_read_b128 a[112:115], v4 offset:0\n"
    "ds_read_b128 a[116:119], v4 offset:1024\n"
    "v_mul_f32 v232, v220, v232\n"
    "v_mul_f32 v233, v220, v233\n"
    "v_mul_f32 v234, v220, v234\n"
    "v_mul_f32 v235, v220, v235\n"
    "v_cvt_pk_bf16_f32 v144, v228, v229\n"
    "v_cvt_pk_bf16_f32 v145, v230, v231\n"
    "v_mfma_f32_16x16x32_bf16 v[92:95], a[248:251], a[88:91], v[92:95]\n"
    "buffer_load_dwordx4 v1, s[8:11], s70 offen offset:2048 lds\n"
    "v_mfma_f32_16x16x32_bf16 v[96:99], a[240:243], a[64:67], v[96:99]\n"
    "s_add_u32 s70, s70, 8192\n"
    "ds_read_b64_tr_b16 a[228:229], v2 offset:49152\n"
    "ds_read_b64_tr_b16 a[230:231], v2 offset:51200\n"
    "v_cvt_pk_bf16_f32 v146, v232, v233\n"
    "v_cvt_pk_bf16_f32 v147, v234, v235\n"
    "v_accvgpr_read_b32 v236, a120\n"
    "v_accvgpr_read_b32 v237, a121\n"
    "v_accvgpr_read_b32 v238, a122\n"
    "v_accvgpr_read_b32 v239, a123\n"
    "v_accvgpr_read_b32 v240, a124\n"
    "v_accvgpr_read_b32 v241, a125\n"
    "v_accvgpr_read_b32 v242, a126\n"
    "v_accvgpr_read_b32 v243, a127\n"
    "v_mul_f32 v236, v221, v236\n"
    "v_mul_f32 v237, v221, v237\n"
    "v_mfma_f32_16x16x32_bf16 v[96:99], a[248:251], a[92:95], v[96:99]\n"
    "ds_write_b128 v9, v[144:147] offset:32768\n"
    "ds_read_b128 a[120:123], v4 offset:2048\n"
    "v_mul_f32 v238, v221, v238\n"
    "v_mul_f32 v239, v221, v239\n"
    "v_mul_f32 v240, v221, v240\n"
    "v_mul_f32 v241, v221, v241\n"
    "v_mul_f32 v242, v221, v242\n"
    "v_mfma_f32_16x16x32_bf16 v[100:103], a[240:243], a[68:71], v[100:103]\n"
    "ds_read_b64_tr_b16 a[232:233], v3 offset:8192\n"
    "ds_read_b64_tr_b16 a[234:235], v3 offset:10240\n"
    "v_mul_f32 v243, v221, v243\n"
    "v_cvt_pk_bf16_f32 v148, v236, v237\n"
    "v_cvt_pk_bf16_f32 v149, v238, v239\n"
    "v_cvt_pk_bf16_f32 v150, v240, v241\n"
    "v_cvt_pk_bf16_f32 v151, v242, v243\n"
    "v_accvgpr_read_b32 v228, a128\n"
    "v_mfma_f32_16x16x32_bf16 v[100:103], a[248:251], a[96:99], v[100:103]\n"
    "ds_write_b128 v9, v[148:151] offset:33792\n"
    "ds_read_b128 a[124:127], v4 offset:3072\n"
    "v_accvgpr_read_b32 v229, a129\n"
    "v_accvgpr_read_b32 v230, a130\n"
    "v_accvgpr_read_b32 v231, a131\n"
    "v_accvgpr_read_b32 v232, a132\n"
    "v_accvgpr_read_b32 v233, a133\n"
    "v_accvgpr_read_b32 v234, a134\n"
    "v_mfma_f32_16x16x32_bf16 v[104:107], a[240:243], a[72:75], v[104:107]\n"
    "buffer_load_dwordx4 v1, s[8:11], s70 offen offset:3072 lds\n"
    "v_mfma_f32_16x16x32_bf16 v[104:107], a[248:251], a[100:103], v[104:107]\n"
    "s_add_u32 s70, s21, 368640\n"
    "ds_read_b64_tr_b16 a[236:237], v3 offset:49152\n"
    "ds_read_b64_tr_b16 a[238:239], v3 offset:51200\n"
    "v_accvgpr_read_b32 v235, a135\n"
    "v_mul_f32 v228, v222, v228\n"
    "v_mul_f32 v229, v222, v229\n"
    "v_mul_f32 v230, v222, v230\n"
    "v_mul_f32 v231, v222, v231\n"
    "v_mul_f32 v232, v222, v232\n"
    "v_mul_f32 v233, v222, v233\n"
    "v_mul_f32 v234, v222, v234\n"
    "v_mul_f32 v235, v222, v235\n"
    "v_cvt_pk_bf16_f32 v152, v228, v229\n"
    "v_cvt_pk_bf16_f32 v153, v230, v231\n"
    "v_cvt_pk_bf16_f32 v154, v232, v233\n"
    "v_mfma_f32_16x16x32_bf16 v[108:111], a[240:243], a[76:79], v[108:111]\n"
    "s_add_u32 m0, s60, 28672\n"
    "ds_read_b128 a[128:131], v4 offset:4096\n"
    "ds_read_b128 a[132:135], v4 offset:5120\n"
    "v_cvt_pk_bf16_f32 v155, v234, v235\n"
    "v_accvgpr_read_b32 v236, a136\n"
    "v_accvgpr_read_b32 v237, a137\n"
    "v_accvgpr_read_b32 v238, a138\n"
    "v_accvgpr_read_b32 v239, a139\n"
    "v_accvgpr_read_b32 v240, a140\n"
    "v_mfma_f32_16x16x32_bf16 v[108:111], a[248:251], a[104:107], v[108:111]\n"
    "ds_write_b128 v9, v[152:155] offset:34816\n"
    "v_accvgpr_read_b32 v241, a141\n"
    "v_accvgpr_read_b32 v242, a142\n"
    "v_accvgpr_read_b32 v243, a143\n"
    "v_mul_f32 v236, v223, v236\n"
    "v_mul_f32 v237, v223, v237\n"
    "v_mfma_f32_16x16x32_bf16 v[112:115], a[240:243], a[80:83], v[112:115]\n"
    "ds_read_b128 a[136:139], v4 offset:6144\n"
    "ds_read_b128 a[140:143], v4 offset:40960\n"
    "v_mul_f32 v238, v223, v238\n"
    "v_mul_f32 v239, v223, v239\n"
    "v_mul_f32 v240, v223, v240\n"
    "v_mul_f32 v241, v223, v241\n"
    "v_mul_f32 v242, v223, v242\n"
    "v_mul_f32 v243, v223, v243\n"
    "v_mfma_f32_16x16x32_bf16 v[112:115], a[248:251], a[108:111], v[112:115]\n"
    "buffer_load_dwordx4 v1, s[8:11], s70 offen offset:0 lds\n"
    "v_mfma_f32_16x16x32_bf16 v[116:119], a[244:247], a[56:59], v[116:119]\n"
    "s_add_u32 s70, s70, 8192\n"
    "v_cvt_pk_bf16_f32 v156, v236, v237\n"
    "v_cvt_pk_bf16_f32 v157, v238, v239\n"
    "v_cvt_pk_bf16_f32 v158, v240, v241\n"
    "v_cvt_pk_bf16_f32 v159, v242, v243\n"
    "v_accvgpr_read_b32 v228, a144\n"
    "v_accvgpr_read_b32 v229, a145\n"
    "v_accvgpr_read_b32 v230, a146\n"
    "v_accvgpr_read_b32 v231, a147\n"
    "v_accvgpr_read_b32 v232, a148\n"
    "v_accvgpr_read_b32 v233, a149\n"
    "v_accvgpr_read_b32 v234, a150\n"
    "v_accvgpr_read_b32 v235, a151\n"
    "v_mfma_f32_16x16x32_bf16 v[116:119], a[252:255], a[84:87], v[116:119]\n"
    "ds_write_b128 v9, v[156:159] offset:35840\n"
    "ds_read_b128 a[144:147], v4 offset:41984\n"
    "v_mul_f32 v228, v224, v228\n"
    "v_mul_f32 v229, v224, v229\n"
    "v_mul_f32 v230, v224, v230\n"
    "v_mul_f32 v231, v224, v231\n"
    "v_mul_f32 v232, v224, v232\n"
    "v_mul_f32 v233, v224, v233\n"
    "v_mfma_f32_16x16x32_bf16 v[120:123], a[244:247], a[60:63], v[120:123]\n"
    "ds_read_b128 a[148:151], v4 offset:43008\n"
    "v_mul_f32 v234, v224, v234\n"
    "v_mul_f32 v235, v224, v235\n"
    "v_cvt_pk_bf16_f32 v160, v228, v229\n"
    "v_cvt_pk_bf16_f32 v161, v230, v231\n"
    "v_cvt_pk_bf16_f32 v162, v232, v233\n"
    "v_cvt_pk_bf16_f32 v163, v234, v235\n"
    "v_mfma_f32_16x16x32_bf16 v[120:123], a[252:255], a[88:91], v[120:123]\n"
    "ds_write_b128 v9, v[160:163] offset:36864\n"
    "v_accvgpr_read_b32 v236, a152\n"
    "v_accvgpr_read_b32 v237, a153\n"
    "v_accvgpr_read_b32 v238, a154\n"
    "v_accvgpr_read_b32 v239, a155\n"
    "v_accvgpr_read_b32 v240, a156\n"
    "v_accvgpr_read_b32 v241, a157\n"
    "v_mfma_f32_16x16x32_bf16 v[124:127], a[244:247], a[64:67], v[124:127]\n"
    "buffer_load_dwordx4 v1, s[8:11], s70 offen offset:1024 lds\n"
    "v_mfma_f32_16x16x32_bf16 v[124:127], a[252:255], a[92:95], v[124:127]\n"
    "s_add_u32 s70, s70, 8192\n"
    "v_accvgpr_read_b32 v242, a158\n"
    "v_accvgpr_read_b32 v243, a159\n"
    "v_mul_f32 v236, v225, v236\n"
    "v_mul_f32 v237, v225, v237\n"
    "v_mul_f32 v238, v225, v238\n"
    "v_mul_f32 v239, v225, v239\n"
    "v_mul_f32 v240, v225, v240\n"
    "v_mul_f32 v241, v225, v241\n"
    "v_mul_f32 v242, v225, v242\n"
    "v_mul_f32 v243, v225, v243\n"
    "v_cvt_pk_bf16_f32 v164, v236, v237\n"
    "v_mfma_f32_16x16x32_bf16 v[128:131], a[244:247], a[68:71], v[128:131]\n"
    "ds_read_b128 a[152:155], v4 offset:44032\n"
    "ds_read_b128 a[156:159], v4 offset:45056\n"
    "v_cvt_pk_bf16_f32 v165, v238, v239\n"
    "v_cvt_pk_bf16_f32 v166, v240, v241\n"
    "v_cvt_pk_bf16_f32 v167, v242, v243\n"
    "v_accvgpr_read_b32 v228, a160\n"
    "v_accvgpr_read_b32 v229, a161\n"
    "v_accvgpr_read_b32 v230, a162\n"
    "v_mfma_f32_16x16x32_bf16 v[128:131], a[252:255], a[96:99], v[128:131]\n"
    "ds_write_b128 v9, v[164:167] offset:37888\n"
    "v_accvgpr_read_b32 v231, a163\n"
    "v_accvgpr_read_b32 v232, a164\n"
    "v_accvgpr_read_b32 v233, a165\n"
    "v_accvgpr_read_b32 v234, a166\n"
    "v_accvgpr_read_b32 v235, a167\n"
    "v_mul_f32 v228, v226, v228\n"
    "v_mfma_f32_16x16x32_bf16 v[132:135], a[244:247], a[72:75], v[132:135]\n"
    "ds_read_b128 a[160:163], v4 offset:46080\n"
    "ds_read_b128 a[164:167], v4 offset:47104\n"
    "v_mul_f32 v229, v226, v229\n"
    "v_mul_f32 v230, v226, v230\n"
    "v_mul_f32 v231, v226, v231\n"
    "v_mul_f32 v232, v226, v232\n"
    "v_mul_f32 v233, v226, v233\n"
    "v_mul_f32 v234, v226, v234\n"
    "v_mfma_f32_16x16x32_bf16 v[132:135], a[252:255], a[100:103], v[132:135]\n"
    "buffer_load_dwordx4 v1, s[8:11], s70 offen offset:2048 lds\n"
    "v_mfma_f32_16x16x32_bf16 v[136:139], a[244:247], a[76:79], v[136:139]\n"
    "s_add_u32 s70, s70, 8192\n"
    "v_mul_f32 v235, v226, v235\n"
    "v_cvt_pk_bf16_f32 v168, v228, v229\n"
    "v_cvt_pk_bf16_f32 v169, v230, v231\n"
    "v_cvt_pk_bf16_f32 v170, v232, v233\n"
    "v_cvt_pk_bf16_f32 v171, v234, v235\n"
    "v_mfma_f32_16x16x32_bf16 v[136:139], a[252:255], a[104:107], v[136:139]\n"
    "ds_write_b128 v9, v[168:171] offset:38912\n"
    "v_mfma_f32_16x16x32_bf16 v[140:143], a[244:247], a[80:83], v[140:143]\n"
    "v_mfma_f32_16x16x32_bf16 v[140:143], a[252:255], a[108:111], v[140:143]\n"
    "s_waitcnt vmcnt(7) lgkmcnt(0)\n"
    "v_mfma_f32_16x16x32_bf16 v[88:91], a[224:227], a[112:115], v[88:91]\n"
    "s_barrier\n"
    "buffer_load_dwordx4 v1, s[8:11], s70 offen offset:3072 lds\n"
    "v_mfma_f32_16x16x32_bf16 v[88:91], a[232:235], a[140:143], v[88:91]\n"
    "s_add_u32 s70, s21, 110592\n"
    "ds_read_b64_tr_b16 a[240:241], v2 offset:12288\n"
    "ds_read_b64_tr_b16 a[242:243], v2 offset:14336\n"
    "v_accvgpr_read_b32 v236, a168\n"
    "v_accvgpr_read_b32 v237, a169\n"
    "v_accvgpr_read_b32 v238, a170\n"
    "v_accvgpr_read_b32 v239, a171\n"
    "v_accvgpr_read_b32 v240, a172\n"
    "v_accvgpr_read_b32 v241, a173\n"
    "v_accvgpr_read_b32 v242, a174\n"
    "v_accvgpr_read_b32 v243, a175\n"
    "v_mul_f32 v236, v220, v236\n"
    "v_mul_f32 v237, v220, v237\n"
    "v_mul_f32 v238, v220, v238\n"
    "v_mul_f32 v239, v220, v239\n"
    "v_mfma_f32_16x16x32_bf16 v[92:95], a[224:227], a[116:119], v[92:95]\n"
    "s_add_u32 m0, s60, 8192\n"
    "ds_read_b128 a[168:171], v10 offset:8192\n"
    "ds_read_b128 a[172:175], v10 offset:9216\n"
    "v_mul_f32 v240, v220, v240\n"
    "v_mul_f32 v241, v220, v241\n"
    "v_mul_f32 v242, v220, v242\n"
    "v_mul_f32 v243, v220, v243\n"
    "v_cvt_pk_bf16_f32 v172, v236, v237\n"
    "v_cvt_pk_bf16_f32 v173, v238, v239\n"
    "v_mfma_f32_16x16x32_bf16 v[92:95], a[232:235], a[144:147], v[92:95]\n"
    "ds_read_b64_tr_b16 a[244:245], v2 offset:53248\n"
    "ds_read_b64_tr_b16 a[246:247], v2 offset:55296\n"
    "v_cvt_pk_bf16_f32 v174, v240, v241\n"
    "v_cvt_pk_bf16_f32 v175, v242, v243\n"
    "v_accvgpr_read_b32 v228, a176\n"
    "v_accvgpr_read_b32 v229, a177\n"
    "v_accvgpr_read_b32 v230, a178\n"
    "v_accvgpr_read_b32 v231, a179\n"
    "v_mfma_f32_16x16x32_bf16 v[96:99], a[224:227], a[120:123], v[96:99]\n"
    "v_accvgpr_read_b32 v232, a180\n"
    "v_accvgpr_read_b32 v233, a181\n"
    "v_accvgpr_read_b32 v234, a182\n"
    "v_accvgpr_read_b32 v235, a183\n"
    "v_mul_f32 v228, v221, v228\n"
    "v_mul_f32 v229, v221, v229\n"
    "v_mfma_f32_16x16x32_bf16 v[96:99], a[232:235], a[148:151], v[96:99]\n"
    "buffer_load_dwordx4 v1, s[8:11], s70 offen offset:0 lds\n"
    "v_mfma_f32_16x16x32_bf16 v[100:103], a[224:227], a[124:127], v[100:103]\n"
    "s_add_u32 s70, s70, 8192\n"
    "ds_read_b64_tr_b16 a[248:249], v3 offset:12288\n"
    "ds_read_b64_tr_b16 a[250:251], v3 offset:14336\n"
    "v_mul_f32 v230, v221, v230\n"
    "v_mul_f32 v231, v221, v231\n"
    "v_mul_f32 v232, v221, v232\n"
    "v_mul_f32 v233, v221, v233\n"
    "v_mul_f32 v234, v221, v234\n"
    "v_mul_f32 v235, v221, v235\n"
    "v_cvt_pk_bf16_f32 v176, v228, v229\n"
    "v_cvt_pk_bf16_f32 v177, v230, v231\n"
    "v_cvt_pk_bf16_f32 v178, v232, v233\n"
    "v_cvt_pk_bf16_f32 v179, v234, v235\n"
    "v_accvgpr_read_b32 v236, a184\n"
    "v_mfma_f32_16x16x32_bf16 v[100:103], a[232:235], a[152:155], v[100:103]\n"
    "ds_read_b128 a[176:179], v10 offset:10240\n"
    "ds_read_b128 a[180:183], v10 offset:11264\n"
    "v_accvgpr_read_b32 v237, a185\n"
    "v_accvgpr_read_b32 v238, a186\n"
    "v_accvgpr_read_b32 v239, a187\n"
    "v_accvgpr_read_b32 v240, a188\n"
    "v_accvgpr_read_b32 v241, a189\n"
    "v_accvgpr_read_b32 v242, a190\n"
    "v_mfma_f32_16x16x32_bf16 v[104:107], a[224:227], a[128:131], v[104:107]\n"
    "ds_read_b64_tr_b16 a[252:253], v3 offset:53248\n"
    "ds_read_b64_tr_b16 a[254:255], v3 offset:55296\n"
    "v_accvgpr_read_b32 v243, a191\n"
    "v_mul_f32 v236, v222, v236\n"
    "v_mul_f32 v237, v222, v237\n"
    "v_mul_f32 v238, v222, v238\n"
    "v_mul_f32 v239, v222, v239\n"
    "v_mul_f32 v240, v222, v240\n"
    "v_mfma_f32_16x16x32_bf16 v[104:107], a[232:235], a[156:159], v[104:107]\n"
    "ds_read_b128 a[184:187], v10 offset:12288\n"
    "ds_read_b128 a[188:191], v10 offset:13312\n"
    "v_mul_f32 v241, v222, v241\n"
    "v_mul_f32 v242, v222, v242\n"
    "v_mul_f32 v243, v222, v243\n"
    "v_cvt_pk_bf16_f32 v180, v236, v237\n"
    "v_cvt_pk_bf16_f32 v181, v238, v239\n"
    "v_cvt_pk_bf16_f32 v182, v240, v241\n"
    "v_mfma_f32_16x16x32_bf16 v[108:111], a[224:227], a[132:135], v[108:111]\n"
    "buffer_load_dwordx4 v1, s[8:11], s70 offen offset:1024 lds\n"
    "v_mfma_f32_16x16x32_bf16 v[108:111], a[232:235], a[160:163], v[108:111]\n"
    "s_add_u32 s70, s70, 8192\n"
    "v_cvt_pk_bf16_f32 v183, v242, v243\n"
    "v_accvgpr_read_b32 v228, a192\n"
    "v_accvgpr_read_b32 v229, a193\n"
    "v_accvgpr_read_b32 v230, a194\n"
    "v_accvgpr_read_b32 v231, a195\n"
    "v_accvgpr_read_b32 v232, a196\n"
    "v_accvgpr_read_b32 v233, a197\n"
    "v_accvgpr_read_b32 v234, a198\n"
    "v_accvgpr_read_b32 v235, a199\n"
    "v_mul_f32 v228, v223, v228\n"
    "v_mul_f32 v229, v223, v229\n"
    "v_mul_f32 v230, v223, v230\n"
    "v_mfma_f32_16x16x32_bf16 v[112:115], a[224:227], a[136:139], v[112:115]\n"
    "ds_read_b128 a[192:195], v10 offset:14336\n"
    "ds_read_b128 a[196:199], v10 offset:49152\n"
    "v_mul_f32 v231, v223, v231\n"
    "v_mul_f32 v232, v223, v232\n"
    "v_mul_f32 v233, v223, v233\n"
    "v_mul_f32 v234, v223, v234\n"
    "v_mul_f32 v235, v223, v235\n"
    "v_mfma_f32_16x16x32_bf16 v[112:115], a[232:235], a[164:167], v[112:115]\n"
    "v_cvt_pk_bf16_f32 v184, v228, v229\n"
    "v_cvt_pk_bf16_f32 v185, v230, v231\n"
    "v_cvt_pk_bf16_f32 v186, v232, v233\n"
    "v_cvt_pk_bf16_f32 v187, v234, v235\n"
    "v_accvgpr_read_b32 v236, a200\n"
    "v_accvgpr_read_b32 v237, a201\n"
    "v_mfma_f32_16x16x32_bf16 v[116:119], a[228:231], a[112:115], v[116:119]\n"
    "v_accvgpr_read_b32 v238, a202\n"
    "v_accvgpr_read_b32 v239, a203\n"
    "v_accvgpr_read_b32 v240, a204\n"
    "v_accvgpr_read_b32 v241, a205\n"
    "v_accvgpr_read_b32 v242, a206\n"
    "v_accvgpr_read_b32 v243, a207\n"
    "v_mfma_f32_16x16x32_bf16 v[116:119], a[236:239], a[140:143], v[116:119]\n"
    "buffer_load_dwordx4 v1, s[8:11], s70 offen offset:2048 lds\n"
    "v_mfma_f32_16x16x32_bf16 v[120:123], a[228:231], a[116:119], v[120:123]\n"
    "s_add_u32 s70, s70, 8192\n"
    "ds_read_b128 a[200:203], v10 offset:50176\n"
    "ds_read_b128 a[204:207], v10 offset:51200\n"
    "v_mul_f32 v236, v224, v236\n"
    "v_mul_f32 v237, v224, v237\n"
    "v_mul_f32 v238, v224, v238\n"
    "v_mul_f32 v239, v224, v239\n"
    "v_mul_f32 v240, v224, v240\n"
    "v_mul_f32 v241, v224, v241\n"
    "v_mul_f32 v242, v224, v242\n"
    "v_mul_f32 v243, v224, v243\n"
    "v_cvt_pk_bf16_f32 v188, v236, v237\n"
    "v_cvt_pk_bf16_f32 v189, v238, v239\n"
    "v_cvt_pk_bf16_f32 v190, v240, v241\n"
    "v_cvt_pk_bf16_f32 v191, v242, v243\n"
    "v_mfma_f32_16x16x32_bf16 v[120:123], a[236:239], a[144:147], v[120:123]\n"
    "v_accvgpr_read_b32 v228, a208\n"
    "v_accvgpr_read_b32 v229, a209\n"
    "v_accvgpr_read_b32 v230, a210\n"
    "v_accvgpr_read_b32 v231, a211\n"
    "v_accvgpr_read_b32 v232, a212\n"
    "v_mfma_f32_16x16x32_bf16 v[124:127], a[228:231], a[120:123], v[124:127]\n"
    "v_accvgpr_read_b32 v233, a213\n"
    "v_accvgpr_read_b32 v234, a214\n"
    "v_accvgpr_read_b32 v235, a215\n"
    "v_mul_f32 v228, v225, v228\n"
    "v_mul_f32 v229, v225, v229\n"
    "v_mul_f32 v230, v225, v230\n"
    "v_mfma_f32_16x16x32_bf16 v[124:127], a[236:239], a[148:151], v[124:127]\n"
    "ds_read_b128 a[208:211], v10 offset:52224\n"
    "ds_read_b128 a[212:215], v10 offset:53248\n"
    "v_mul_f32 v231, v225, v231\n"
    "v_mul_f32 v232, v225, v232\n"
    "v_mul_f32 v233, v225, v233\n"
    "v_mul_f32 v234, v225, v234\n"
    "v_mul_f32 v235, v225, v235\n"
    "v_cvt_pk_bf16_f32 v192, v228, v229\n"
    "v_mfma_f32_16x16x32_bf16 v[128:131], a[228:231], a[124:127], v[128:131]\n"
    "buffer_load_dwordx4 v1, s[8:11], s70 offen offset:3072 lds\n"
    "v_mfma_f32_16x16x32_bf16 v[128:131], a[236:239], a[152:155], v[128:131]\n"
    "s_add_u32 s70, s21, 405504\n"
    "v_cvt_pk_bf16_f32 v193, v230, v231\n"
    "v_cvt_pk_bf16_f32 v194, v232, v233\n"
    "v_cvt_pk_bf16_f32 v195, v234, v235\n"
    "v_accvgpr_read_b32 v236, a216\n"
    "v_accvgpr_read_b32 v237, a217\n"
    "v_accvgpr_read_b32 v238, a218\n"
    "v_accvgpr_read_b32 v239, a219\n"
    "v_accvgpr_read_b32 v240, a220\n"
    "v_accvgpr_read_b32 v241, a221\n"
    "v_accvgpr_read_b32 v242, a222\n"
    "v_accvgpr_read_b32 v243, a223\n"
    "v_mul_f32 v236, v226, v236\n"
    "v_mfma_f32_16x16x32_bf16 v[132:135], a[228:231], a[128:131], v[132:135]\n"
    "s_add_u32 m0, s60, 12288\n"
    "ds_read_b128 a[216:219], v10 offset:54272\n"
    "ds_read_b128 a[220:223], v10 offset:55296\n"
    "v_mul_f32 v237, v226, v237\n"
    "v_mul_f32 v238, v226, v238\n"
    "v_mul_f32 v239, v226, v239\n"
    "v_mul_f32 v240, v226, v240\n"
    "v_mul_f32 v241, v226, v241\n"
    "v_mul_f32 v242, v226, v242\n"
    "v_mfma_f32_16x16x32_bf16 v[132:135], a[236:239], a[156:159], v[132:135]\n"
    "v_mul_f32 v243, v226, v243\n"
    "v_cvt_pk_bf16_f32 v196, v236, v237\n"
    "v_cvt_pk_bf16_f32 v197, v238, v239\n"
    "v_cvt_pk_bf16_f32 v198, v240, v241\n"
    "v_cvt_pk_bf16_f32 v199, v242, v243\n"
    "v_mfma_f32_16x16x32_bf16 v[136:139], a[228:231], a[132:135], v[136:139]\n"
    "v_mfma_f32_16x16x32_bf16 v[136:139], a[236:239], a[160:163], v[136:139]\n"
    "buffer_load_dwordx4 v1, s[8:11], s70 offen offset:0 lds\n"
    "v_mfma_f32_16x16x32_bf16 v[140:143], a[228:231], a[136:139], v[140:143]\n"
    "s_add_u32 s70, s70, 8192\n"
    "v_mfma_f32_16x16x32_bf16 v[140:143], a[236:239], a[164:167], v[140:143]\n"
    "s_waitcnt vmcnt(9) lgkmcnt(0)\n"
    "v_mfma_f32_16x16x32_bf16 v[88:91], a[240:243], a[168:171], v[88:91]\n"
    "s_barrier\n"
    "v_mfma_f32_16x16x32_bf16 v[88:91], a[248:251], a[196:199], v[88:91]\n"
    "ds_read_b64_tr_b16 a[224:225], v2 offset:16384\n"
    "ds_read_b64_tr_b16 a[226:227], v2 offset:18432\n"
    "v_mfma_f32_16x16x32_bf16 v[92:95], a[240:243], a[172:175], v[92:95]\n"
    "buffer_load_dwordx4 v1, s[8:11], s70 offen offset:1024 lds\n"
    "v_mfma_f32_16x16x32_bf16 v[92:95], a[248:251], a[200:203], v[92:95]\n"
    "s_add_u32 s70, s70, 8192\n"
    "ds_read_b64_tr_b16 a[228:229], v2 offset:57344\n"
    "ds_read_b64_tr_b16 a[230:231], v2 offset:59392\n"
    "v_mfma_f32_16x16x32_bf16 v[96:99], a[240:243], a[176:179], v[96:99]\n"
    "ds_write_b128 v9, v[172:175] offset:0\n"
    "v_mfma_f32_16x16x32_bf16 v[96:99], a[248:251], a[204:207], v[96:99]\n"
    "ds_read_b64_tr_b16 a[232:233], v3 offset:16384\n"
    "ds_read_b64_tr_b16 a[234:235], v3 offset:18432\n"
    "v_mfma_f32_16x16x32_bf16 v[100:103], a[240:243], a[180:183], v[100:103]\n"
    "ds_read_b128 v[200:203], v4 offset:32768\n"
    "v_mfma_f32_16x16x32_bf16 v[100:103], a[248:251], a[208:211], v[100:103]\n"
    "buffer_load_dwordx4 v1, s[8:11], s70 offen offset:2048 lds\n"
    "v_mfma_f32_16x16x32_bf16 v[104:107], a[240:243], a[184:187], v[104:107]\n"
    "s_add_u32 s70, s70, 8192\n"
    "ds_read_b64_tr_b16 a[236:237], v3 offset:57344\n"
    "ds_read_b64_tr_b16 a[238:239], v3 offset:59392\n"
    "v_mfma_f32_16x16x32_bf16 v[104:107], a[248:251], a[212:215], v[104:107]\n"
    "ds_read_b128 v[228:231], v10 offset:0\n"
    "v_mfma_f32_16x16x32_bf16 v[108:111], a[240:243], a[188:191], v[108:111]\n"
    "ds_write_b128 v9, v[176:179] offset:1024\n"
    "v_mfma_f32_16x16x32_bf16 v[108:111], a[248:251], a[216:219], v[108:111]\n"
    "ds_read_b128 v[204:207], v4 offset:33792\n"
    "v_mfma_f32_16x16x32_bf16 v[112:115], a[240:243], a[192:195], v[112:115]\n"
    "buffer_load_dwordx4 v1, s[8:11], s70 offen offset:3072 lds\n"
    "v_mfma_f32_16x16x32_bf16 v[112:115], a[248:251], a[220:223], v[112:115]\n"
    "s_add_u32 s70, s52, 0\n"
    "ds_read_b128 v[232:235], v10 offset:1024\n"
    "v_mfma_f32_16x16x32_bf16 v[116:119], a[244:247], a[168:171], v[116:119]\n"
    "s_add_u32 m0, s60, 16384\n"
    "ds_read_b128 v[208:211], v4 offset:34816\n"
    "v_mfma_f32_16x16x32_bf16 v[116:119], a[252:255], a[196:199], v[116:119]\n"
    "ds_read_b128 v[236:239], v10 offset:2048\n"
    "v_mfma_f32_16x16x32_bf16 v[120:123], a[244:247], a[172:175], v[120:123]\n"
    "ds_write_b128 v9, v[180:183] offset:2048\n"
    "v_mfma_f32_16x16x32_bf16 v[120:123], a[252:255], a[200:203], v[120:123]\n"
    "buffer_load_dwordx4 v1, s[8:11], s70 offen offset:0 lds\n"
    "v_mfma_f32_16x16x32_bf16 v[124:127], a[244:247], a[176:179], v[124:127]\n"
    "s_add_u32 s70, s70, 8192\n"
    "ds_read_b128 v[212:215], v4 offset:35840\n"
    "v_mfma_f32_16x16x32_bf16 v[124:127], a[252:255], a[204:207], v[124:127]\n"
    "ds_read_b128 v[240:243], v10 offset:3072\n"
    "v_mfma_f32_16x16x32_bf16 v[128:131], a[244:247], a[180:183], v[128:131]\n"
    "ds_read_b128 v[216:219], v4 offset:36864\n"
    "v_mfma_f32_16x16x32_bf16 v[128:131], a[252:255], a[208:211], v[128:131]\n"
    "ds_read_b128 v[244:247], v10 offset:4096\n"
    "v_mfma_f32_16x16x32_bf16 v[132:135], a[244:247], a[184:187], v[132:135]\n"
    "buffer_load_dwordx4 v1, s[8:11], s70 offen offset:1024 lds\n"
    "v_mfma_f32_16x16x32_bf16 v[132:135], a[252:255], a[212:215], v[132:135]\n"
    "s_add_u32 s70, s70, 8192\n"
    "ds_write_b128 v9, v[184:187] offset:3072\n"
    "v_mfma_f32_16x16x32_bf16 v[136:139], a[244:247], a[188:191], v[136:139]\n"
    "ds_read_b128 v[220:223], v4 offset:37888\n"
    "v_mfma_f32_16x16x32_bf16 v[136:139], a[252:255], a[216:219], v[136:139]\n"
    "ds_read_b128 v[248:251], v10 offset:5120\n"
    "v_mfma_f32_16x16x32_bf16 v[140:143], a[244:247], a[192:195], v[140:143]\n"
    "ds_read_b128 v[224:227], v4 offset:38912\n"
    "v_mfma_f32_16x16x32_bf16 v[140:143], a[252:255], a[220:223], v[140:143]\n"
    "s_waitcnt vmcnt(10) lgkmcnt(0)\n"
    "buffer_load_dwordx4 v1, s[8:11], s70 offen offset:2048 lds\n"
    "v_mfma_f32_16x16x32_bf16 v[88:91], a[224:227], v[200:203], v[88:91]\n"
    "s_barrier\n"
    "ds_read_b128 v[252:255], v10 offset:6144\n"
    "v_mfma_f32_16x16x32_bf16 v[88:91], a[232:235], v[228:231], v[88:91]\n"
    "s_add_u32 s70, s70, 8192\n"
    "ds_read_b64_tr_b16 a[240:241], v2 offset:20480\n"
    "ds_read_b64_tr_b16 a[242:243], v2 offset:22528\n"
    "v_mfma_f32_16x16x32_bf16 v[92:95], a[224:227], v[204:207], v[92:95]\n"
    "ds_write_b128 v9, v[188:191] offset:4096\n"
    "v_mfma_f32_16x16x32_bf16 v[92:95], a[232:235], v[232:235], v[92:95]\n"
    "ds_read_b64_tr_b16 a[244:245], v2 offset:61440\n"
    "ds_read_b64_tr_b16 a[246:247], v2 offset:63488\n"
    "v_mfma_f32_16x16x32_bf16 v[96:99], a[224:227], v[208:211], v[96:99]\n"
    "s_lshl_b32 s72, s20, 3\n"
    "buffer_load_dwordx4 v1, s[8:11], s70 offen offset:3072 lds\n"
    "v_mfma_f32_16x16x32_bf16 v[96:99], a[232:235], v[236:239], v[96:99]\n"
    "s_add_u32 s70, s52, 147456\n"
    "ds_read_b64_tr_b16 a[248:249], v3 offset:20480\n"
    "ds_read_b64_tr_b16 a[250:251], v3 offset:22528\n"
    "v_mfma_f32_16x16x32_bf16 v[100:103], a[224:227], v[212:215], v[100:103]\n"
    "s_add_u32 m0, s60, 20480\n"
    "ds_read_b128 v[32:35], v10 offset:40960\n"
    "v_mfma_f32_16x16x32_bf16 v[100:103], a[232:235], v[240:243], v[100:103]\n"
    "ds_read_b64_tr_b16 a[252:253], v3 offset:61440\n"
    "ds_read_b64_tr_b16 a[254:255], v3 offset:63488\n"
    "v_mfma_f32_16x16x32_bf16 v[104:107], a[224:227], v[216:219], v[104:107]\n"
    "ds_write_b128 v9, v[192:195] offset:5120\n"
    "v_mfma_f32_16x16x32_bf16 v[104:107], a[232:235], v[244:247], v[104:107]\n"
    "buffer_load_dwordx4 v1, s[8:11], s70 offen offset:0 lds\n"
    "v_mfma_f32_16x16x32_bf16 v[108:111], a[224:227], v[220:223], v[108:111]\n"
    "s_add_u32 s70, s70, 8192\n"
    "ds_read_b128 v[60:63], v18 offset:8192\n"
    "v_mfma_f32_16x16x32_bf16 v[108:111], a[232:235], v[248:251], v[108:111]\n"
    "ds_read_b128 v[36:39], v10 offset:41984\n"
    "v_mfma_f32_16x16x32_bf16 v[112:115], a[224:227], v[224:227], v[112:115]\n"
    "s_waitcnt lgkmcnt(13)\n"
    "ds_read_b128 v[64:67], v18 offset:9216\n"
    "v_mfma_f32_16x16x32_bf16 v[112:115], a[232:235], v[252:255], v[112:115]\n"
    "ds_read_b128 v[40:43], v10 offset:43008\n"
    "v_mfma_f32_16x16x32_bf16 v[116:119], a[228:231], v[200:203], v[116:119]\n"
    "buffer_load_dwordx4 v1, s[8:11], s70 offen offset:1024 lds\n"
    "v_mfma_f32_16x16x32_bf16 v[116:119], a[236:239], v[228:231], v[116:119]\n"
    "s_add_u32 s70, s70, 8192\n"
    "ds_write_b128 v9, v[196:199] offset:6144\n"
    "v_mfma_f32_16x16x32_bf16 v[120:123], a[228:231], v[204:207], v[120:123]\n"
    "ds_read_b128 v[68:71], v18 offset:10240\n"
    "v_mfma_f32_16x16x32_bf16 v[120:123], a[236:239], v[232:235], v[120:123]\n"
    "ds_read_b128 v[44:47], v10 offset:44032\n"
    "v_mfma_f32_16x16x32_bf16 v[124:127], a[228:231], v[208:211], v[124:127]\n"
    "ds_read_b128 v[72:75], v18 offset:11264\n"
    "v_mfma_f32_16x16x32_bf16 v[124:127], a[236:239], v[236:239], v[124:127]\n"
    "buffer_load_dwordx4 v1, s[8:11], s70 offen offset:2048 lds\n"
    "v_mfma_f32_16x16x32_bf16 v[128:131], a[228:231], v[212:215], v[128:131]\n"
    "s_add_u32 s70, s70, 8192\n"
    "ds_read_b128 v[48:51], v10 offset:45056\n"
    "v_mfma_f32_16x16x32_bf16 v[128:131], a[236:239], v[240:243], v[128:131]\n"
    "ds_read_b128 v[76:79], v18 offset:12288\n"
    "v_mfma_f32_16x16x32_bf16 v[132:135], a[228:231], v[216:219], v[132:135]\n"
    "ds_read_b128 v[52:55], v10 offset:46080\n"
    "v_mfma_f32_16x16x32_bf16 v[132:135], a[236:239], v[244:247], v[132:135]\n"
    "ds_read_b128 v[80:83], v18 offset:13312\n"
    "v_mfma_f32_16x16x32_bf16 v[136:139], a[228:231], v[220:223], v[136:139]\n"
    "buffer_load_dwordx4 v1, s[8:11], s70 offen offset:3072 lds\n"
    "v_mfma_f32_16x16x32_bf16 v[136:139], a[236:239], v[248:251], v[136:139]\n"
    "s_add_u32 s70, s52, 294912\n"
    "ds_read_b128 v[56:59], v10 offset:47104\n"
    "v_mfma_f32_16x16x32_bf16 v[140:143], a[228:231], v[224:227], v[140:143]\n"
    "s_add_u32 m0, s60, 24576\n"
    "ds_read_b128 v[84:87], v18 offset:14336\n"
    "v_mfma_f32_16x16x32_bf16 v[140:143], a[236:239], v[252:255], v[140:143]\n"
    "s_waitcnt vmcnt(12) lgkmcnt(0)\n"
    "v_mfma_f32_16x16x32_bf16 v[88:91], a[240:243], v[32:35], v[88:91]\n"
    "s_barrier\n"
    "v_mfma_f32_16x16x32_bf16 v[88:91], a[248:251], v[60:63], v[88:91]\n"
    "buffer_load_dwordx4 v1, s[8:11], s70 offen offset:0 lds\n"
    "v_mfma_f32_16x16x32_bf16 v[92:95], a[240:243], v[36:39], v[92:95]\n"
    "s_add_u32 s70, s70, 8192\n"
    "ds_read_b64_tr_b16 a[224:225], v2 offset:0\n"
    "ds_read_b64_tr_b16 a[226:227], v2 offset:2048\n"
    "v_mfma_f32_16x16x32_bf16 v[92:95], a[248:251], v[64:67], v[92:95]\n"
    "ds_read_b128 v[200:203], v4 offset:0\n"
    "v_mov_b32 v20, 0\n"
    "v_mfma_f32_16x16x32_bf16 v[96:99], a[240:243], v[40:43], v[96:99]\n"
    "ds_read_b64_tr_b16 a[228:229], v2 offset:40960\n"
    "ds_read_b64_tr_b16 a[230:231], v2 offset:43008\n"
    "v_mfma_f32_16x16x32_bf16 v[96:99], a[248:251], v[68:71], v[96:99]\n"
    "ds_read_b128 v[228:231], v4 offset:40960\n"
    "v_mov_b32 v21, 0\n"
    "v_mfma_f32_16x16x32_bf16 v[100:103], a[240:243], v[44:47], v[100:103]\n"
    "buffer_load_dwordx4 v1, s[8:11], s70 offen offset:1024 lds\n"
    "v_mfma_f32_16x16x32_bf16 v[100:103], a[248:251], v[72:75], v[100:103]\n"
    "s_add_u32 s70, s70, 8192\n"
    "ds_read_b64_tr_b16 a[232:233], v3 offset:0\n"
    "ds_read_b64_tr_b16 a[234:235], v3 offset:2048\n"
    "v_mfma_f32_16x16x32_bf16 v[104:107], a[240:243], v[48:51], v[104:107]\n"
    "ds_read_b128 v[204:207], v4 offset:1024\n"
    "v_mov_b32 v22, v16\n"
    "v_mfma_f32_16x16x32_bf16 v[104:107], a[248:251], v[76:79], v[104:107]\n"
    "ds_read_b64_tr_b16 a[236:237], v3 offset:40960\n"
    "ds_read_b64_tr_b16 a[238:239], v3 offset:43008\n"
    "v_mfma_f32_16x16x32_bf16 v[108:111], a[240:243], v[52:55], v[108:111]\n"
    "ds_read_b128 v[232:235], v4 offset:41984\n"
    "v_mov_b32 v23, v17\n"
    "v_mfma_f32_16x16x32_bf16 v[108:111], a[248:251], v[80:83], v[108:111]\n"
    "buffer_load_dwordx4 v1, s[8:11], s70 offen offset:2048 lds\n"
    "v_mfma_f32_16x16x32_bf16 v[112:115], a[240:243], v[56:59], v[112:115]\n"
    "s_add_u32 s70, s70, 8192\n"
    "ds_read_b128 v[208:211], v4 offset:2048\n"
    "v_mov_b32 v24, 0\n"
    "v_mfma_f32_16x16x32_bf16 v[112:115], a[248:251], v[84:87], v[112:115]\n"
    "ds_read_b128 v[236:239], v4 offset:43008\n"
    "v_mov_b32 v25, 0\n"
    "v_mfma_f32_16x16x32_bf16 v[116:119], a[244:247], v[32:35], v[116:119]\n"
    "ds_read_b128 v[212:215], v4 offset:3072\n"
    "v_mov_b32 v26, v16\n"
    "v_mfma_f32_16x16x32_bf16 v[116:119], a[252:255], v[60:63], v[116:119]\n"
    "ds_read_b128 v[240:243], v4 offset:44032\n"
    "v_mov_b32 v27, v17\n"
    "v_mfma_f32_16x16x32_bf16 v[120:123], a[244:247], v[36:39], v[120:123]\n"
    "buffer_load_dwordx4 v1, s[8:11], s70 offen offset:3072 lds\n"
    "v_mfma_f32_16x16x32_bf16 v[120:123], a[252:255], v[64:67], v[120:123]\n"
    "s_add_u32 s70, s52, 442368\n"
    "ds_read_b128 v[216:219], v4 offset:4096\n"
    "v_mov_b32 v28, v16\n"
    "v_mfma_f32_16x16x32_bf16 v[124:127], a[244:247], v[40:43], v[124:127]\n"
    "s_add_u32 m0, s60, 28672\n"
    "ds_read_b128 v[244:247], v4 offset:45056\n"
    "v_mov_b32 v29, v17\n"
    "v_mfma_f32_16x16x32_bf16 v[124:127], a[252:255], v[68:71], v[124:127]\n"
    "ds_read_b128 v[220:223], v4 offset:5120\n"
    "v_mov_b32 v30, s68\n"
    "v_mfma_f32_16x16x32_bf16 v[128:131], a[244:247], v[44:47], v[128:131]\n"
    "ds_read_b128 v[248:251], v4 offset:46080\n"
    "v_mov_b32 v31, s69\n"
    "v_mfma_f32_16x16x32_bf16 v[128:131], a[252:255], v[72:75], v[128:131]\n"
    "buffer_load_dwordx4 v1, s[8:11], s70 offen offset:0 lds\n"
    "v_mfma_f32_16x16x32_bf16 v[132:135], a[244:247], v[48:51], v[132:135]\n"
    "s_add_u32 s70, s70, 8192\n"
    "ds_read_b128 v[224:227], v4 offset:6144\n"
    "v_mov_b32 v5, 0\n"
    "v_mfma_f32_16x16x32_bf16 v[132:135], a[252:255], v[76:79], v[132:135]\n"
    "ds_read_b128 v[252:255], v4 offset:47104\n"
    "v_lshrrev_b32 v6, 1, v0\n"
    "v_mfma_f32_16x16x32_bf16 v[136:139], a[244:247], v[52:55], v[136:139]\n"
    "ds_read_b128 v[32:35], v10 offset:8192\n"
    "v_and_b32 v7, 56, v6\n"
    "v_mfma_f32_16x16x32_bf16 v[136:139], a[252:255], v[80:83], v[136:139]\n"
    "ds_read_b128 v[60:63], v10 offset:49152\n"
    "v_and_b32 v196, 8, v6\n"
    "v_mfma_f32_16x16x32_bf16 v[140:143], a[244:247], v[56:59], v[140:143]\n"
    "buffer_load_dwordx4 v1, s[8:11], s70 offen offset:1024 lds\n"
    "v_mfma_f32_16x16x32_bf16 v[140:143], a[252:255], v[84:87], v[140:143]\n"
    "s_waitcnt vmcnt(14) lgkmcnt(13)\n"
    "ds_read_b128 v[36:39], v10 offset:9216\n"
    "v_sub_u32 v196, s72, v196\n"
    "v_mfma_f32_16x16x32_bf16 v[88:91], a[224:227], v[200:203], v[88:91]\n"
    "s_barrier\n"
    "ds_read_b128 v[64:67], v10 offset:50176\n"
    "v_subrev_u32 v8, 16, v196\n"
    "v_mfma_f32_16x16x32_bf16 v[88:91], a[232:235], v[228:231], v[88:91]\n"
    "s_add_u32 s70, s70, 8192\n"
    "ds_read_b64_tr_b16 a[240:241], v2 offset:4096\n"
    "ds_read_b64_tr_b16 a[242:243], v2 offset:6144\n"
    "v_mfma_f32_16x16x32_bf16 v[92:95], a[224:227], v[204:207], v[92:95]\n"
    "s_waitcnt lgkmcnt(15)\n"
    "ds_read_b128 v[40:43], v10 offset:10240\n"
    "v_and_b32 v8, 56, v8\n"
    "v_mfma_f32_16x16x32_bf16 v[92:95], a[232:235], v[232:235], v[92:95]\n"
    "s_waitcnt lgkmcnt(15)\n"
    "buffer_load_dwordx4 v1, s[8:11], s70 offen offset:2048 lds\n"
    "v_mfma_f32_16x16x32_bf16 v[96:99], a[224:227], v[208:211], v[96:99]\n"
    "s_waitcnt lgkmcnt(15)\n"
    "s_add_u32 s70, s70, 8192\n"
    "ds_read_b64_tr_b16 a[244:245], v2 offset:45056\n"
    "ds_read_b64_tr_b16 a[246:247], v2 offset:47104\n"
    "v_mfma_f32_16x16x32_bf16 v[96:99], a[232:235], v[236:239], v[96:99]\n"
    "s_waitcnt lgkmcnt(15)\n"
    "ds_read_b128 v[68:71], v10 offset:51200\n"
    "v_subrev_u32 v19, 32, v196\n"
    "v_mfma_f32_16x16x32_bf16 v[100:103], a[224:227], v[212:215], v[100:103]\n"
    "s_waitcnt lgkmcnt(15)\n"
    "ds_read_b64_tr_b16 a[248:249], v3 offset:4096\n"
    "ds_read_b64_tr_b16 a[250:251], v3 offset:6144\n"
    "v_mfma_f32_16x16x32_bf16 v[100:103], a[232:235], v[240:243], v[100:103]\n"
    "s_waitcnt lgkmcnt(15)\n"
    "ds_read_b128 v[44:47], v10 offset:11264\n"
    "v_and_b32 v19, 56, v19\n"
    "v_mfma_f32_16x16x32_bf16 v[104:107], a[224:227], v[216:219], v[104:107]\n"
    "s_waitcnt lgkmcnt(15)\n"
    "buffer_load_dwordx4 v1, s[8:11], s70 offen offset:3072 lds\n"
    "v_mfma_f32_16x16x32_bf16 v[104:107], a[232:235], v[244:247], v[104:107]\n"
    "s_waitcnt lgkmcnt(15)\n"
    "s_add_u32 s70, s52, 36864\n"
    "ds_read_b64_tr_b16 a[252:253], v3 offset:45056\n"
    "ds_read_b64_tr_b16 a[254:255], v3 offset:47104\n"
    "v_mfma_f32_16x16x32_bf16 v[108:111], a[224:227], v[220:223], v[108:111]\n"
    "s_waitcnt lgkmcnt(15)\n"
    "s_add_u32 m0, s60, 8192\n"
    "ds_read_b128 v[72:75], v10 offset:52224\n"
    "v_subrev_u32 v196, 48, v196\n"
    "v_mfma_f32_16x16x32_bf16 v[108:111], a[232:235], v[248:251], v[108:111]\n"
    "s_waitcnt lgkmcnt(15)\n"
    "ds_read_b128 v[48:51], v10 offset:12288\n"
    "v_and_b32 v196, 56, v196\n"
    "v_mfma_f32_16x16x32_bf16 v[112:115], a[224:227], v[224:227], v[112:115]\n"
    "s_waitcnt lgkmcnt(15)\n"
    "ds_read_b128 v[76:79], v10 offset:53248\n"
    "v_mfma_f32_16x16x32_bf16 v[112:115], a[232:235], v[252:255], v[112:115]\n"
    "buffer_load_dwordx4 v1, s[8:11], s70 offen offset:0 lds\n"
    "v_mfma_f32_16x16x32_bf16 v[116:119], a[228:231], v[200:203], v[116:119]\n"
    "s_add_u32 s70, s70, 8192\n"
    "ds_read_b128 v[52:55], v10 offset:13312\n"
    "v_mfma_f32_16x16x32_bf16 v[116:119], a[236:239], v[228:231], v[116:119]\n"
    "ds_read_b128 v[80:83], v10 offset:54272\n"
    "v_mfma_f32_16x16x32_bf16 v[120:123], a[228:231], v[204:207], v[120:123]\n"
    "ds_read_b128 v[56:59], v10 offset:14336\n"
    "v_mfma_f32_16x16x32_bf16 v[120:123], a[236:239], v[232:235], v[120:123]\n"
    "ds_read_b128 v[84:87], v10 offset:55296\n"
    "v_mfma_f32_16x16x32_bf16 v[124:127], a[228:231], v[208:211], v[124:127]\n"
    "buffer_load_dwordx4 v1, s[8:11], s70 offen offset:1024 lds\n"
    "v_mfma_f32_16x16x32_bf16 v[124:127], a[236:239], v[236:239], v[124:127]\n"
    "s_add_u32 s70, s70, 8192\n"
    "ds_read_b128 v[200:203], v4 offset:32768\n"
    "v_mfma_f32_16x16x32_bf16 v[128:131], a[228:231], v[212:215], v[128:131]\n"
    "ds_read_b128 v[204:207], v4 offset:33792\n"
    "v_mfma_f32_16x16x32_bf16 v[128:131], a[236:239], v[240:243], v[128:131]\n"
    "ds_read_b128 v[208:211], v4 offset:34816\n"
    "v_mfma_f32_16x16x32_bf16 v[132:135], a[228:231], v[216:219], v[132:135]\n"
    "v_mfma_f32_16x16x32_bf16 v[132:135], a[236:239], v[244:247], v[132:135]\n"
    "buffer_load_dwordx4 v1, s[8:11], s70 offen offset:2048 lds\n"
    "v_mfma_f32_16x16x32_bf16 v[136:139], a[228:231], v[220:223], v[136:139]\n"
    "s_add_u32 s70, s70, 8192\n"
    "ds_read_b128 v[212:215], v4 offset:35840\n"
    "v_mfma_f32_16x16x32_bf16 v[136:139], a[236:239], v[248:251], v[136:139]\n"
    "ds_read_b128 v[216:219], v4 offset:36864\n"
    "v_mfma_f32_16x16x32_bf16 v[140:143], a[228:231], v[224:227], v[140:143]\n"
    "v_mfma_f32_16x16x32_bf16 v[140:143], a[236:239], v[252:255], v[140:143]\n"
    "s_waitcnt vmcnt(15) lgkmcnt(12)\n"
    "ds_read_b128 v[220:223], v4 offset:37888\n"
    "v_mfma_f32_16x16x32_bf16 v[88:91], a[240:243], v[32:35], v[88:91]\n"
    "s_barrier\n"
    "buffer_load_dwordx4 v1, s[8:11], s70 offen offset:3072 lds\n"
    "v_mfma_f32_16x16x32_bf16 v[88:91], a[248:251], v[60:63], v[88:91]\n"
    "s_add_u32 s70, s52, 184320\n"
    "ds_read_b64_tr_b16 a[224:225], v2 offset:8192\n"
    "ds_read_b64_tr_b16 a[226:227], v2 offset:10240\n"
    "v_mfma_f32_16x16x32_bf16 v[92:95], a[240:243], v[36:39], v[92:95]\n"
    "s_add_u32 m0, s60, 12288\n"
    "ds_read_b128 v[224:227], v4 offset:38912\n"
    "v_mfma_f32_16x16x32_bf16 v[92:95], a[248:251], v[64:67], v[92:95]\n"
    "ds_read_b64_tr_b16 a[228:229], v2 offset:49152\n"
    "ds_read_b64_tr_b16 a[230:231], v2 offset:51200\n"
    "v_mfma_f32_16x16x32_bf16 v[96:99], a[240:243], v[40:43], v[96:99]\n"
    "v_cvt_pk_bf16_f32 v88, v88, v89\n"
    "v_cvt_pk_bf16_f32 v89, v90, v91\n"
    "v_mfma_f32_16x16x32_bf16 v[96:99], a[248:251], v[68:71], v[96:99]\n"
    "buffer_load_dwordx4 v1, s[8:11], s70 offen offset:0 lds\n"
    "v_mfma_f32_16x16x32_bf16 v[100:103], a[240:243], v[44:47], v[100:103]\n"
    "s_waitcnt lgkmcnt(15)\n"
    "s_add_u32 s70, s70, 8192\n"
    "ds_read_b64_tr_b16 a[232:233], v3 offset:8192\n"
    "ds_read_b64_tr_b16 a[234:235], v3 offset:10240\n"
    "v_mfma_f32_16x16x32_bf16 v[100:103], a[248:251], v[72:75], v[100:103]\n"
    "s_waitcnt lgkmcnt(15)\n"
    "v_cvt_pk_bf16_f32 v90, v92, v93\n"
    "v_cvt_pk_bf16_f32 v91, v94, v95\n"
    "v_mfma_f32_16x16x32_bf16 v[104:107], a[240:243], v[48:51], v[104:107]\n"
    "s_waitcnt lgkmcnt(15)\n"
    "ds_read_b64_tr_b16 a[236:237], v3 offset:49152\n"
    "ds_read_b64_tr_b16 a[238:239], v3 offset:51200\n"
    "v_mfma_f32_16x16x32_bf16 v[104:107], a[248:251], v[76:79], v[104:107]\n"
    "s_waitcnt lgkmcnt(15)\n"
    "v_cvt_pk_bf16_f32 v92, v96, v97\n"
    "v_cvt_pk_bf16_f32 v93, v98, v99\n"
    "v_mfma_f32_16x16x32_bf16 v[108:111], a[240:243], v[52:55], v[108:111]\n"
    "s_waitcnt lgkmcnt(15)\n"
    "buffer_load_dwordx4 v1, s[8:11], s70 offen offset:1024 lds\n"
    "v_mfma_f32_16x16x32_bf16 v[108:111], a[248:251], v[80:83], v[108:111]\n"
    "s_waitcnt lgkmcnt(15)\n"
    "s_add_u32 s70, s70, 8192\n"
    "v_cvt_pk_bf16_f32 v94, v100, v101\n"
    "v_cvt_pk_bf16_f32 v95, v102, v103\n"
    "v_mfma_f32_16x16x32_bf16 v[112:115], a[240:243], v[56:59], v[112:115]\n"
    "s_waitcnt lgkmcnt(15)\n"
    "s_add_u32 s83, s40, 1048576\n"
    "v_cvt_pk_bf16_f32 v96, v104, v105\n"
    "v_cvt_pk_bf16_f32 v97, v106, v107\n"
    "v_mfma_f32_16x16x32_bf16 v[112:115], a[248:251], v[84:87], v[112:115]\n"
    "buffer_store_dwordx2 v[88:89], v6, s[12:15], s83 offen offset:0 sc1\n"
    "v_mfma_f32_16x16x32_bf16 v[116:119], a[244:247], v[32:35], v[116:119]\n"
    "v_cvt_pk_bf16_f32 v98, v108, v109\n"
    "v_cvt_pk_bf16_f32 v99, v110, v111\n"
    "v_mfma_f32_16x16x32_bf16 v[116:119], a[252:255], v[60:63], v[116:119]\n"
    "v_cvt_pk_bf16_f32 v100, v112, v113\n"
    "v_cvt_pk_bf16_f32 v101, v114, v115\n"
    "v_mfma_f32_16x16x32_bf16 v[120:123], a[244:247], v[36:39], v[120:123]\n"
    "buffer_load_dwordx4 v1, s[8:11], s70 offen offset:2048 lds\n"
    "v_mfma_f32_16x16x32_bf16 v[120:123], a[252:255], v[64:67], v[120:123]\n"
    "s_add_u32 s70, s70, 8192\n"
    "ds_read_b128 v[228:231], v10 offset:0\n"
    "v_cvt_pk_bf16_f32 v116, v116, v117\n"
    "v_mfma_f32_16x16x32_bf16 v[124:127], a[244:247], v[40:43], v[124:127]\n"
    "v_cvt_pk_bf16_f32 v117, v118, v119\n"
    "v_mfma_f32_16x16x32_bf16 v[124:127], a[252:255], v[68:71], v[124:127]\n"
    "ds_read_b128 v[232:235], v10 offset:1024\n"
    "v_cvt_pk_bf16_f32 v118, v120, v121\n"
    "v_mfma_f32_16x16x32_bf16 v[128:131], a[244:247], v[44:47], v[128:131]\n"
    "buffer_store_dwordx2 v[90:91], v6, s[12:15], s83 offen offset:512 sc1\n"
    "v_mfma_f32_16x16x32_bf16 v[128:131], a[252:255], v[72:75], v[128:131]\n"
    "ds_read_b128 v[236:239], v10 offset:2048\n"
    "v_cvt_pk_bf16_f32 v119, v122, v123\n"
    "v_mfma_f32_16x16x32_bf16 v[132:135], a[244:247], v[48:51], v[132:135]\n"
    "v_cvt_pk_bf16_f32 v120, v124, v125\n"
    "v_cvt_pk_bf16_f32 v121, v126, v127\n"
    "v_mfma_f32_16x16x32_bf16 v[132:135], a[252:255], v[76:79], v[132:135]\n"
    "buffer_load_dwordx4 v1, s[8:11], s70 offen offset:3072 lds\n"
    "v_mfma_f32_16x16x32_bf16 v[136:139], a[244:247], v[52:55], v[136:139]\n"
    "ds_read_b128 v[240:243], v10 offset:3072\n"
    "v_cvt_pk_bf16_f32 v122, v128, v129\n"
    "v_mfma_f32_16x16x32_bf16 v[136:139], a[252:255], v[80:83], v[136:139]\n"
    "ds_read_b128 v[244:247], v10 offset:4096\n"
    "v_cvt_pk_bf16_f32 v123, v130, v131\n"
    "v_mfma_f32_16x16x32_bf16 v[140:143], a[244:247], v[56:59], v[140:143]\n"
    "v_cvt_pk_bf16_f32 v124, v132, v133\n"
    "v_cvt_pk_bf16_f32 v125, v134, v135\n"
    "v_mfma_f32_16x16x32_bf16 v[140:143], a[252:255], v[84:87], v[140:143]\n"
    "s_waitcnt vmcnt(18) lgkmcnt(5)\n"
    "ds_read_b128 v[248:251], v10 offset:5120\n"
    "v_cvt_pk_bf16_f32 v126, v136, v137\n"
    "v_mfma_f32_16x16x32_bf16 v[32:35], a[224:227], a[0:3], 0\n"
    "s_barrier\n"
    "v_cvt_pk_bf16_f32 v127, v138, v139\n"
    "v_mfma_f32_16x16x32_bf16 v[32:35], a[232:235], v[200:203], v[32:35]\n"
    "buffer_store_dwordx2 v[92:93], v6, s[12:15], s83 offen offset:1024 sc1\n"
    "v_mfma_f32_16x16x32_bf16 v[36:39], a[224:227], a[4:7], 0\n"
    "s_add_u32 s70, s52, 331776\n"
    "ds_read_b64_tr_b16 a[240:241], v2 offset:12288\n"
    "v_cvt_pk_bf16_f32 v128, v140, v141\n"
    "v_mfma_f32_16x16x32_bf16 v[36:39], a[232:235], v[204:207], v[36:39]\n"
    "s_add_u32 m0, s60, 16384\n"
    "ds_read_b64_tr_b16 a[242:243], v2 offset:14336\n"
    "v_cvt_pk_bf16_f32 v129, v142, v143\n"
    "v_mfma_f32_16x16x32_bf16 v[40:43], a[224:227], a[8:11], 0\n"
    "buffer_load_dwordx4 v1, s[8:11], s70 offen offset:0 lds\n"
    "v_mfma_f32_16x16x32_bf16 v[40:43], a[232:235], v[208:211], v[40:43]\n"
    "s_add_u32 s70, s70, 8192\n"
    "ds_read_b64_tr_b16 a[244:245], v2 offset:53248\n"
    "v_mfma_f32_16x16x32_bf16 v[44:47], a[224:227], a[12:15], 0\n"
    "ds_read_b64_tr_b16 a[246:247], v2 offset:55296\n"
    "v_mfma_f32_16x16x32_bf16 v[44:47], a[232:235], v[212:215], v[44:47]\n"
    "ds_read_b64_tr_b16 a[248:249], v3 offset:12288\n"
    "v_mfma_f32_16x16x32_bf16 v[48:51], a[224:227], a[16:19], 0\n"
    "ds_read_b64_tr_b16 a[250:251], v3 offset:14336\n"
    "v_mfma_f32_16x16x32_bf16 v[48:51], a[232:235], v[216:219], v[48:51]\n"
    "buffer_load_dwordx4 v1, s[8:11], s70 offen offset:1024 lds\n"
    "v_mfma_f32_16x16x32_bf16 v[52:55], a[224:227], a[20:23], 0\n"
    "s_add_u32 s70, s70, 8192\n"
    "ds_read_b64_tr_b16 a[252:253], v3 offset:53248\n"
    "v_mfma_f32_16x16x32_bf16 v[52:55], a[232:235], v[220:223], v[52:55]\n"
    "buffer_store_dwordx2 v[94:95], v6, s[12:15], s83 offen offset:1536 sc1\n"
    "v_mfma_f32_16x16x32_bf16 v[56:59], a[224:227], a[24:27], 0\n"
    "ds_read_b64_tr_b16 a[254:255], v3 offset:55296\n"
    "v_mfma_f32_16x16x32_bf16 v[56:59], a[232:235], v[224:227], v[56:59]\n"
    "ds_read_b128 v[252:255], v10 offset:6144\n"
    "v_mfma_f32_16x16x32_bf16 v[60:63], a[228:231], a[0:3], 0\n"
    "v_mfma_f32_16x16x32_bf16 v[60:63], a[236:239], v[200:203], v[60:63]\n"
    "v_mfma_f32_16x16x32_bf16 v[64:67], a[228:231], a[4:7], 0\n"
    "buffer_load_dwordx4 v1, s[8:11], s70 offen offset:2048 lds\n"
    "v_mfma_f32_16x16x32_bf16 v[64:67], a[236:239], v[204:207], v[64:67]\n"
    "s_add_u32 s70, s70, 8192\n"
    "ds_read_b128 v[200:203], v10 offset:40960\n"
    "v_mfma_f32_16x16x32_bf16 v[68:71], a[228:231], a[8:11], 0\n"
    "v_mfma_f32_16x16x32_bf16 v[68:71], a[236:239], v[208:211], v[68:71]\n"
    "buffer_store_dwordx2 v[96:97], v6, s[12:15], s83 offen offset:2048 sc1\n"
    "v_mfma_f32_16x16x32_bf16 v[72:75], a[228:231], a[12:15], 0\n"
    "ds_read_b128 v[204:207], v10 offset:41984\n"
    "v_mfma_f32_16x16x32_bf16 v[72:75], a[236:239], v[212:215], v[72:75]\n"
    "ds_read_b128 v[208:211], v10 offset:43008\n"
    "v_mfma_f32_16x16x32_bf16 v[76:79], a[228:231], a[16:19], 0\n"
    "v_mfma_f32_16x16x32_bf16 v[76:79], a[236:239], v[216:219], v[76:79]\n"
    "buffer_load_dwordx4 v1, s[8:11], s70 offen offset:3072 lds\n"
    "v_mfma_f32_16x16x32_bf16 v[80:83], a[228:231], a[20:23], 0\n"
    "ds_read_b128 v[212:215], v10 offset:44032\n"
    "v_mfma_f32_16x16x32_bf16 v[80:83], a[236:239], v[220:223], v[80:83]\n"
    "ds_read_b128 v[216:219], v10 offset:45056\n"
    "v_mfma_f32_16x16x32_bf16 v[84:87], a[228:231], a[24:27], 0\n"
    "v_mfma_f32_16x16x32_bf16 v[84:87], a[236:239], v[224:227], v[84:87]\n"
    "s_waitcnt vmcnt(21) lgkmcnt(6)\n"
    "ds_read_b128 v[220:223], v10 offset:46080\n"
    "v_mfma_f32_16x16x32_bf16 v[32:35], a[240:243], a[28:31], v[32:35]\n"
    "s_barrier\n"
    "buffer_store_dwordx2 v[98:99], v6, s[12:15], s83 offen offset:2560 sc1\n"
    "v_mfma_f32_16x16x32_bf16 v[32:35], a[248:251], v[228:231], v[32:35]\n"
    "s_add_u32 s70, s52, 479232\n"
    "ds_read_b64_tr_b16 a[224:225], v2 offset:16384\n"
    "v_mfma_f32_16x16x32_bf16 v[36:39], a[240:243], a[32:35], v[36:39]\n"
    "s_add_u32 m0, s60, 20480\n"
    "ds_read_b64_tr_b16 a[226:227], v2 offset:18432\n"
    "v_mfma_f32_16x16x32_bf16 v[36:39], a[248:251], v[232:235], v[36:39]\n"
    "buffer_load_dwordx4 v1, s[8:11], s70 offen offset:0 lds\n"
    "v_mfma_f32_16x16x32_bf16 v[40:43], a[240:243], a[36:39], v[40:43]\n"
    "s_add_u32 s70, s70, 8192\n"
    "ds_read_b64_tr_b16 a[228:229], v2 offset:57344\n"
    "v_mfma_f32_16x16x32_bf16 v[40:43], a[248:251], v[236:239], v[40:43]\n"
    "ds_read_b64_tr_b16 a[230:231], v2 offset:59392\n"
    "v_mfma_f32_16x16x32_bf16 v[44:47], a[240:243], a[40:43], v[44:47]\n"
    "ds_read_b64_tr_b16 a[232:233], v3 offset:16384\n"
    "v_mfma_f32_16x16x32_bf16 v[44:47], a[248:251], v[240:243], v[44:47]\n"
    "ds_read_b64_tr_b16 a[234:235], v3 offset:18432\n"
    "v_mfma_f32_16x16x32_bf16 v[48:51], a[240:243], a[44:47], v[48:51]\n"
    "ds_read_b64_tr_b16 a[236:237], v3 offset:57344\n"
    "v_mfma_f32_16x16x32_bf16 v[48:51], a[248:251], v[244:247], v[48:51]\n"
    "buffer_load_dwordx4 v1, s[8:11], s70 offen offset:1024 lds\n"
    "v_mfma_f32_16x16x32_bf16 v[52:55], a[240:243], a[48:51], v[52:55]\n"
    "s_add_u32 s70, s70, 8192\n"
    "ds_read_b64_tr_b16 a[238:239], v3 offset:59392\n"
    "v_mfma_f32_16x16x32_bf16 v[52:55], a[248:251], v[248:251], v[52:55]\n"
    "buffer_store_dwordx2 v[100:101], v6, s[12:15], s83 offen offset:3072 sc1\n"
    "v_mfma_f32_16x16x32_bf16 v[56:59], a[240:243], a[52:55], v[56:59]\n"
    "s_waitcnt lgkmcnt(14)\n"
    "ds_read_b128 v[224:227], v10 offset:47104\n"
    "v_mfma_f32_16x16x32_bf16 v[56:59], a[248:251], v[252:255], v[56:59]\n"
    "v_mfma_f32_16x16x32_bf16 v[60:63], a[244:247], a[28:31], v[60:63]\n"
    "v_mfma_f32_16x16x32_bf16 v[60:63], a[252:255], v[228:231], v[60:63]\n"
    "v_mfma_f32_16x16x32_bf16 v[64:67], a[244:247], a[32:35], v[64:67]\n"
    "buffer_load_dwordx4 v1, s[8:11], s70 offen offset:2048 lds\n"
    "v_mfma_f32_16x16x32_bf16 v[64:67], a[252:255], v[232:235], v[64:67]\n"
    "s_add_u32 s70, s70, 8192\n"
    "ds_read_b128 v[228:231], v18 offset:8192\n"
    "v_mfma_f32_16x16x32_bf16 v[68:71], a[244:247], a[36:39], v[68:71]\n"
    "s_add_u32 s74, s20, 1\n"
    "v_mfma_f32_16x16x32_bf16 v[68:71], a[252:255], v[236:239], v[68:71]\n"
    "s_and_b32 s74, s74, 7\n"
    "ds_read_b128 v[232:235], v18 offset:9216\n"
    "v_mfma_f32_16x16x32_bf16 v[72:75], a[244:247], a[40:43], v[72:75]\n"
    "s_lshl_b32 s74, s74, 17\n"
    "s_add_u32 s83, s40, s74\n"
    "v_mfma_f32_16x16x32_bf16 v[72:75], a[252:255], v[240:243], v[72:75]\n"
    "buffer_store_dwordx4 v[116:119], v0, s[12:15], s83 offen offset:0 sc1\n"
    "v_mfma_f32_16x16x32_bf16 v[76:79], a[244:247], a[44:47], v[76:79]\n"
    "ds_read_b128 v[236:239], v18 offset:10240\n"
    "v_mfma_f32_16x16x32_bf16 v[76:79], a[252:255], v[244:247], v[76:79]\n"
    "buffer_load_dwordx4 v1, s[8:11], s70 offen offset:3072 lds\n"
    "v_mfma_f32_16x16x32_bf16 v[80:83], a[244:247], a[48:51], v[80:83]\n"
    "s_add_u32 s70, s53, 0\n"
    "ds_read_b128 v[240:243], v18 offset:11264\n"
    "v_mfma_f32_16x16x32_bf16 v[80:83], a[252:255], v[248:251], v[80:83]\n"
    "s_add_u32 m0, s60, 24576\n"
    "ds_read_b128 v[244:247], v18 offset:12288\n"
    "v_mfma_f32_16x16x32_bf16 v[84:87], a[244:247], a[52:55], v[84:87]\n"
    "v_mfma_f32_16x16x32_bf16 v[84:87], a[252:255], v[252:255], v[84:87]\n"
    "s_waitcnt vmcnt(24) lgkmcnt(6)\n"
    "ds_read_b128 v[248:251], v18 offset:13312\n"
    "v_mfma_f32_16x16x32_bf16 v[32:35], a[224:227], a[56:59], v[32:35]\n"
    "s_barrier\n"
    "v_mov_b32 v14, 0\n"
    "v_mov_b32 v15, 0\n"
    "v_mfma_f32_16x16x32_bf16 v[32:35], a[232:235], v[200:203], v[32:35]\n"
    "ds_read_b64_tr_b16 a[240:241], v2 offset:20480\n"
    "v_mfma_f32_16x16x32_bf16 v[36:39], a[224:227], a[60:63], v[36:39]\n"
    "buffer_load_dwordx4 v1, s[8:11], s70 offen offset:0 lds\n"
    "v_mfma_f32_16x16x32_bf16 v[36:39], a[232:235], v[204:207], v[36:39]\n"
    "s_add_u32 s70, s70, 8192\n"
    "ds_read_b64_tr_b16 a[242:243], v2 offset:22528\n"
    "v_mfma_f32_16x16x32_bf16 v[40:43], a[224:227], a[64:67], v[40:43]\n"
    "ds_read_b64_tr_b16 a[244:245], v2 offset:61440\n"
    "v_mfma_f32_16x16x32_bf16 v[40:43], a[232:235], v[208:211], v[40:43]\n"
    "ds_read_b64_tr_b16 a[246:247], v2 offset:63488\n"
    "v_mfma_f32_16x16x32_bf16 v[44:47], a[224:227], a[68:71], v[44:47]\n"
    "ds_read_b64_tr_b16 a[248:249], v3 offset:20480\n"
    "v_mfma_f32_16x16x32_bf16 v[44:47], a[232:235], v[212:215], v[44:47]\n"
    "ds_read_b64_tr_b16 a[250:251], v3 offset:22528\n"
    "v_mfma_f32_16x16x32_bf16 v[48:51], a[224:227], a[72:75], v[48:51]\n"
    "buffer_store_dwordx4 v[120:123], v0, s[12:15], s83 offen offset:1024 sc1\n"
    "v_mfma_f32_16x16x32_bf16 v[48:51], a[232:235], v[216:219], v[48:51]\n"
    "ds_read_b64_tr_b16 a[252:253], v3 offset:61440\n"
    "v_mfma_f32_16x16x32_bf16 v[52:55], a[224:227], a[76:79], v[52:55]\n"
    "buffer_load_dwordx4 v1, s[8:11], s70 offen offset:1024 lds\n"
    "v_mfma_f32_16x16x32_bf16 v[52:55], a[232:235], v[220:223], v[52:55]\n"
    "s_add_u32 s70, s70, 8192\n"
    "ds_read_b64_tr_b16 a[254:255], v3 offset:63488\n"
    "v_mfma_f32_16x16x32_bf16 v[56:59], a[224:227], a[80:83], v[56:59]\n"
    "s_waitcnt lgkmcnt(14)\n"
    "ds_read_b128 v[252:255], v18 offset:14336\n"
    "v_mfma_f32_16x16x32_bf16 v[56:59], a[232:235], v[224:227], v[56:59]\n"
    "v_mfma_f32_16x16x32_bf16 v[60:63], a[228:231], a[56:59], v[60:63]\n"
    "v_mfma_f32_16x16x32_bf16 v[60:63], a[236:239], v[200:203], v[60:63]\n"
    "v_mfma_f32_16x16x32_bf16 v[64:67], a[228:231], a[60:63], v[64:67]\n"
    "buffer_load_dwordx4 v1, s[8:11], s70 offen offset:2048 lds\n"
    "v_mfma_f32_16x16x32_bf16 v[64:67], a[236:239], v[204:207], v[64:67]\n"
    "s_add_u32 s70, s70, 8192\n"
    "ds_read_b128 v[200:203], v4 offset:0\n"
    "v_mfma_f32_16x16x32_bf16 v[68:71], a[228:231], a[64:67], v[68:71]\n"
    "v_mfma_f32_16x16x32_bf16 v[68:71], a[236:239], v[208:211], v[68:71]\n"
    "ds_read_b128 v[204:207], v4 offset:1024\n"
    "v_mfma_f32_16x16x32_bf16 v[72:75], a[228:231], a[68:71], v[72:75]\n"
    "v_mfma_f32_16x16x32_bf16 v[72:75], a[236:239], v[212:215], v[72:75]\n"
    "ds_read_b128 v[208:211], v4 offset:2048\n"
    "v_mfma_f32_16x16x32_bf16 v[76:79], a[228:231], a[72:75], v[76:79]\n"
    "v_mfma_f32_16x16x32_bf16 v[76:79], a[236:239], v[216:219], v[76:79]\n"
    "buffer_load_dwordx4 v1, s[8:11], s70 offen offset:3072 lds\n"
    "v_mfma_f32_16x16x32_bf16 v[80:83], a[228:231], a[76:79], v[80:83]\n"
    "ds_read_b128 v[212:215], v4 offset:3072\n"
    "v_mfma_f32_16x16x32_bf16 v[80:83], a[236:239], v[220:223], v[80:83]\n"
    "ds_read_b128 v[216:219], v4 offset:4096\n"
    "v_mfma_f32_16x16x32_bf16 v[84:87], a[228:231], a[80:83], v[84:87]\n"
    "buffer_store_dwordx4 v[124:127], v0, s[12:15], s83 offen offset:2048 sc1\n"
    "v_mfma_f32_16x16x32_bf16 v[84:87], a[236:239], v[224:227], v[84:87]\n"
    "s_waitcnt vmcnt(26) lgkmcnt(6)\n"
    "ds_read_b128 v[220:223], v4 offset:5120\n"
    "v_mfma_f32_16x16x32_bf16 v[32:35], a[240:243], a[84:87], v[32:35]\n"
    "s_barrier\n"
    "v_mfma_f32_16x16x32_bf16 v[32:35], a[248:251], v[228:231], v[32:35]\n"
    "s_add_u32 s70, s53, 147456\n"
    "s_add_u32 m0, s60, 28672\n"
    "ds_read_b64_tr_b16 a[224:225], v2 offset:0\n"
    "v_mfma_f32_16x16x32_bf16 v[36:39], a[240:243], a[88:91], v[36:39]\n"
    "buffer_load_dwordx4 v1, s[8:11], s70 offen offset:0 lds\n"
    "v_mfma_f32_16x16x32_bf16 v[36:39], a[248:251], v[232:235], v[36:39]\n"
    "s_add_u32 s70, s70, 8192\n"
    "ds_read_b64_tr_b16 a[226:227], v2 offset:2048\n"
    "v_mfma_f32_16x16x32_bf16 v[40:43], a[240:243], a[92:95], v[40:43]\n"
    "ds_read_b64_tr_b16 a[228:229], v2 offset:40960\n"
    "v_mfma_f32_16x16x32_bf16 v[40:43], a[248:251], v[236:239], v[40:43]\n"
    "ds_read_b64_tr_b16 a[230:231], v2 offset:43008\n"
    "v_mfma_f32_16x16x32_bf16 v[44:47], a[240:243], a[96:99], v[44:47]\n"
    "ds_read_b64_tr_b16 a[232:233], v3 offset:0\n"
    "v_mfma_f32_16x16x32_bf16 v[44:47], a[248:251], v[240:243], v[44:47]\n"
    "ds_read_b64_tr_b16 a[234:235], v3 offset:2048\n"
    "v_mfma_f32_16x16x32_bf16 v[48:51], a[240:243], a[100:103], v[48:51]\n"
    "ds_read_b64_tr_b16 a[236:237], v3 offset:40960\n"
    "v_mfma_f32_16x16x32_bf16 v[48:51], a[248:251], v[244:247], v[48:51]\n"
    "buffer_load_dwordx4 v1, s[8:11], s70 offen offset:1024 lds\n"
    "v_mfma_f32_16x16x32_bf16 v[52:55], a[240:243], a[104:107], v[52:55]\n"
    "s_add_u32 s70, s70, 8192\n"
    "ds_read_b64_tr_b16 a[238:239], v3 offset:43008\n"
    "v_mfma_f32_16x16x32_bf16 v[52:55], a[248:251], v[248:251], v[52:55]\n"
    "ds_read_b128 v[224:227], v4 offset:6144\n"
    "v_mfma_f32_16x16x32_bf16 v[56:59], a[240:243], a[108:111], v[56:59]\n"
    "s_waitcnt lgkmcnt(15)\n"
    "v_mfma_f32_16x16x32_bf16 v[56:59], a[248:251], v[252:255], v[56:59]\n"
    "v_mfma_f32_16x16x32_bf16 v[60:63], a[244:247], a[84:87], v[60:63]\n"
    "v_mfma_f32_16x16x32_bf16 v[60:63], a[252:255], v[228:231], v[60:63]\n"
    "v_mfma_f32_16x16x32_bf16 v[64:67], a[244:247], a[88:91], v[64:67]\n"
    "buffer_load_dwordx4 v1, s[8:11], s70 offen offset:2048 lds\n"
    "v_mfma_f32_16x16x32_bf16 v[64:67], a[252:255], v[232:235], v[64:67]\n"
    "s_add_u32 s70, s70, 8192\n"
    "ds_read_b128 v[228:231], v4 offset:40960\n"
    "v_mfma_f32_16x16x32_bf16 v[68:71], a[244:247], a[92:95], v[68:71]\n"
    "buffer_store_dwordx2 v[128:129], v6, s[12:15], s83 offen offset:3072 sc1\n"
    "v_mfma_f32_16x16x32_bf16 v[68:71], a[252:255], v[236:239], v[68:71]\n"
    "ds_read_b128 v[232:235], v4 offset:41984\n"
    "v_mfma_f32_16x16x32_bf16 v[72:75], a[244:247], a[96:99], v[72:75]\n"
    "v_mfma_f32_16x16x32_bf16 v[72:75], a[252:255], v[240:243], v[72:75]\n"
    "ds_read_b128 v[236:239], v4 offset:43008\n"
    "v_mfma_f32_16x16x32_bf16 v[76:79], a[244:247], a[100:103], v[76:79]\n"
    "v_mfma_f32_16x16x32_bf16 v[76:79], a[252:255], v[244:247], v[76:79]\n"
    "buffer_load_dwordx4 v1, s[8:11], s70 offen offset:3072 lds\n"
    "v_mfma_f32_16x16x32_bf16 v[80:83], a[244:247], a[104:107], v[80:83]\n"
    "s_add_u32 s70, s53, 294912\n"
    "ds_read_b128 v[240:243], v4 offset:44032\n"
    "v_mfma_f32_16x16x32_bf16 v[80:83], a[252:255], v[248:251], v[80:83]\n"
    "s_add_u32 m0, s60, 8192\n"
    "ds_read_b128 v[244:247], v4 offset:45056\n"
    "v_mfma_f32_16x16x32_bf16 v[84:87], a[244:247], a[108:111], v[84:87]\n"
    "v_mfma_f32_16x16x32_bf16 v[84:87], a[252:255], v[252:255], v[84:87]\n"
    "s_waitcnt vmcnt(25) lgkmcnt(6)\n"
    "ds_read_b128 v[248:251], v4 offset:46080\n"
    "v_mfma_f32_16x16x32_bf16 v[32:35], a[224:227], a[112:115], v[32:35]\n"
    "s_barrier\n"
    "v_mfma_f32_16x16x32_bf16 v[32:35], a[232:235], v[200:203], v[32:35]\n"
    "ds_read_b64_tr_b16 a[240:241], v2 offset:4096\n"
    "v_mfma_f32_16x16x32_bf16 v[36:39], a[224:227], a[116:119], v[36:39]\n"
    "buffer_load_dwordx4 v1, s[8:11], s70 offen offset:0 lds\n"
    "v_mfma_f32_16x16x32_bf16 v[36:39], a[232:235], v[204:207], v[36:39]\n"
    "s_add_u32 s70, s70, 8192\n"
    "ds_read_b64_tr_b16 a[242:243], v2 offset:6144\n"
    "v_mfma_f32_16x16x32_bf16 v[40:43], a[224:227], a[120:123], v[40:43]\n"
    "ds_read_b64_tr_b16 a[244:245], v2 offset:45056\n"
    "v_mfma_f32_16x16x32_bf16 v[40:43], a[232:235], v[208:211], v[40:43]\n"
    "ds_read_b64_tr_b16 a[246:247], v2 offset:47104\n"
    "v_mfma_f32_16x16x32_bf16 v[44:47], a[224:227], a[124:127], v[44:47]\n"
    "ds_read_b64_tr_b16 a[248:249], v3 offset:4096\n"
    "v_mfma_f32_16x16x32_bf16 v[44:47], a[232:235], v[212:215], v[44:47]\n"
    "ds_read_b64_tr_b16 a[250:251], v3 offset:6144\n"
    "v_mfma_f32_16x16x32_bf16 v[48:51], a[224:227], a[128:131], v[48:51]\n"
    "ds_read_b64_tr_b16 a[252:253], v3 offset:45056\n"
    "v_mfma_f32_16x16x32_bf16 v[48:51], a[232:235], v[216:219], v[48:51]\n"
    "buffer_load_dwordx4 v1, s[8:11], s70 offen offset:1024 lds\n"
    "v_mfma_f32_16x16x32_bf16 v[52:55], a[224:227], a[132:135], v[52:55]\n"
    "s_add_u32 s70, s70, 8192\n"
    "ds_read_b64_tr_b16 a[254:255], v3 offset:47104\n"
    "v_mfma_f32_16x16x32_bf16 v[52:55], a[232:235], v[220:223], v[52:55]\n"
    "ds_read_b128 v[252:255], v4 offset:47104\n"
    "v_mfma_f32_16x16x32_bf16 v[56:59], a[224:227], a[136:139], v[56:59]\n"
    "s_waitcnt lgkmcnt(15)\n"
    "v_mfma_f32_16x16x32_bf16 v[56:59], a[232:235], v[224:227], v[56:59]\n"
    "v_mfma_f32_16x16x32_bf16 v[60:63], a[228:231], a[112:115], v[60:63]\n"
    "v_mfma_f32_16x16x32_bf16 v[60:63], a[236:239], v[200:203], v[60:63]\n"
    "v_mfma_f32_16x16x32_bf16 v[64:67], a[228:231], a[116:119], v[64:67]\n"
    "buffer_load_dwordx4 v1, s[8:11], s70 offen offset:2048 lds\n"
    "v_mfma_f32_16x16x32_bf16 v[64:67], a[236:239], v[204:207], v[64:67]\n"
    "s_add_u32 s70, s70, 8192\n"
    "ds_read_b128 v[200:203], v10 offset:8192\n"
    "v_mfma_f32_16x16x32_bf16 v[68:71], a[228:231], a[120:123], v[68:71]\n"
    "v_mfma_f32_16x16x32_bf16 v[68:71], a[236:239], v[208:211], v[68:71]\n"
    "ds_read_b128 v[204:207], v10 offset:9216\n"
    "v_mfma_f32_16x16x32_bf16 v[72:75], a[228:231], a[124:127], v[72:75]\n"
    "v_mfma_f32_16x16x32_bf16 v[72:75], a[236:239], v[212:215], v[72:75]\n"
    "ds_read_b128 v[208:211], v10 offset:10240\n"
    "v_mfma_f32_16x16x32_bf16 v[76:79], a[228:231], a[128:131], v[76:79]\n"
    "v_mfma_f32_16x16x32_bf16 v[76:79], a[236:239], v[216:219], v[76:79]\n"
    "buffer_load_dwordx4 v1, s[8:11], s70 offen offset:3072 lds\n"
    "v_mfma_f32_16x16x32_bf16 v[80:83], a[228:231], a[132:135], v[80:83]\n"
    "s_add_u32 s70, s53, 442368\n"
    "ds_read_b128 v[212:215], v10 offset:11264\n"
    "v_mfma_f32_16x16x32_bf16 v[80:83], a[236:239], v[220:223], v[80:83]\n"
    "s_add_u32 m0, s60, 12288\n"
    "ds_read_b128 v[216:219], v10 offset:12288\n"
    "v_mfma_f32_16x16x32_bf16 v[84:87], a[228:231], a[136:139], v[84:87]\n"
    "v_mfma_f32_16x16x32_bf16 v[84:87], a[236:239], v[224:227], v[84:87]\n"
    "s_waitcnt vmcnt(22) lgkmcnt(6)\n"
    "ds_read_b128 v[220:223], v10 offset:13312\n"
    "v_mfma_f32_16x16x32_bf16 v[32:35], a[240:243], a[140:143], v[32:35]\n"
    "s_barrier\n"
    "v_mfma_f32_16x16x32_bf16 v[32:35], a[248:251], v[228:231], v[32:35]\n"
    "ds_read_b64_tr_b16 a[224:225], v2 offset:8192\n"
    "v_mfma_f32_16x16x32_bf16 v[36:39], a[240:243], a[144:147], v[36:39]\n"
    "buffer_load_dwordx4 v1, s[8:11], s70 offen offset:0 lds\n"
    "v_mfma_f32_16x16x32_bf16 v[36:39], a[248:251], v[232:235], v[36:39]\n"
    "s_add_u32 s70, s70, 8192\n"
    "ds_read_b64_tr_b16 a[226:227], v2 offset:10240\n"
    "v_mfma_f32_16x16x32_bf16 v[40:43], a[240:243], a[148:151], v[40:43]\n"
    "ds_read_b64_tr_b16 a[228:229], v2 offset:49152\n"
    "v_mfma_f32_16x16x32_bf16 v[40:43], a[248:251], v[236:239], v[40:43]\n"
    "ds_read_b64_tr_b16 a[230:231], v2 offset:51200\n"
    "v_mfma_f32_16x16x32_bf16 v[44:47], a[240:243], a[152:155], v[44:47]\n"
    "ds_read_b64_tr_b16 a[232:233], v3 offset:8192\n"
    "v_mfma_f32_16x16x32_bf16 v[44:47], a[248:251], v[240:243], v[44:47]\n"
    "ds_read_b64_tr_b16 a[234:235], v3 offset:10240\n"
    "v_mfma_f32_16x16x32_bf16 v[48:51], a[240:243], a[156:159], v[48:51]\n"
    "ds_read_b64_tr_b16 a[236:237], v3 offset:49152\n"
    "v_mfma_f32_16x16x32_bf16 v[48:51], a[248:251], v[244:247], v[48:51]\n"
    "buffer_load_dwordx4 v1, s[8:11], s70 offen offset:1024 lds\n"
    "v_mfma_f32_16x16x32_bf16 v[52:55], a[240:243], a[160:163], v[52:55]\n"
    "s_add_u32 s70, s70, 8192\n"
    "ds_read_b64_tr_b16 a[238:239], v3 offset:51200\n"
    "v_mfma_f32_16x16x32_bf16 v[52:55], a[248:251], v[248:251], v[52:55]\n"
    "ds_read_b128 v[224:227], v10 offset:14336\n"
    "v_mfma_f32_16x16x32_bf16 v[56:59], a[240:243], a[164:167], v[56:59]\n"
    "s_waitcnt lgkmcnt(15)\n"
    "v_mfma_f32_16x16x32_bf16 v[56:59], a[248:251], v[252:255], v[56:59]\n"
    "v_mfma_f32_16x16x32_bf16 v[60:63], a[244:247], a[140:143], v[60:63]\n"
    "v_mfma_f32_16x16x32_bf16 v[60:63], a[252:255], v[228:231], v[60:63]\n"
    "v_mfma_f32_16x16x32_bf16 v[64:67], a[244:247], a[144:147], v[64:67]\n"
    "buffer_load_dwordx4 v1, s[8:11], s70 offen offset:2048 lds\n"
    "v_mfma_f32_16x16x32_bf16 v[64:67], a[252:255], v[232:235], v[64:67]\n"
    "s_add_u32 s70, s70, 8192\n"
    "ds_read_b128 v[228:231], v10 offset:49152\n"
    "v_mfma_f32_16x16x32_bf16 v[68:71], a[244:247], a[148:151], v[68:71]\n"
    "v_mfma_f32_16x16x32_bf16 v[68:71], a[252:255], v[236:239], v[68:71]\n"
    "ds_read_b128 v[232:235], v10 offset:50176\n"
    "v_mfma_f32_16x16x32_bf16 v[72:75], a[244:247], a[152:155], v[72:75]\n"
    "v_mfma_f32_16x16x32_bf16 v[72:75], a[252:255], v[240:243], v[72:75]\n"
    "ds_read_b128 v[236:239], v10 offset:51200\n"
    "v_mfma_f32_16x16x32_bf16 v[76:79], a[244:247], a[156:159], v[76:79]\n"
    "v_mfma_f32_16x16x32_bf16 v[76:79], a[252:255], v[244:247], v[76:79]\n"
    "buffer_load_dwordx4 v1, s[8:11], s70 offen offset:3072 lds\n"
    "v_mfma_f32_16x16x32_bf16 v[80:83], a[244:247], a[160:163], v[80:83]\n"
    "s_add_u32 s70, s53, 36864\n"
    "ds_read_b128 v[240:243], v10 offset:52224\n"
    "v_mfma_f32_16x16x32_bf16 v[80:83], a[252:255], v[248:251], v[80:83]\n"
    "s_add_u32 m0, s60, 16384\n"
    "ds_read_b128 v[244:247], v10 offset:53248\n"
    "v_mfma_f32_16x16x32_bf16 v[84:87], a[244:247], a[164:167], v[84:87]\n"
    "v_mfma_f32_16x16x32_bf16 v[84:87], a[252:255], v[252:255], v[84:87]\n"
    "s_waitcnt vmcnt(19) lgkmcnt(6)\n"
    "ds_read_b128 v[248:251], v10 offset:54272\n"
    "v_mfma_f32_16x16x32_bf16 v[32:35], a[224:227], a[168:171], v[32:35]\n"
    "s_barrier\n"
    "v_mfma_f32_16x16x32_bf16 v[32:35], a[232:235], v[200:203], v[32:35]\n"
    "ds_read_b64_tr_b16 a[240:241], v2 offset:12288\n"
    "v_mfma_f32_16x16x32_bf16 v[36:39], a[224:227], a[172:175], v[36:39]\n"
    "buffer_load_dwordx4 v1, s[8:11], s70 offen offset:0 lds\n"
    "v_mfma_f32_16x16x32_bf16 v[36:39], a[232:235], v[204:207], v[36:39]\n"
    "s_add_u32 s70, s70, 8192\n"
    "ds_read_b64_tr_b16 a[242:243], v2 offset:14336\n"
    "v_mfma_f32_16x16x32_bf16 v[40:43], a[224:227], a[176:179], v[40:43]\n"
    "ds_read_b64_tr_b16 a[244:245], v2 offset:53248\n"
    "v_mfma_f32_16x16x32_bf16 v[40:43], a[232:235], v[208:211], v[40:43]\n"
    "ds_read_b64_tr_b16 a[246:247], v2 offset:55296\n"
    "v_mfma_f32_16x16x32_bf16 v[44:47], a[224:227], a[180:183], v[44:47]\n"
    "ds_read_b64_tr_b16 a[248:249], v3 offset:12288\n"
    "v_mfma_f32_16x16x32_bf16 v[44:47], a[232:235], v[212:215], v[44:47]\n"
    "ds_read_b64_tr_b16 a[250:251], v3 offset:14336\n"
    "v_mfma_f32_16x16x32_bf16 v[48:51], a[224:227], a[184:187], v[48:51]\n"
    "ds_read_b64_tr_b16 a[252:253], v3 offset:53248\n"
    "v_mfma_f32_16x16x32_bf16 v[48:51], a[232:235], v[216:219], v[48:51]\n"
    "buffer_load_dwordx4 v1, s[8:11], s70 offen offset:1024 lds\n"
    "v_mfma_f32_16x16x32_bf16 v[52:55], a[224:227], a[188:191], v[52:55]\n"
    "s_add_u32 s70, s70, 8192\n"
    "ds_read_b64_tr_b16 a[254:255], v3 offset:55296\n"
    "v_mfma_f32_16x16x32_bf16 v[52:55], a[232:235], v[220:223], v[52:55]\n"
    "ds_read_b128 v[252:255], v10 offset:55296\n"
    "v_mfma_f32_16x16x32_bf16 v[56:59], a[224:227], a[192:195], v[56:59]\n"
    "s_waitcnt lgkmcnt(15)\n"
    "v_mfma_f32_16x16x32_bf16 v[56:59], a[232:235], v[224:227], v[56:59]\n"
    "v_mfma_f32_16x16x32_bf16 v[60:63], a[228:231], a[168:171], v[60:63]\n"
    "v_mfma_f32_16x16x32_bf16 v[60:63], a[236:239], v[200:203], v[60:63]\n"
    "v_mfma_f32_16x16x32_bf16 v[64:67], a[228:231], a[172:175], v[64:67]\n"
    "buffer_load_dwordx4 v1, s[8:11], s70 offen offset:2048 lds\n"
    "v_mfma_f32_16x16x32_bf16 v[64:67], a[236:239], v[204:207], v[64:67]\n"
    "s_add_u32 s70, s70, 8192\n"
    "ds_read_b128 v[200:203], v4 offset:32768\n"
    "v_mfma_f32_16x16x32_bf16 v[68:71], a[228:231], a[176:179], v[68:71]\n"
    "v_mfma_f32_16x16x32_bf16 v[68:71], a[236:239], v[208:211], v[68:71]\n"
    "ds_read_b128 v[204:207], v4 offset:33792\n"
    "v_mfma_f32_16x16x32_bf16 v[72:75], a[228:231], a[180:183], v[72:75]\n"
    "v_mfma_f32_16x16x32_bf16 v[72:75], a[236:239], v[212:215], v[72:75]\n"
    "ds_read_b128 v[208:211], v4 offset:34816\n"
    "v_mfma_f32_16x16x32_bf16 v[76:79], a[228:231], a[184:187], v[76:79]\n"
    "v_mfma_f32_16x16x32_bf16 v[76:79], a[236:239], v[216:219], v[76:79]\n"
    "buffer_load_dwordx4 v1, s[8:11], s70 offen offset:3072 lds\n"
    "v_mfma_f32_16x16x32_bf16 v[80:83], a[228:231], a[188:191], v[80:83]\n"
    "ds_read_b128 v[212:215], v4 offset:35840\n"
    "v_mfma_f32_16x16x32_bf16 v[80:83], a[236:239], v[220:223], v[80:83]\n"
    "ds_read_b128 v[216:219], v4 offset:36864\n"
    "v_mfma_f32_16x16x32_bf16 v[84:87], a[228:231], a[192:195], v[84:87]\n"
    "v_mfma_f32_16x16x32_bf16 v[84:87], a[236:239], v[224:227], v[84:87]\n"
    "s_waitcnt vmcnt(13) lgkmcnt(6)\n"
    "ds_read_b128 v[220:223], v4 offset:37888\n"
    "v_mfma_f32_16x16x32_bf16 v[32:35], a[240:243], a[196:199], v[32:35]\n"
    "s_barrier\n"
    "v_mfma_f32_16x16x32_bf16 v[32:35], a[248:251], v[228:231], v[32:35]\n"
    "buffer_store_dwordx2 v[30:31], v5, s[12:15], s22 offen offset:0 sc1\n"
    "v_mfma_f32_16x16x32_bf16 v[36:39], a[240:243], a[200:203], v[36:39]\n"
    "s_add_u32 s70, s53, 184320\n"
    "ds_read_b64_tr_b16 a[224:225], v2 offset:16384\n"
    "v_mfma_f32_16x16x32_bf16 v[36:39], a[248:251], v[232:235], v[36:39]\n"
    "s_add_u32 m0, s60, 20480\n"
    "ds_read_b64_tr_b16 a[226:227], v2 offset:18432\n"
    "v_cvt_pk_bf16_f32 v32, v32, v33\n"
    "v_mfma_f32_16x16x32_bf16 v[40:43], a[240:243], a[204:207], v[40:43]\n"
    "buffer_load_dwordx4 v1, s[8:11], s70 offen offset:0 lds\n"
    "v_mfma_f32_16x16x32_bf16 v[40:43], a[248:251], v[236:239], v[40:43]\n"
    "s_add_u32 s70, s70, 8192\n"
    "ds_read_b64_tr_b16 a[228:229], v2 offset:57344\n"
    "v_cvt_pk_bf16_f32 v33, v34, v35\n"
    "v_mfma_f32_16x16x32_bf16 v[44:47], a[240:243], a[208:211], v[44:47]\n"
    "ds_read_b64_tr_b16 a[230:231], v2 offset:59392\n"
    "v_cvt_pk_bf16_f32 v34, v36, v37\n"
    "v_mfma_f32_16x16x32_bf16 v[44:47], a[248:251], v[240:243], v[44:47]\n"
    "ds_read_b64_tr_b16 a[232:233], v3 offset:16384\n"
    "v_cvt_pk_bf16_f32 v35, v38, v39\n"
    "v_mfma_f32_16x16x32_bf16 v[48:51], a[240:243], a[212:215], v[48:51]\n"
    "ds_read_b64_tr_b16 a[234:235], v3 offset:18432\n"
    "v_cvt_pk_bf16_f32 v36, v40, v41\n"
    "v_mfma_f32_16x16x32_bf16 v[48:51], a[248:251], v[244:247], v[48:51]\n"
    "buffer_load_dwordx4 v1, s[8:11], s70 offen offset:1024 lds\n"
    "v_mfma_f32_16x16x32_bf16 v[52:55], a[240:243], a[216:219], v[52:55]\n"
    "s_add_u32 s70, s70, 8192\n"
    "ds_read_b64_tr_b16 a[236:237], v3 offset:57344\n"
    "v_cvt_pk_bf16_f32 v37, v42, v43\n"
    "v_mfma_f32_16x16x32_bf16 v[52:55], a[248:251], v[248:251], v[52:55]\n"
    "s_add_u32 s74, s20, 2\n"
    "ds_read_b64_tr_b16 a[238:239], v3 offset:59392\n"
    "v_cvt_pk_bf16_f32 v38, v44, v45\n"
    "v_mfma_f32_16x16x32_bf16 v[56:59], a[240:243], a[220:223], v[56:59]\n"
    "s_waitcnt lgkmcnt(14)\n"
    "s_and_b32 s74, s74, 7\n"
    "ds_read_b128 v[224:227], v4 offset:38912\n"
    "v_cvt_pk_bf16_f32 v39, v46, v47\n"
    "v_mfma_f32_16x16x32_bf16 v[56:59], a[248:251], v[252:255], v[56:59]\n"
    "s_lshl_b32 s74, s74, 17\n"
    "s_add_u32 s83, s40, s74\n"
    "v_cvt_pk_bf16_f32 v40, v48, v49\n"
    "v_mfma_f32_16x16x32_bf16 v[60:63], a[244:247], a[196:199], v[60:63]\n"
    "buffer_store_dwordx4 v[32:35], v0, s[12:15], s83 offen offset:0 sc1\n"
    "v_mfma_f32_16x16x32_bf16 v[60:63], a[252:255], v[228:231], v[60:63]\n"
    "v_cvt_pk_bf16_f32 v41, v50, v51\n"
    "v_cvt_pk_bf16_f32 v42, v52, v53\n"
    "v_mfma_f32_16x16x32_bf16 v[64:67], a[244:247], a[200:203], v[64:67]\n"
    "buffer_load_dwordx4 v1, s[8:11], s70 offen offset:2048 lds\n"
    "v_mfma_f32_16x16x32_bf16 v[64:67], a[252:255], v[232:235], v[64:67]\n"
    "s_add_u32 s70, s70, 8192\n"
    "ds_read_b128 v[228:231], v10 offset:0\n"
    "v_cvt_pk_bf16_f32 v43, v54, v55\n"
    "v_mfma_f32_16x16x32_bf16 v[68:71], a[244:247], a[204:207], v[68:71]\n"
    "v_cvt_pk_bf16_f32 v44, v56, v57\n"
    "v_cvt_pk_bf16_f32 v45, v58, v59\n"
    "v_mfma_f32_16x16x32_bf16 v[68:71], a[252:255], v[236:239], v[68:71]\n"
    "ds_read_b128 v[232:235], v10 offset:1024\n"
    "v_cvt_pk_bf16_f32 v60, v60, v61\n"
    "v_mfma_f32_16x16x32_bf16 v[72:75], a[244:247], a[208:211], v[72:75]\n"
    "v_cvt_pk_bf16_f32 v61, v62, v63\n"
    "v_cvt_pk_bf16_f32 v62, v64, v65\n"
    "v_mfma_f32_16x16x32_bf16 v[72:75], a[252:255], v[240:243], v[72:75]\n"
    "ds_read_b128 v[236:239], v10 offset:2048\n"
    "v_cvt_pk_bf16_f32 v63, v66, v67\n"
    "v_mfma_f32_16x16x32_bf16 v[76:79], a[244:247], a[212:215], v[76:79]\n"
    "v_cvt_pk_bf16_f32 v64, v68, v69\n"
    "v_cvt_pk_bf16_f32 v65, v70, v71\n"
    "v_mfma_f32_16x16x32_bf16 v[76:79], a[252:255], v[244:247], v[76:79]\n"
    "buffer_load_dwordx4 v1, s[8:11], s70 offen offset:3072 lds\n"
    "v_mfma_f32_16x16x32_bf16 v[80:83], a[244:247], a[216:219], v[80:83]\n"
    "ds_read_b128 v[240:243], v10 offset:3072\n"
    "v_cvt_pk_bf16_f32 v66, v72, v73\n"
    "v_mfma_f32_16x16x32_bf16 v[80:83], a[252:255], v[248:251], v[80:83]\n"
    "ds_read_b128 v[244:247], v10 offset:4096\n"
    "v_cvt_pk_bf16_f32 v67, v74, v75\n"
    "v_mfma_f32_16x16x32_bf16 v[84:87], a[244:247], a[220:223], v[84:87]\n"
    "v_cvt_pk_bf16_f32 v68, v76, v77\n"
    "v_cvt_pk_bf16_f32 v69, v78, v79\n"
    "v_mfma_f32_16x16x32_bf16 v[84:87], a[252:255], v[252:255], v[84:87]\n"
    "s_waitcnt vmcnt(18) lgkmcnt(6)\n"
    "ds_read_b128 v[248:251], v10 offset:5120\n"
    "v_cvt_pk_bf16_f32 v70, v80, v81\n"
    "v_mfma_f32_16x16x32_bf16 v[88:91], a[224:227], a[0:3], 0\n"
    "s_barrier\n"
    "v_cvt_pk_bf16_f32 v71, v82, v83\n"
    "v_mfma_f32_16x16x32_bf16 v[88:91], a[232:235], v[200:203], v[88:91]\n"
    "buffer_store_dwordx4 v[36:39], v0, s[12:15], s83 offen offset:1024 sc1\n"
    "v_mfma_f32_16x16x32_bf16 v[92:95], a[224:227], a[4:7], 0\n"
    "s_add_u32 s70, s53, 331776\n"
    "ds_read_b64_tr_b16 a[240:241], v2 offset:20480\n"
    "v_cvt_pk_bf16_f32 v72, v84, v85\n"
    "v_mfma_f32_16x16x32_bf16 v[92:95], a[232:235], v[204:207], v[92:95]\n"
    "s_add_u32 m0, s60, 24576\n"
    "ds_read_b64_tr_b16 a[242:243], v2 offset:22528\n"
    "v_cvt_pk_bf16_f32 v73, v86, v87\n"
    "v_mfma_f32_16x16x32_bf16 v[96:99], a[224:227], a[8:11], 0\n"
    "buffer_load_dwordx4 v1, s[8:11], s70 offen offset:0 lds\n"
    "v_mfma_f32_16x16x32_bf16 v[96:99], a[232:235], v[208:211], v[96:99]\n"
    "s_add_u32 s70, s70, 8192\n"
    "ds_read_b64_tr_b16 a[244:245], v2 offset:61440\n"
    "v_mfma_f32_16x16x32_bf16 v[100:103], a[224:227], a[12:15], 0\n"
    "ds_read_b64_tr_b16 a[246:247], v2 offset:63488\n"
    "v_mfma_f32_16x16x32_bf16 v[100:103], a[232:235], v[212:215], v[100:103]\n"
    "ds_read_b64_tr_b16 a[248:249], v3 offset:20480\n"
    "v_mfma_f32_16x16x32_bf16 v[104:107], a[224:227], a[16:19], 0\n"
    "ds_read_b64_tr_b16 a[250:251], v3 offset:22528\n"
    "v_mfma_f32_16x16x32_bf16 v[104:107], a[232:235], v[216:219], v[104:107]\n"
    "buffer_load_dwordx4 v1, s[8:11], s70 offen offset:1024 lds\n"
    "v_mfma_f32_16x16x32_bf16 v[108:111], a[224:227], a[20:23], 0\n"
    "s_add_u32 s70, s70, 8192\n"
    "ds_read_b64_tr_b16 a[252:253], v3 offset:61440\n"
    "v_mfma_f32_16x16x32_bf16 v[108:111], a[232:235], v[220:223], v[108:111]\n"
    "ds_read_b64_tr_b16 a[254:255], v3 offset:63488\n"
    "v_mfma_f32_16x16x32_bf16 v[112:115], a[224:227], a[24:27], 0\n"
    "s_waitcnt lgkmcnt(14)\n"
    "ds_read_b128 v[252:255], v10 offset:6144\n"
    "v_mfma_f32_16x16x32_bf16 v[112:115], a[232:235], v[224:227], v[112:115]\n"
    "v_mfma_f32_16x16x32_bf16 v[116:119], a[228:231], a[0:3], 0\n"
    "v_mfma_f32_16x16x32_bf16 v[116:119], a[236:239], v[200:203], v[116:119]\n"
    "v_mfma_f32_16x16x32_bf16 v[120:123], a[228:231], a[4:7], 0\n"
    "buffer_load_dwordx4 v1, s[8:11], s70 offen offset:2048 lds\n"
    "v_mfma_f32_16x16x32_bf16 v[120:123], a[236:239], v[204:207], v[120:123]\n"
    "s_add_u32 s70, s70, 8192\n"
    "ds_read_b128 v[200:203], v10 offset:40960\n"
    "v_mfma_f32_16x16x32_bf16 v[124:127], a[228:231], a[8:11], 0\n"
    "v_mfma_f32_16x16x32_bf16 v[124:127], a[236:239], v[208:211], v[124:127]\n"
    "buffer_store_dwordx4 v[40:43], v0, s[12:15], s83 offen offset:2048 sc1\n"
    "v_mfma_f32_16x16x32_bf16 v[128:131], a[228:231], a[12:15], 0\n"
    "ds_read_b128 v[204:207], v10 offset:41984\n"
    "v_mfma_f32_16x16x32_bf16 v[128:131], a[236:239], v[212:215], v[128:131]\n"
    "ds_read_b128 v[208:211], v10 offset:43008\n"
    "v_mfma_f32_16x16x32_bf16 v[132:135], a[228:231], a[16:19], 0\n"
    "v_mfma_f32_16x16x32_bf16 v[132:135], a[236:239], v[216:219], v[132:135]\n"
    "buffer_load_dwordx4 v1, s[8:11], s70 offen offset:3072 lds\n"
    "v_mfma_f32_16x16x32_bf16 v[136:139], a[228:231], a[20:23], 0\n"
    "s_add_u32 s70, s53, 479232\n"
    "ds_read_b128 v[212:215], v10 offset:44032\n"
    "v_mfma_f32_16x16x32_bf16 v[136:139], a[236:239], v[220:223], v[136:139]\n"
    "s_add_u32 m0, s60, 28672\n"
    "ds_read_b128 v[216:219], v10 offset:45056\n"
    "v_mfma_f32_16x16x32_bf16 v[140:143], a[228:231], a[24:27], 0\n"
    "v_mfma_f32_16x16x32_bf16 v[140:143], a[236:239], v[224:227], v[140:143]\n"
    "s_waitcnt vmcnt(20) lgkmcnt(6)\n"
    "ds_read_b128 v[220:223], v10 offset:46080\n"
    "v_mfma_f32_16x16x32_bf16 v[88:91], a[240:243], a[28:31], v[88:91]\n"
    "s_barrier\n"
    "v_mfma_f32_16x16x32_bf16 v[88:91], a[248:251], v[228:231], v[88:91]\n"
    "ds_read_b64_tr_b16 a[224:225], v2 offset:0\n"
    "v_mfma_f32_16x16x32_bf16 v[92:95], a[240:243], a[32:35], v[92:95]\n"
    "buffer_load_dwordx4 v1, s[8:11], s70 offen offset:0 lds\n"
    "v_mfma_f32_16x16x32_bf16 v[92:95], a[248:251], v[232:235], v[92:95]\n"
    "s_add_u32 s70, s70, 8192\n"
    "ds_read_b64_tr_b16 a[226:227], v2 offset:2048\n"
    "v_mfma_f32_16x16x32_bf16 v[96:99], a[240:243], a[36:39], v[96:99]\n"
    "ds_read_b64_tr_b16 a[228:229], v2 offset:40960\n"
    "v_mfma_f32_16x16x32_bf16 v[96:99], a[248:251], v[236:239], v[96:99]\n"
    "ds_read_b64_tr_b16 a[230:231], v2 offset:43008\n"
    "v_mfma_f32_16x16x32_bf16 v[100:103], a[240:243], a[40:43], v[100:103]\n"
    "ds_read_b64_tr_b16 a[232:233], v3 offset:0\n"
    "v_mfma_f32_16x16x32_bf16 v[100:103], a[248:251], v[240:243], v[100:103]\n"
    "ds_read_b64_tr_b16 a[234:235], v3 offset:2048\n"
    "v_mfma_f32_16x16x32_bf16 v[104:107], a[240:243], a[44:47], v[104:107]\n"
    "ds_read_b64_tr_b16 a[236:237], v3 offset:40960\n"
    "v_mfma_f32_16x16x32_bf16 v[104:107], a[248:251], v[244:247], v[104:107]\n"
    "buffer_load_dwordx4 v1, s[8:11], s70 offen offset:1024 lds\n"
    "v_mfma_f32_16x16x32_bf16 v[108:111], a[240:243], a[48:51], v[108:111]\n"
    "s_add_u32 s70, s70, 8192\n"
    "ds_read_b64_tr_b16 a[238:239], v3 offset:43008\n"
    "v_mfma_f32_16x16x32_bf16 v[108:111], a[248:251], v[248:251], v[108:111]\n"
    "buffer_store_dwordx2 v[44:45], v6, s[12:15], s83 offen offset:3072 sc1\n"
    "v_mfma_f32_16x16x32_bf16 v[112:115], a[240:243], a[52:55], v[112:115]\n"
    "s_waitcnt lgkmcnt(14)\n"
    "ds_read_b128 v[224:227], v10 offset:47104\n"
    "v_mfma_f32_16x16x32_bf16 v[112:115], a[248:251], v[252:255], v[112:115]\n"
    "v_mfma_f32_16x16x32_bf16 v[116:119], a[244:247], a[28:31], v[116:119]\n"
    "buffer_load_dwordx2 v[198:199], v7, s[12:15], 0 offen offset:0 sc1\n"
    "v_mfma_f32_16x16x32_bf16 v[116:119], a[252:255], v[228:231], v[116:119]\n"
    "v_mfma_f32_16x16x32_bf16 v[120:123], a[244:247], a[32:35], v[120:123]\n"
    "buffer_load_dwordx4 v1, s[8:11], s70 offen offset:2048 lds\n"
    "v_mfma_f32_16x16x32_bf16 v[120:123], a[252:255], v[232:235], v[120:123]\n"
    "s_add_u32 s70, s70, 8192\n"
    "ds_read_b128 v[228:231], v18 offset:8192\n"
    "v_mfma_f32_16x16x32_bf16 v[124:127], a[244:247], a[36:39], v[124:127]\n"
    "s_add_u32 s74, s20, 3\n"
    "v_mfma_f32_16x16x32_bf16 v[124:127], a[252:255], v[236:239], v[124:127]\n"
    "s_and_b32 s74, s74, 7\n"
    "ds_read_b128 v[232:235], v18 offset:9216\n"
    "v_mfma_f32_16x16x32_bf16 v[128:131], a[244:247], a[40:43], v[128:131]\n"
    "s_lshl_b32 s74, s74, 17\n"
    "s_add_u32 s83, s40, s74\n"
    "v_mfma_f32_16x16x32_bf16 v[128:131], a[252:255], v[240:243], v[128:131]\n"
    "buffer_store_dwordx4 v[60:63], v0, s[12:15], s83 offen offset:0 sc1\n"
    "v_mfma_f32_16x16x32_bf16 v[132:135], a[244:247], a[44:47], v[132:135]\n"
    "ds_read_b128 v[236:239], v18 offset:10240\n"
    "v_mfma_f32_16x16x32_bf16 v[132:135], a[252:255], v[244:247], v[132:135]\n"
    "buffer_load_dwordx4 v1, s[8:11], s70 offen offset:3072 lds\n"
    "v_mfma_f32_16x16x32_bf16 v[136:139], a[244:247], a[48:51], v[136:139]\n"
    "s_add_u32 s70, s54, 0\n"
    "ds_read_b128 v[240:243], v18 offset:11264\n"
    "v_mfma_f32_16x16x32_bf16 v[136:139], a[252:255], v[248:251], v[136:139]\n"
    "s_add_u32 m0, s60, 8192\n"
    "ds_read_b128 v[244:247], v18 offset:12288\n"
    "v_mfma_f32_16x16x32_bf16 v[140:143], a[244:247], a[52:55], v[140:143]\n"
    "v_mfma_f32_16x16x32_bf16 v[140:143], a[252:255], v[252:255], v[140:143]\n"
    "s_waitcnt vmcnt(23) lgkmcnt(6)\n"
    "ds_read_b128 v[248:251], v18 offset:13312\n"
    "v_mfma_f32_16x16x32_bf16 v[88:91], a[224:227], a[56:59], v[88:91]\n"
    "s_barrier\n"
    "v_mfma_f32_16x16x32_bf16 v[88:91], a[232:235], v[200:203], v[88:91]\n"
    "ds_read_b64_tr_b16 a[240:241], v2 offset:4096\n"
    "v_mfma_f32_16x16x32_bf16 v[92:95], a[224:227], a[60:63], v[92:95]\n"
    "buffer_load_dwordx4 v1, s[8:11], s70 offen offset:0 lds\n"
    "v_mfma_f32_16x16x32_bf16 v[92:95], a[232:235], v[204:207], v[92:95]\n"
    "s_add_u32 s70, s70, 8192\n"
    "ds_read_b64_tr_b16 a[242:243], v2 offset:6144\n"
    "v_mfma_f32_16x16x32_bf16 v[96:99], a[224:227], a[64:67], v[96:99]\n"
    "ds_read_b64_tr_b16 a[244:245], v2 offset:45056\n"
    "v_mfma_f32_16x16x32_bf16 v[96:99], a[232:235], v[208:211], v[96:99]\n"
    "ds_read_b64_tr_b16 a[246:247], v2 offset:47104\n"
    "v_mfma_f32_16x16x32_bf16 v[100:103], a[224:227], a[68:71], v[100:103]\n"
    "ds_read_b64_tr_b16 a[248:249], v3 offset:4096\n"
    "v_mfma_f32_16x16x32_bf16 v[100:103], a[232:235], v[212:215], v[100:103]\n"
    "ds_read_b64_tr_b16 a[250:251], v3 offset:6144\n"
    "v_mfma_f32_16x16x32_bf16 v[104:107], a[224:227], a[72:75], v[104:107]\n"
    "buffer_store_dwordx4 v[64:67], v0, s[12:15], s83 offen offset:1024 sc1\n"
    "v_mfma_f32_16x16x32_bf16 v[104:107], a[232:235], v[216:219], v[104:107]\n"
    "ds_read_b64_tr_b16 a[252:253], v3 offset:45056\n"
    "v_mfma_f32_16x16x32_bf16 v[108:111], a[224:227], a[76:79], v[108:111]\n"
    "buffer_load_dwordx4 v1, s[8:11], s70 offen offset:1024 lds\n"
    "v_mfma_f32_16x16x32_bf16 v[108:111], a[232:235], v[220:223], v[108:111]\n"
    "s_add_u32 s70, s70, 8192\n"
    "ds_read_b64_tr_b16 a[254:255], v3 offset:47104\n"
    "v_mfma_f32_16x16x32_bf16 v[112:115], a[224:227], a[80:83], v[112:115]\n"
    "s_waitcnt lgkmcnt(14)\n"
    "ds_read_b128 v[252:255], v18 offset:14336\n"
    "v_mfma_f32_16x16x32_bf16 v[112:115], a[232:235], v[224:227], v[112:115]\n"
    "v_mfma_f32_16x16x32_bf16 v[116:119], a[228:231], a[56:59], v[116:119]\n"
    "v_mfma_f32_16x16x32_bf16 v[116:119], a[236:239], v[200:203], v[116:119]\n"
    "v_mfma_f32_16x16x32_bf16 v[120:123], a[228:231], a[60:63], v[120:123]\n"
    "buffer_load_dwordx4 v1, s[8:11], s70 offen offset:2048 lds\n"
    "v_mfma_f32_16x16x32_bf16 v[120:123], a[236:239], v[204:207], v[120:123]\n"
    "s_add_u32 s70, s70, 8192\n"
    "ds_read_b128 v[200:203], v4 offset:0\n"
    "v_mfma_f32_16x16x32_bf16 v[124:127], a[228:231], a[64:67], v[124:127]\n"
    "v_mfma_f32_16x16x32_bf16 v[124:127], a[236:239], v[208:211], v[124:127]\n"
    "ds_read_b128 v[204:207], v4 offset:1024\n"
    "v_mfma_f32_16x16x32_bf16 v[128:131], a[228:231], a[68:71], v[128:131]\n"
    "v_mfma_f32_16x16x32_bf16 v[128:131], a[236:239], v[212:215], v[128:131]\n"
    "ds_read_b128 v[208:211], v4 offset:2048\n"
    "v_mfma_f32_16x16x32_bf16 v[132:135], a[228:231], a[72:75], v[132:135]\n"
    "v_mfma_f32_16x16x32_bf16 v[132:135], a[236:239], v[216:219], v[132:135]\n"
    "buffer_load_dwordx4 v1, s[8:11], s70 offen offset:3072 lds\n"
    "v_mfma_f32_16x16x32_bf16 v[136:139], a[228:231], a[76:79], v[136:139]\n"
    "ds_read_b128 v[212:215], v4 offset:3072\n"
    "v_mfma_f32_16x16x32_bf16 v[136:139], a[236:239], v[220:223], v[136:139]\n"
    "ds_read_b128 v[216:219], v4 offset:4096\n"
    "v_mfma_f32_16x16x32_bf16 v[140:143], a[228:231], a[80:83], v[140:143]\n"
    "buffer_store_dwordx4 v[68:71], v0, s[12:15], s83 offen offset:2048 sc1\n"
    "v_mfma_f32_16x16x32_bf16 v[140:143], a[236:239], v[224:227], v[140:143]\n"
    "s_waitcnt vmcnt(25) lgkmcnt(6)\n"
    "ds_read_b128 v[220:223], v4 offset:5120\n"
    "v_mfma_f32_16x16x32_bf16 v[88:91], a[240:243], a[84:87], v[88:91]\n"
    "s_barrier\n"
    "v_mfma_f32_16x16x32_bf16 v[88:91], a[248:251], v[228:231], v[88:91]\n"
    "s_waitcnt vmcnt(9)\n"
    "s_add_u32 s70, s54, 147456\n"
    "s_add_u32 m0, s60, 12288\n"
    "v_cmp_ne_u64 vcc, s[68:69], v[198:199]\n"
    "v_mfma_f32_16x16x32_bf16 v[92:95], a[240:243], a[88:91], v[92:95]\n"
    "s_cbranch_vccnz LSP0_%=\n"
    "LRT0_%=:\n"
    "buffer_load_dwordx4 v1, s[8:11], s70 offen offset:0 lds\n"
    "v_mfma_f32_16x16x32_bf16 v[92:95], a[248:251], v[232:235], v[92:95]\n"
    "s_add_u32 s70, s70, 8192\n"
    "ds_read_b64_tr_b16 a[224:225], v2 offset:8192\n"
    "ds_read_b64_tr_b16 a[226:227], v2 offset:10240\n"
    "v_mfma_f32_16x16x32_bf16 v[96:99], a[240:243], a[92:95], v[96:99]\n"
    "s_add_u32 s71, s42, 0\n"
    "ds_read_b128 v[224:227], v4 offset:6144\n"
    "v_mfma_f32_16x16x32_bf16 v[96:99], a[248:251], v[236:239], v[96:99]\n"
    "buffer_load_dwordx2 v[176:177], v6, s[12:15], s71 offen offset:0 sc1\n"
    "v_mfma_f32_16x16x32_bf16 v[100:103], a[240:243], a[96:99], v[100:103]\n"
    "ds_read_b64_tr_b16 a[228:229], v2 offset:49152\n"
    "ds_read_b64_tr_b16 a[230:231], v2 offset:51200\n"
    "v_mfma_f32_16x16x32_bf16 v[100:103], a[248:251], v[240:243], v[100:103]\n"
    "v_mfma_f32_16x16x32_bf16 v[104:107], a[240:243], a[100:103], v[104:107]\n"
    "ds_read_b64_tr_b16 a[232:233], v3 offset:8192\n"
    "ds_read_b64_tr_b16 a[234:235], v3 offset:10240\n"
    "v_mfma_f32_16x16x32_bf16 v[104:107], a[248:251], v[244:247], v[104:107]\n"
    "buffer_load_dwordx4 v1, s[8:11], s70 offen offset:1024 lds\n"
    "v_mfma_f32_16x16x32_bf16 v[108:111], a[240:243], a[104:107], v[108:111]\n"
    "s_add_u32 s70, s70, 8192\n"
    "ds_read_b64_tr_b16 a[236:237], v3 offset:49152\n"
    "ds_read_b64_tr_b16 a[238:239], v3 offset:51200\n"
    "v_mfma_f32_16x16x32_bf16 v[108:111], a[248:251], v[248:251], v[108:111]\n"
    "s_add_u32 s71, s42, 16384\n"
    "v_mfma_f32_16x16x32_bf16 v[112:115], a[240:243], a[108:111], v[112:115]\n"
    "s_waitcnt lgkmcnt(15)\n"
    "buffer_load_dwordx2 v[178:179], v6, s[12:15], s71 offen offset:0 sc1\n"
    "v_mfma_f32_16x16x32_bf16 v[112:115], a[248:251], v[252:255], v[112:115]\n"
    "v_mfma_f32_16x16x32_bf16 v[116:119], a[244:247], a[84:87], v[116:119]\n"
    "v_mfma_f32_16x16x32_bf16 v[116:119], a[252:255], v[228:231], v[116:119]\n"
    "v_mfma_f32_16x16x32_bf16 v[120:123], a[244:247], a[88:91], v[120:123]\n"
    "buffer_load_dwordx4 v1, s[8:11], s70 offen offset:2048 lds\n"
    "v_mfma_f32_16x16x32_bf16 v[120:123], a[252:255], v[232:235], v[120:123]\n"
    "s_add_u32 s70, s70, 8192\n"
    "ds_read_b128 v[228:231], v4 offset:40960\n"
    "v_mfma_f32_16x16x32_bf16 v[124:127], a[244:247], a[92:95], v[124:127]\n"
    "buffer_store_dwordx2 v[72:73], v6, s[12:15], s83 offen offset:3072 sc1\n"
    "v_mfma_f32_16x16x32_bf16 v[124:127], a[252:255], v[236:239], v[124:127]\n"
    "s_add_u32 s71, s42, 32768\n"
    "ds_read_b128 v[232:235], v4 offset:41984\n"
    "v_mfma_f32_16x16x32_bf16 v[128:131], a[244:247], a[96:99], v[128:131]\n"
    "v_mfma_f32_16x16x32_bf16 v[128:131], a[252:255], v[240:243], v[128:131]\n"
    "buffer_load_dwordx2 v[180:181], v6, s[12:15], s71 offen offset:0 sc1\n"
    "v_mfma_f32_16x16x32_bf16 v[132:135], a[244:247], a[100:103], v[132:135]\n"
    "ds_read_b128 v[236:239], v4 offset:43008\n"
    "v_mfma_f32_16x16x32_bf16 v[132:135], a[252:255], v[244:247], v[132:135]\n"
    "buffer_load_dwordx4 v1, s[8:11], s70 offen offset:3072 lds\n"
    "v_mfma_f32_16x16x32_bf16 v[136:139], a[244:247], a[104:107], v[136:139]\n"
    "s_add_u32 s71, s42, 49152\n"
    "ds_read_b128 v[240:243], v4 offset:44032\n"
    "v_mfma_f32_16x16x32_bf16 v[136:139], a[252:255], v[248:251], v[136:139]\n"
    "ds_read_b128 v[244:247], v4 offset:45056\n"
    "v_mfma_f32_16x16x32_bf16 v[140:143], a[244:247], a[108:111], v[140:143]\n"
    "v_mfma_f32_16x16x32_bf16 v[140:143], a[252:255], v[252:255], v[140:143]\n"
    "s_waitcnt vmcnt(27) lgkmcnt(5)\n"
    "ds_read_b128 v[248:251], v4 offset:46080\n"
    "v_mfma_f32_16x16x32_bf16 v[88:91], a[224:227], a[112:115], v[88:91]\n"
    "s_barrier\n"
    "v_mfma_f32_16x16x32_bf16 v[88:91], a[232:235], v[200:203], v[88:91]\n"
    "buffer_load_dwordx2 v[182:183], v6, s[12:15], s71 offen offset:0 sc1\n"
    "v_mfma_f32_16x16x32_bf16 v[92:95], a[224:227], a[116:119], v[92:95]\n"
    "s_add_u32 s70, s54, 294912\n"
    "ds_read_b64_tr_b16 a[240:241], v2 offset:12288\n"
    "v_mfma_f32_16x16x32_bf16 v[92:95], a[232:235], v[204:207], v[92:95]\n"
    "s_add_u32 m0, s60, 16384\n"
    "ds_read_b64_tr_b16 a[242:243], v2 offset:14336\n"
    "v_mfma_f32_16x16x32_bf16 v[96:99], a[224:227], a[120:123], v[96:99]\n"
    "buffer_load_dwordx4 v1, s[8:11], s70 offen offset:0 lds\n"
    "v_mfma_f32_16x16x32_bf16 v[96:99], a[232:235], v[208:211], v[96:99]\n"
    "s_add_u32 s70, s70, 8192\n"
    "ds_read_b64_tr_b16 a[244:245], v2 offset:53248\n"
    "v_mfma_f32_16x16x32_bf16 v[100:103], a[224:227], a[124:127], v[100:103]\n"
    "ds_read_b64_tr_b16 a[246:247], v2 offset:55296\n"
    "v_mfma_f32_16x16x32_bf16 v[100:103], a[232:235], v[212:215], v[100:103]\n"
    "ds_read_b64_tr_b16 a[248:249], v3 offset:12288\n"
    "v_mfma_f32_16x16x32_bf16 v[104:107], a[224:227], a[128:131], v[104:107]\n"
    "ds_read_b64_tr_b16 a[250:251], v3 offset:14336\n"
    "v_mfma_f32_16x16x32_bf16 v[104:107], a[232:235], v[216:219], v[104:107]\n"
    "buffer_load_dwordx4 v1, s[8:11], s70 offen offset:1024 lds\n"
    "v_mfma_f32_16x16x32_bf16 v[108:111], a[224:227], a[132:135], v[108:111]\n"
    "s_add_u32 s70, s70, 8192\n"
    "ds_read_b64_tr_b16 a[252:253], v3 offset:53248\n"
    "v_mfma_f32_16x16x32_bf16 v[108:111], a[232:235], v[220:223], v[108:111]\n"
    "s_add_u32 s71, s42, 65536\n"
    "ds_read_b64_tr_b16 a[254:255], v3 offset:55296\n"
    "v_mfma_f32_16x16x32_bf16 v[112:115], a[224:227], a[136:139], v[112:115]\n"
    "buffer_load_dwordx2 v[184:185], v6, s[12:15], s71 offen offset:0 sc1\n"
    "v_mfma_f32_16x16x32_bf16 v[112:115], a[232:235], v[224:227], v[112:115]\n"
    "ds_read_b128 v[252:255], v4 offset:47104\n"
    "v_mfma_f32_16x16x32_bf16 v[116:119], a[228:231], a[112:115], v[116:119]\n"
    "v_mfma_f32_16x16x32_bf16 v[116:119], a[236:239], v[200:203], v[116:119]\n"
    "v_mfma_f32_16x16x32_bf16 v[120:123], a[228:231], a[116:119], v[120:123]\n"
    "buffer_load_dwordx4 v1, s[8:11], s70 offen offset:2048 lds\n"
    "v_mfma_f32_16x16x32_bf16 v[120:123], a[236:239], v[204:207], v[120:123]\n"
    "s_add_u32 s70, s70, 8192\n"
    "ds_read_b128 v[200:203], v10 offset:8192\n"
    "v_mfma_f32_16x16x32_bf16 v[124:127], a[228:231], a[120:123], v[124:127]\n"
    "s_add_u32 s71, s42, 81920\n"
    "v_mfma_f32_16x16x32_bf16 v[124:127], a[236:239], v[208:211], v[124:127]\n"
    "buffer_load_dwordx2 v[186:187], v6, s[12:15], s71 offen offset:0 sc1\n"
    "v_mfma_f32_16x16x32_bf16 v[128:131], a[228:231], a[124:127], v[128:131]\n"
    "ds_read_b128 v[204:207], v10 offset:9216\n"
    "v_mfma_f32_16x16x32_bf16 v[128:131], a[236:239], v[212:215], v[128:131]\n"
    "ds_read_b128 v[208:211], v10 offset:10240\n"
    "v_mfma_f32_16x16x32_bf16 v[132:135], a[228:231], a[128:131], v[132:135]\n"
    "v_mfma_f32_16x16x32_bf16 v[132:135], a[236:239], v[216:219], v[132:135]\n"
    "buffer_load_dwordx4 v1, s[8:11], s70 offen offset:3072 lds\n"
    "v_mfma_f32_16x16x32_bf16 v[136:139], a[228:231], a[132:135], v[136:139]\n"
    "s_add_u32 s71, s42, 98304\n"
    "ds_read_b128 v[212:215], v10 offset:11264\n"
    "v_mfma_f32_16x16x32_bf16 v[136:139], a[236:239], v[220:223], v[136:139]\n"
    "ds_read_b128 v[216:219], v10 offset:12288\n"
    "v_mfma_f32_16x16x32_bf16 v[140:143], a[228:231], a[136:139], v[140:143]\n"
    "v_mfma_f32_16x16x32_bf16 v[140:143], a[236:239], v[224:227], v[140:143]\n"
    "s_waitcnt vmcnt(28) lgkmcnt(6)\n"
    "buffer_load_dwordx2 v[188:189], v6, s[12:15], s71 offen offset:0 sc1\n"
    "v_mfma_f32_16x16x32_bf16 v[88:91], a[240:243], a[140:143], v[88:91]\n"
    "s_barrier\n"
    "ds_read_b128 v[220:223], v10 offset:13312\n"
    "v_mfma_f32_16x16x32_bf16 v[88:91], a[248:251], v[228:231], v[88:91]\n"
    "s_add_u32 s70, s54, 442368\n"
    "s_add_u32 m0, s60, 20480\n"
    "ds_read_b64_tr_b16 a[224:225], v2 offset:16384\n"
    "v_mfma_f32_16x16x32_bf16 v[92:95], a[240:243], a[144:147], v[92:95]\n"
    "buffer_load_dwordx4 v1, s[8:11], s70 offen offset:0 lds\n"
    "v_mfma_f32_16x16x32_bf16 v[92:95], a[248:251], v[232:235], v[92:95]\n"
    "s_add_u32 s70, s70, 8192\n"
    "ds_read_b64_tr_b16 a[226:227], v2 offset:18432\n"
    "v_mfma_f32_16x16x32_bf16 v[96:99], a[240:243], a[148:151], v[96:99]\n"
    "s_add_u32 s71, s42, 114688\n"
    "ds_read_b64_tr_b16 a[228:229], v2 offset:57344\n"
    "v_mfma_f32_16x16x32_bf16 v[96:99], a[248:251], v[236:239], v[96:99]\n"
    "ds_read_b64_tr_b16 a[230:231], v2 offset:59392\n"
    "v_mfma_f32_16x16x32_bf16 v[100:103], a[240:243], a[152:155], v[100:103]\n"
    "ds_read_b64_tr_b16 a[232:233], v3 offset:16384\n"
    "v_mfma_f32_16x16x32_bf16 v[100:103], a[248:251], v[240:243], v[100:103]\n"
    "buffer_load_dwordx2 v[190:191], v6, s[12:15], s71 offen offset:0 sc1\n"
    "v_mfma_f32_16x16x32_bf16 v[104:107], a[240:243], a[156:159], v[104:107]\n"
    "ds_read_b64_tr_b16 a[234:235], v3 offset:18432\n"
    "v_mfma_f32_16x16x32_bf16 v[104:107], a[248:251], v[244:247], v[104:107]\n"
    "buffer_load_dwordx4 v1, s[8:11], s70 offen offset:1024 lds\n"
    "v_mfma_f32_16x16x32_bf16 v[108:111], a[240:243], a[160:163], v[108:111]\n"
    "s_add_u32 s70, s70, 8192\n"
    "ds_read_b64_tr_b16 a[236:237], v3 offset:57344\n"
    "v_mfma_f32_16x16x32_bf16 v[108:111], a[248:251], v[248:251], v[108:111]\n"
    "ds_read_b64_tr_b16 a[238:239], v3 offset:59392\n"
    "v_mfma_f32_16x16x32_bf16 v[112:115], a[240:243], a[164:167], v[112:115]\n"
    "s_waitcnt lgkmcnt(14)\n"
    "ds_read_b128 v[224:227], v10 offset:14336\n"
    "v_mfma_f32_16x16x32_bf16 v[112:115], a[248:251], v[252:255], v[112:115]\n"
    "v_mfma_f32_16x16x32_bf16 v[116:119], a[244:247], a[140:143], v[116:119]\n"
    "v_mfma_f32_16x16x32_bf16 v[116:119], a[252:255], v[228:231], v[116:119]\n"
    "v_mfma_f32_16x16x32_bf16 v[120:123], a[244:247], a[144:147], v[120:123]\n"
    "buffer_load_dwordx4 v1, s[8:11], s70 offen offset:2048 lds\n"
    "v_mfma_f32_16x16x32_bf16 v[120:123], a[252:255], v[232:235], v[120:123]\n"
    "s_add_u32 s70, s70, 8192\n"
    "ds_read_b128 v[228:231], v10 offset:49152\n"
    "v_mfma_f32_16x16x32_bf16 v[124:127], a[244:247], a[148:151], v[124:127]\n"
    "s_sub_u32 s74, s20, 1\n"
    "v_mfma_f32_16x16x32_bf16 v[124:127], a[252:255], v[236:239], v[124:127]\n"
    "s_and_b32 s74, s74, 7\n"
    "ds_read_b128 v[232:235], v10 offset:50176\n"
    "v_mfma_f32_16x16x32_bf16 v[128:131], a[244:247], a[152:155], v[128:131]\n"
    "s_lshl_b32 s74, s74, 14\n"
    "s_add_u32 s71, s41, s74\n"
    "v_mfma_f32_16x16x32_bf16 v[128:131], a[252:255], v[240:243], v[128:131]\n"
    "buffer_load_dwordx4 v[192:195], v0, s[12:15], s71 offen offset:0 sc1\n"
    "v_mfma_f32_16x16x32_bf16 v[132:135], a[244:247], a[156:159], v[132:135]\n"
    "ds_read_b128 v[236:239], v10 offset:51200\n"
    "v_mfma_f32_16x16x32_bf16 v[132:135], a[252:255], v[244:247], v[132:135]\n"
    "buffer_load_dwordx4 v1, s[8:11], s70 offen offset:3072 lds\n"
    "v_mfma_f32_16x16x32_bf16 v[136:139], a[244:247], a[160:163], v[136:139]\n"
    "s_add_u32 s70, s54, 36864\n"
    "ds_read_b128 v[240:243], v10 offset:52224\n"
    "v_mfma_f32_16x16x32_bf16 v[136:139], a[252:255], v[248:251], v[136:139]\n"
    "s_add_u32 m0, s60, 24576\n"
    "ds_read_b128 v[244:247], v10 offset:53248\n"
    "v_mfma_f32_16x16x32_bf16 v[140:143], a[244:247], a[164:167], v[140:143]\n"
    "v_mfma_f32_16x16x32_bf16 v[140:143], a[252:255], v[252:255], v[140:143]\n"
    "s_waitcnt vmcnt(28) lgkmcnt(6)\n"
    "ds_read_b128 v[248:251], v10 offset:54272\n"
    "v_mfma_f32_16x16x32_bf16 v[88:91], a[224:227], a[168:171], v[88:91]\n"
    "s_barrier\n"
    "v_mfma_f32_16x16x32_bf16 v[88:91], a[232:235], v[200:203], v[88:91]\n"
    "s_waitcnt vmcnt(18)\n"
    "ds_read_b64_tr_b16 a[240:241], v2 offset:20480\n"
    "v_mfma_f32_16x16x32_bf16 v[172:175], v[26:29], v[176:179], 0\n"
    "v_mfma_f32_16x16x32_bf16 v[92:95], a[224:227], a[172:175], v[92:95]\n"
    "buffer_load_dwordx4 v1, s[8:11], s70 offen offset:0 lds\n"
    "v_mfma_f32_16x16x32_bf16 v[92:95], a[232:235], v[204:207], v[92:95]\n"
    "s_add_u32 s70, s70, 8192\n"
    "ds_read_b64_tr_b16 a[242:243], v2 offset:22528\n"
    "v_mfma_f32_16x16x32_bf16 v[96:99], a[224:227], a[176:179], v[96:99]\n"
    "ds_read_b64_tr_b16 a[244:245], v2 offset:61440\n"
    "v_mfma_f32_16x16x32_bf16 v[96:99], a[232:235], v[208:211], v[96:99]\n"
    "buffer_load_dwordx4 v[176:179], v0, s[12:15], s71 offen offset:1024 sc1\n"
    "v_mfma_f32_16x16x32_bf16 v[100:103], a[224:227], a[180:183], v[100:103]\n"
    "ds_read_b64_tr_b16 a[246:247], v2 offset:63488\n"
    "v_mfma_f32_16x16x32_bf16 v[100:103], a[232:235], v[212:215], v[100:103]\n"
    "ds_read_b64_tr_b16 a[248:249], v3 offset:20480\n"
    "v_mfma_f32_16x16x32_bf16 v[104:107], a[224:227], a[184:187], v[104:107]\n"
    "ds_read_b64_tr_b16 a[250:251], v3 offset:22528\n"
    "v_mfma_f32_16x16x32_bf16 v[104:107], a[232:235], v[216:219], v[104:107]\n"
    "buffer_load_dwordx4 v1, s[8:11], s70 offen offset:1024 lds\n"
    "v_mfma_f32_16x16x32_bf16 v[108:111], a[224:227], a[188:191], v[108:111]\n"
    "s_add_u32 s70, s70, 8192\n"
    "ds_read_b64_tr_b16 a[252:253], v3 offset:61440\n"
    "v_mfma_f32_16x16x32_bf16 v[108:111], a[232:235], v[220:223], v[108:111]\n"
    "ds_read_b64_tr_b16 a[254:255], v3 offset:63488\n"
    "v_mfma_f32_16x16x32_bf16 v[112:115], a[224:227], a[192:195], v[112:115]\n"
    "s_waitcnt lgkmcnt(14)\n"
    "ds_read_b128 v[252:255], v10 offset:55296\n"
    "v_mfma_f32_16x16x32_bf16 v[112:115], a[232:235], v[224:227], v[112:115]\n"
    "v_mfma_f32_16x16x32_bf16 v[116:119], a[228:231], a[168:171], v[116:119]\n"
    "v_mfma_f32_16x16x32_bf16 v[116:119], a[236:239], v[200:203], v[116:119]\n"
    "v_mfma_f32_16x16x32_bf16 v[120:123], a[228:231], a[172:175], v[120:123]\n"
    "buffer_load_dwordx4 v1, s[8:11], s70 offen offset:2048 lds\n"
    "v_mfma_f32_16x16x32_bf16 v[120:123], a[236:239], v[204:207], v[120:123]\n"
    "s_add_u32 s70, s70, 8192\n"
    "ds_read_b128 v[200:203], v4 offset:32768\n"
    "v_mfma_f32_16x16x32_bf16 v[124:127], a[228:231], a[176:179], v[124:127]\n"
    "s_waitcnt vmcnt(17)\n"
    "v_mfma_f32_16x16x32_bf16 v[172:175], v[26:29], v[180:183], v[172:175]\n"
    "v_mfma_f32_16x16x32_bf16 v[124:127], a[236:239], v[208:211], v[124:127]\n"
    "ds_read_b128 v[204:207], v4 offset:33792\n"
    "v_mfma_f32_16x16x32_bf16 v[128:131], a[228:231], a[180:183], v[128:131]\n"
    "v_mfma_f32_16x16x32_bf16 v[128:131], a[236:239], v[212:215], v[128:131]\n"
    "ds_read_b128 v[208:211], v4 offset:34816\n"
    "v_mfma_f32_16x16x32_bf16 v[132:135], a[228:231], a[184:187], v[132:135]\n"
    "buffer_load_dwordx4 v[180:183], v0, s[12:15], s71 offen offset:2048 sc1\n"
    "v_mfma_f32_16x16x32_bf16 v[132:135], a[236:239], v[216:219], v[132:135]\n"
    "ds_read_b128 v[212:215], v4 offset:35840\n"
    "v_mfma_f32_16x16x32_bf16 v[136:139], a[228:231], a[188:191], v[136:139]\n"
    "buffer_load_dwordx4 v1, s[8:11], s70 offen offset:3072 lds\n"
    "v_mfma_f32_16x16x32_bf16 v[136:139], a[236:239], v[220:223], v[136:139]\n"
    "ds_read_b128 v[216:219], v4 offset:36864\n"
    "v_mfma_f32_16x16x32_bf16 v[140:143], a[228:231], a[192:195], v[140:143]\n"
    "v_mfma_f32_16x16x32_bf16 v[140:143], a[236:239], v[224:227], v[140:143]\n"
    "s_waitcnt vmcnt(22) lgkmcnt(6)\n"
    "ds_read_b128 v[220:223], v4 offset:37888\n"
    "v_mfma_f32_16x16x32_bf16 v[88:91], a[240:243], a[196:199], v[88:91]\n"
    "s_barrier\n"
    "v_mfma_f32_16x16x32_bf16 v[88:91], a[248:251], v[228:231], v[88:91]\n"
    "buffer_store_dwordx2 v[30:31], v5, s[12:15], s22 offen offset:64 sc1\n"
    "v_mfma_f32_16x16x32_bf16 v[92:95], a[240:243], a[200:203], v[92:95]\n"
    "s_add_u32 s70, s54, 184320\n"
    "ds_read_b64_tr_b16 a[224:225], v2 offset:0\n"
    "v_mfma_f32_16x16x32_bf16 v[92:95], a[248:251], v[232:235], v[92:95]\n"
    "s_add_u32 m0, s60, 28672\n"
    "ds_read_b64_tr_b16 a[226:227], v2 offset:2048\n"
    "v_cvt_pk_bf16_f32 v88, v88, v89\n"
    "v_mfma_f32_16x16x32_bf16 v[96:99], a[240:243], a[204:207], v[96:99]\n"
    "buffer_load_dwordx4 v1, s[8:11], s70 offen offset:0 lds\n"
    "v_mfma_f32_16x16x32_bf16 v[96:99], a[248:251], v[236:239], v[96:99]\n"
    "s_add_u32 s70, s70, 8192\n"
    "ds_read_b64_tr_b16 a[228:229], v2 offset:40960\n"
    "v_cvt_pk_bf16_f32 v89, v90, v91\n"
    "v_mfma_f32_16x16x32_bf16 v[100:103], a[240:243], a[208:211], v[100:103]\n"
    "ds_read_b64_tr_b16 a[230:231], v2 offset:43008\n"
    "v_cvt_pk_bf16_f32 v90, v92, v93\n"
    "v_mfma_f32_16x16x32_bf16 v[100:103], a[248:251], v[240:243], v[100:103]\n"
    "s_waitcnt vmcnt(16)\n"
    "ds_read_b64_tr_b16 a[232:233], v3 offset:0\n"
    "v_cvt_pk_bf16_f32 v91, v94, v95\n"
    "v_mfma_f32_16x16x32_bf16 v[172:175], v[26:29], v[184:187], v[172:175]\n"
    "v_mfma_f32_16x16x32_bf16 v[104:107], a[240:243], a[212:215], v[104:107]\n"
    "ds_read_b64_tr_b16 a[234:235], v3 offset:2048\n"
    "v_cvt_pk_bf16_f32 v92, v96, v97\n"
    "v_mfma_f32_16x16x32_bf16 v[104:107], a[248:251], v[244:247], v[104:107]\n"
    "buffer_load_dwordx4 v1, s[8:11], s70 offen offset:1024 lds\n"
    "v_mfma_f32_16x16x32_bf16 v[108:111], a[240:243], a[216:219], v[108:111]\n"
    "s_add_u32 s70, s70, 8192\n"
    "ds_read_b64_tr_b16 a[236:237], v3 offset:40960\n"
    "v_cvt_pk_bf16_f32 v93, v98, v99\n"
    "v_mfma_f32_16x16x32_bf16 v[108:111], a[248:251], v[248:251], v[108:111]\n"
    "buffer_load_dwordx2 v[12:13], v6, s[12:15], s71 offen offset:3072 sc1\n"
    "v_mfma_f32_16x16x32_bf16 v[112:115], a[240:243], a[220:223], v[112:115]\n"
    "s_waitcnt lgkmcnt(13)\n"
    "s_add_u32 s74, s20, 4\n"
    "ds_read_b64_tr_b16 a[238:239], v3 offset:43008\n"
    "v_cvt_pk_bf16_f32 v94, v100, v101\n"
    "v_mfma_f32_16x16x32_bf16 v[112:115], a[248:251], v[252:255], v[112:115]\n"
    "s_and_b32 s74, s74, 7\n"
    "ds_read_b128 v[224:227], v4 offset:38912\n"
    "v_cvt_pk_bf16_f32 v95, v102, v103\n"
    "v_mfma_f32_16x16x32_bf16 v[116:119], a[244:247], a[196:199], v[116:119]\n"
    "s_lshl_b32 s74, s74, 17\n"
    "v_cvt_pk_bf16_f32 v96, v104, v105\n"
    "v_cvt_pk_bf16_f32 v97, v106, v107\n"
    "v_mfma_f32_16x16x32_bf16 v[116:119], a[252:255], v[228:231], v[116:119]\n"
    "s_add_u32 s83, s40, s74\n"
    "v_cvt_pk_bf16_f32 v98, v108, v109\n"
    "v_cvt_pk_bf16_f32 v99, v110, v111\n"
    "v_mfma_f32_16x16x32_bf16 v[120:123], a[244:247], a[200:203], v[120:123]\n"
    "buffer_store_dwordx4 v[88:91], v0, s[12:15], s83 offen offset:0 sc1\n"
    "v_mfma_f32_16x16x32_bf16 v[120:123], a[252:255], v[232:235], v[120:123]\n"
    "ds_read_b128 v[228:231], v10 offset:0\n"
    "v_cvt_pk_bf16_f32 v100, v112, v113\n"
    "v_mfma_f32_16x16x32_bf16 v[124:127], a[244:247], a[204:207], v[124:127]\n"
    "buffer_load_dwordx4 v1, s[8:11], s70 offen offset:2048 lds\n"
    "v_mfma_f32_16x16x32_bf16 v[124:127], a[252:255], v[236:239], v[124:127]\n"
    "s_add_u32 s70, s70, 8192\n"
    "ds_read_b128 v[232:235], v10 offset:1024\n"
    "v_cvt_pk_bf16_f32 v101, v114, v115\n"
    "v_mfma_f32_16x16x32_bf16 v[128:131], a[244:247], a[208:211], v[128:131]\n"
    "v_cvt_pk_bf16_f32 v116, v116, v117\n"
    "v_cvt_pk_bf16_f32 v117, v118, v119\n"
    "v_mfma_f32_16x16x32_bf16 v[128:131], a[252:255], v[240:243], v[128:131]\n"
    "ds_read_b128 v[236:239], v10 offset:2048\n"
    "v_cvt_pk_bf16_f32 v118, v120, v121\n"
    "v_mfma_f32_16x16x32_bf16 v[132:135], a[244:247], a[212:215], v[132:135]\n"
    "v_cvt_pk_bf16_f32 v119, v122, v123\n"
    "v_cvt_pk_bf16_f32 v120, v124, v125\n"
    "v_mfma_f32_16x16x32_bf16 v[132:135], a[252:255], v[244:247], v[132:135]\n"
    "s_waitcnt vmcnt(16)\n"
    "buffer_load_dwordx4 v1, s[8:11], s70 offen offset:3072 lds\n"
    "v_mfma_f32_16x16x32_bf16 v[172:175], v[26:29], v[188:191], v[172:175]\n"
    "v_mfma_f32_16x16x32_bf16 v[136:139], a[244:247], a[216:219], v[136:139]\n"
    "ds_read_b128 v[240:243], v10 offset:3072\n"
    "v_cvt_pk_bf16_f32 v121, v126, v127\n"
    "v_mfma_f32_16x16x32_bf16 v[136:139], a[252:255], v[248:251], v[136:139]\n"
    "ds_read_b128 v[244:247], v10 offset:4096\n"
    "v_cvt_pk_bf16_f32 v122, v128, v129\n"
    "v_mfma_f32_16x16x32_bf16 v[140:143], a[244:247], a[220:223], v[140:143]\n"
    "v_cvt_pk_bf16_f32 v123, v130, v131\n"
    "v_cvt_pk_bf16_f32 v124, v132, v133\n"
    "v_mfma_f32_16x16x32_bf16 v[140:143], a[252:255], v[252:255], v[140:143]\n"
    "s_waitcnt vmcnt(27) lgkmcnt(6)\n"
    "ds_read_b128 v[248:251], v10 offset:5120\n"
    "v_cvt_pk_bf16_f32 v125, v134, v135\n"
    "v_mfma_f32_16x16x32_bf16 v[32:35], a[224:227], a[0:3], 0\n"
    "s_barrier\n"
    "v_cvt_pk_bf16_f32 v126, v136, v137\n"
    "v_cvt_pk_bf16_f32 v127, v138, v139\n"
    "v_mfma_f32_16x16x32_bf16 v[32:35], a[232:235], v[200:203], v[32:35]\n"
    "buffer_store_dwordx4 v[92:95], v0, s[12:15], s83 offen offset:1024 sc1\n"
    "v_mfma_f32_16x16x32_bf16 v[36:39], a[224:227], a[4:7], 0\n"
    "s_add_u32 s70, s54, 331776\n"
    "ds_read_b64_tr_b16 a[240:241], v2 offset:4096\n"
    "v_cvt_pk_bf16_f32 v128, v140, v141\n"
    "v_mfma_f32_16x16x32_bf16 v[36:39], a[232:235], v[204:207], v[36:39]\n"
    "s_add_u32 m0, s60, 8192\n"
    "ds_read_b64_tr_b16 a[242:243], v2 offset:6144\n"
    "v_cvt_pk_bf16_f32 v129, v142, v143\n"
    "v_mfma_f32_16x16x32_bf16 v[40:43], a[224:227], a[8:11], 0\n"
    "buffer_load_dwordx4 v1, s[8:11], s70 offen offset:0 lds\n"
    "v_mfma_f32_16x16x32_bf16 v[40:43], a[232:235], v[208:211], v[40:43]\n"
    "s_add_u32 s70, s70, 8192\n"
    "ds_read_b64_tr_b16 a[244:245], v2 offset:45056\n"
    "v_mfma_f32_16x16x32_bf16 v[44:47], a[224:227], a[12:15], 0\n"
    "ds_read_b64_tr_b16 a[246:247], v2 offset:47104\n"
    "v_mfma_f32_16x16x32_bf16 v[44:47], a[232:235], v[212:215], v[44:47]\n"
    "ds_read_b64_tr_b16 a[248:249], v3 offset:4096\n"
    "v_mfma_f32_16x16x32_bf16 v[48:51], a[224:227], a[16:19], 0\n"
    "ds_read_b64_tr_b16 a[250:251], v3 offset:6144\n"
    "v_mfma_f32_16x16x32_bf16 v[48:51], a[232:235], v[216:219], v[48:51]\n"
    "s_waitcnt vmcnt(16)\n"
    "buffer_load_dwordx4 v1, s[8:11], s70 offen offset:1024 lds\n"
    "v_mfma_f32_16x16x32_bf16 v[144:147], v[22:25], v[192:195], 0\n"
    "v_mfma_f32_16x16x32_bf16 v[148:151], v[20:23], v[192:195], 0\n"
    "v_mfma_f32_16x16x32_bf16 v[52:55], a[224:227], a[20:23], 0\n"
    "s_add_u32 s70, s70, 8192\n"
    "ds_read_b64_tr_b16 a[252:253], v3 offset:45056\n"
    "v_mfma_f32_16x16x32_bf16 v[52:55], a[232:235], v[220:223], v[52:55]\n"
    "ds_read_b64_tr_b16 a[254:255], v3 offset:47104\n"
    "v_mfma_f32_16x16x32_bf16 v[56:59], a[224:227], a[24:27], 0\n"
    "s_waitcnt lgkmcnt(14)\n"
    "ds_read_b128 v[252:255], v10 offset:6144\n"
    "v_mfma_f32_16x16x32_bf16 v[56:59], a[232:235], v[224:227], v[56:59]\n"
    "v_mfma_f32_16x16x32_bf16 v[60:63], a[228:231], a[0:3], 0\n"
    "v_mfma_f32_16x16x32_bf16 v[60:63], a[236:239], v[200:203], v[60:63]\n"
    "v_mfma_f32_16x16x32_bf16 v[64:67], a[228:231], a[4:7], 0\n"
    "buffer_load_dwordx4 v1, s[8:11], s70 offen offset:2048 lds\n"
    "v_mfma_f32_16x16x32_bf16 v[64:67], a[236:239], v[204:207], v[64:67]\n"
    "s_add_u32 s70, s70, 8192\n"
    "ds_read_b128 v[200:203], v10 offset:40960\n"
    "v_mfma_f32_16x16x32_bf16 v[68:71], a[228:231], a[8:11], 0\n"
    "v_mfma_f32_16x16x32_bf16 v[68:71], a[236:239], v[208:211], v[68:71]\n"
    "buffer_store_dwordx4 v[96:99], v0, s[12:15], s83 offen offset:2048 sc1\n"
    "v_mfma_f32_16x16x32_bf16 v[72:75], a[228:231], a[12:15], 0\n"
    "ds_read_b128 v[204:207], v10 offset:41984\n"
    "v_mfma_f32_16x16x32_bf16 v[72:75], a[236:239], v[212:215], v[72:75]\n"
    "s_waitcnt vmcnt(16)\n"
    "ds_read_b128 v[208:211], v10 offset:43008\n"
    "v_mfma_f32_16x16x32_bf16 v[152:155], v[22:25], v[176:179], 0\n"
    "v_mfma_f32_16x16x32_bf16 v[156:159], v[20:23], v[176:179], 0\n"
    "v_mfma_f32_16x16x32_bf16 v[76:79], a[228:231], a[16:19], 0\n"
    "v_mfma_f32_16x16x32_bf16 v[76:79], a[236:239], v[216:219], v[76:79]\n"
    "buffer_load_dwordx4 v1, s[8:11], s70 offen offset:3072 lds\n"
    "v_mfma_f32_16x16x32_bf16 v[80:83], a[228:231], a[20:23], 0\n"
    "s_add_u32 s70, s54, 479232\n"
    "ds_read_b128 v[212:215], v10 offset:44032\n"
    "v_mfma_f32_16x16x32_bf16 v[80:83], a[236:239], v[220:223], v[80:83]\n"
    "s_add_u32 m0, s60, 12288\n"
    "ds_read_b128 v[216:219], v10 offset:45056\n"
    "v_mfma_f32_16x16x32_bf16 v[84:87], a[228:231], a[24:27], 0\n"
    "v_mfma_f32_16x16x32_bf16 v[84:87], a[236:239], v[224:227], v[84:87]\n"
    "s_waitcnt vmcnt(26) lgkmcnt(6)\n"
    "ds_read_b128 v[220:223], v10 offset:46080\n"
    "v_mfma_f32_16x16x32_bf16 v[32:35], a[240:243], a[28:31], v[32:35]\n"
    "s_barrier\n"
    "v_mfma_f32_16x16x32_bf16 v[32:35], a[248:251], v[228:231], v[32:35]\n"
    "ds_read_b64_tr_b16 a[224:225], v2 offset:8192\n"
    "v_mfma_f32_16x16x32_bf16 v[36:39], a[240:243], a[32:35], v[36:39]\n"
    "buffer_load_dwordx4 v1, s[8:11], s70 offen offset:0 lds\n"
    "v_mfma_f32_16x16x32_bf16 v[36:39], a[248:251], v[232:235], v[36:39]\n"
    "s_add_u32 s70, s70, 8192\n"
    "ds_read_b64_tr_b16 a[226:227], v2 offset:10240\n"
    "v_mfma_f32_16x16x32_bf16 v[40:43], a[240:243], a[36:39], v[40:43]\n"
    "ds_read_b64_tr_b16 a[228:229], v2 offset:49152\n"
    "v_mfma_f32_16x16x32_bf16 v[40:43], a[248:251], v[236:239], v[40:43]\n"
    "ds_read_b64_tr_b16 a[230:231], v2 offset:51200\n"
    "v_mfma_f32_16x16x32_bf16 v[44:47], a[240:243], a[40:43], v[44:47]\n"
    "ds_read_b64_tr_b16 a[232:233], v3 offset:8192\n"
    "v_mfma_f32_16x16x32_bf16 v[44:47], a[248:251], v[240:243], v[44:47]\n"
    "ds_read_b64_tr_b16 a[234:235], v3 offset:10240\n"
    "v_mfma_f32_16x16x32_bf16 v[48:51], a[240:243], a[44:47], v[48:51]\n"
    "ds_read_b64_tr_b16 a[236:237], v3 offset:49152\n"
    "v_mfma_f32_16x16x32_bf16 v[48:51], a[248:251], v[244:247], v[48:51]\n"
    "buffer_load_dwordx4 v1, s[8:11], s70 offen offset:1024 lds\n"
    "v_mfma_f32_16x16x32_bf16 v[52:55], a[240:243], a[48:51], v[52:55]\n"
    "s_add_u32 s70, s70, 8192\n"
    "ds_read_b64_tr_b16 a[238:239], v3 offset:51200\n"
    "v_mfma_f32_16x16x32_bf16 v[52:55], a[248:251], v[248:251], v[52:55]\n"
    "buffer_store_dwordx2 v[100:101], v6, s[12:15], s83 offen offset:3072 sc1\n"
    "v_mfma_f32_16x16x32_bf16 v[56:59], a[240:243], a[52:55], v[56:59]\n"
    "s_waitcnt lgkmcnt(14)\n"
    "ds_read_b128 v[224:227], v10 offset:47104\n"
    "v_mfma_f32_16x16x32_bf16 v[56:59], a[248:251], v[252:255], v[56:59]\n"
    "v_mfma_f32_16x16x32_bf16 v[60:63], a[244:247], a[28:31], v[60:63]\n"
    "buffer_load_dwordx2 v[198:199], v8, s[12:15], 0 offen offset:64 sc1\n"
    "v_mfma_f32_16x16x32_bf16 v[60:63], a[252:255], v[228:231], v[60:63]\n"
    "v_mfma_f32_16x16x32_bf16 v[64:67], a[244:247], a[32:35], v[64:67]\n"
    "buffer_load_dwordx4 v1, s[8:11], s70 offen offset:2048 lds\n"
    "v_mfma_f32_16x16x32_bf16 v[64:67], a[252:255], v[232:235], v[64:67]\n"
    "s_add_u32 s70, s70, 8192\n"
    "ds_read_b128 v[228:231], v18 offset:8192\n"
    "v_mfma_f32_16x16x32_bf16 v[68:71], a[244:247], a[36:39], v[68:71]\n"
    "s_add_u32 s74, s20, 5\n"
    "v_mfma_f32_16x16x32_bf16 v[68:71], a[252:255], v[236:239], v[68:71]\n"
    "s_and_b32 s74, s74, 7\n"
    "ds_read_b128 v[232:235], v18 offset:9216\n"
    "v_mfma_f32_16x16x32_bf16 v[72:75], a[244:247], a[40:43], v[72:75]\n"
    "s_lshl_b32 s74, s74, 17\n"
    "s_add_u32 s83, s40, s74\n"
    "v_mfma_f32_16x16x32_bf16 v[72:75], a[252:255], v[240:243], v[72:75]\n"
    "buffer_store_dwordx4 v[116:119], v0, s[12:15], s83 offen offset:0 sc1\n"
    "v_mfma_f32_16x16x32_bf16 v[76:79], a[244:247], a[44:47], v[76:79]\n"
    "ds_read_b128 v[236:239], v18 offset:10240\n"
    "v_mfma_f32_16x16x32_bf16 v[76:79], a[252:255], v[244:247], v[76:79]\n"
    "buffer_load_dwordx4 v1, s[8:11], s70 offen offset:3072 lds\n"
    "v_mfma_f32_16x16x32_bf16 v[80:83], a[244:247], a[48:51], v[80:83]\n"
    "s_add_u32 s70, s55, 0\n"
    "ds_read_b128 v[240:243], v18 offset:11264\n"
    "v_mfma_f32_16x16x32_bf16 v[80:83], a[252:255], v[248:251], v[80:83]\n"
    "s_add_u32 m0, s60, 16384\n"
    "ds_read_b128 v[244:247], v18 offset:12288\n"
    "v_mfma_f32_16x16x32_bf16 v[84:87], a[244:247], a[52:55], v[84:87]\n"
    "v_mfma_f32_16x16x32_bf16 v[84:87], a[252:255], v[252:255], v[84:87]\n"
    "s_waitcnt vmcnt(26) lgkmcnt(6)\n"
    "ds_read_b128 v[248:251], v18 offset:13312\n"
    "v_mfma_f32_16x16x32_bf16 v[32:35], a[224:227], a[56:59], v[32:35]\n"
    "s_barrier\n"
    "v_mfma_f32_16x16x32_bf16 v[32:35], a[232:235], v[200:203], v[32:35]\n"
    "s_waitcnt vmcnt(16)\n"
    "ds_read_b64_tr_b16 a[240:241], v2 offset:12288\n"
    "v_mfma_f32_16x16x32_bf16 v[160:163], v[22:25], v[180:183], 0\n"
    "v_mfma_f32_16x16x32_bf16 v[164:167], v[20:23], v[180:183], 0\n"
    "v_mfma_f32_16x16x32_bf16 v[168:171], v[26:29], v[12:15], 0\n"
    "v_mfma_f32_16x16x32_bf16 v[36:39], a[224:227], a[60:63], v[36:39]\n"
    "buffer_load_dwordx4 v1, s[8:11], s70 offen offset:0 lds\n"
    "v_mfma_f32_16x16x32_bf16 v[36:39], a[232:235], v[204:207], v[36:39]\n"
    "s_add_u32 s70, s70, 8192\n"
    "ds_read_b64_tr_b16 a[242:243], v2 offset:14336\n"
    "v_mfma_f32_16x16x32_bf16 v[40:43], a[224:227], a[64:67], v[40:43]\n"
    "ds_read_b64_tr_b16 a[244:245], v2 offset:53248\n"
    "v_mfma_f32_16x16x32_bf16 v[40:43], a[232:235], v[208:211], v[40:43]\n"
    "ds_read_b64_tr_b16 a[246:247], v2 offset:55296\n"
    "v_mfma_f32_16x16x32_bf16 v[44:47], a[224:227], a[68:71], v[44:47]\n"
    "ds_read_b64_tr_b16 a[248:249], v3 offset:12288\n"
    "v_mfma_f32_16x16x32_bf16 v[44:47], a[232:235], v[212:215], v[44:47]\n"
    "ds_read_b64_tr_b16 a[250:251], v3 offset:14336\n"
    "v_mfma_f32_16x16x32_bf16 v[48:51], a[224:227], a[72:75], v[48:51]\n"
    "buffer_store_dwordx4 v[120:123], v0, s[12:15], s83 offen offset:1024 sc1\n"
    "v_mfma_f32_16x16x32_bf16 v[48:51], a[232:235], v[216:219], v[48:51]\n"
    "ds_read_b64_tr_b16 a[252:253], v3 offset:53248\n"
    "v_mfma_f32_16x16x32_bf16 v[52:55], a[224:227], a[76:79], v[52:55]\n"
    "buffer_load_dwordx4 v1, s[8:11], s70 offen offset:1024 lds\n"
    "v_mfma_f32_16x16x32_bf16 v[52:55], a[232:235], v[220:223], v[52:55]\n"
    "s_add_u32 s70, s70, 8192\n"
    "ds_read_b64_tr_b16 a[254:255], v3 offset:55296\n"
    "v_mfma_f32_16x16x32_bf16 v[56:59], a[224:227], a[80:83], v[56:59]\n"
    "s_waitcnt lgkmcnt(14)\n"
    "ds_read_b128 v[252:255], v18 offset:14336\n"
    "v_mfma_f32_16x16x32_bf16 v[56:59], a[232:235], v[224:227], v[56:59]\n"
    "v_mfma_f32_16x16x32_bf16 v[60:63], a[228:231], a[56:59], v[60:63]\n"
    "v_mfma_f32_16x16x32_bf16 v[60:63], a[236:239], v[200:203], v[60:63]\n"
    "v_mfma_f32_16x16x32_bf16 v[64:67], a[228:231], a[60:63], v[64:67]\n"
    "buffer_load_dwordx4 v1, s[8:11], s70 offen offset:2048 lds\n"
    "v_mfma_f32_16x16x32_bf16 v[64:67], a[236:239], v[204:207], v[64:67]\n"
    "s_add_u32 s70, s70, 8192\n"
    "ds_read_b128 v[200:203], v4 offset:0\n"
    "v_mfma_f32_16x16x32_bf16 v[68:71], a[228:231], a[64:67], v[68:71]\n"
    "v_mfma_f32_16x16x32_bf16 v[68:71], a[236:239], v[208:211], v[68:71]\n"
    "ds_read_b128 v[204:207], v4 offset:1024\n"
    "v_mfma_f32_16x16x32_bf16 v[72:75], a[228:231], a[68:71], v[72:75]\n"
    "v_mfma_f32_16x16x32_bf16 v[72:75], a[236:239], v[212:215], v[72:75]\n"
    "ds_read_b128 v[208:211], v4 offset:2048\n"
    "v_mfma_f32_16x16x32_bf16 v[76:79], a[228:231], a[72:75], v[76:79]\n"
    "v_mfma_f32_16x16x32_bf16 v[76:79], a[236:239], v[216:219], v[76:79]\n"
    "buffer_load_dwordx4 v1, s[8:11], s70 offen offset:3072 lds\n"
    "v_mfma_f32_16x16x32_bf16 v[80:83], a[228:231], a[76:79], v[80:83]\n"
    "ds_read_b128 v[212:215], v4 offset:3072\n"
    "v_mfma_f32_16x16x32_bf16 v[80:83], a[236:239], v[220:223], v[80:83]\n"
    "ds_read_b128 v[216:219], v4 offset:4096\n"
    "v_mfma_f32_16x16x32_bf16 v[84:87], a[228:231], a[80:83], v[84:87]\n"
    "buffer_store_dwordx4 v[124:127], v0, s[12:15], s83 offen offset:2048 sc1\n"
    "v_mfma_f32_16x16x32_bf16 v[84:87], a[236:239], v[224:227], v[84:87]\n"
    "s_waitcnt vmcnt(26) lgkmcnt(6)\n"
    "ds_read_b128 v[220:223], v4 offset:5120\n"
    "v_mfma_f32_16x16x32_bf16 v[32:35], a[240:243], a[84:87], v[32:35]\n"
    "s_barrier\n"
    "v_mfma_f32_16x16x32_bf16 v[32:35], a[248:251], v[228:231], v[32:35]\n"
    "s_waitcnt vmcnt(9)\n"
    "s_add_u32 s70, s55, 294912\n"
    "s_add_u32 m0, s60, 20480\n"
    "v_cmp_ne_u64 vcc, s[68:69], v[198:199]\n"
    "v_mfma_f32_16x16x32_bf16 v[36:39], a[240:243], a[88:91], v[36:39]\n"
    "s_cbranch_vccnz LSP1_%=\n"
    "LRT1_%=:\n"
    "buffer_load_dwordx4 v1, s[8:11], s70 offen offset:0 lds\n"
    "v_mfma_f32_16x16x32_bf16 v[36:39], a[248:251], v[232:235], v[36:39]\n"
    "s_add_u32 s70, s70, 8192\n"
    "ds_read_b64_tr_b16 a[224:225], v2 offset:16384\n"
    "ds_read_b64_tr_b16 a[226:227], v2 offset:18432\n"
    "v_mfma_f32_16x16x32_bf16 v[40:43], a[240:243], a[92:95], v[40:43]\n"
    "s_sub_u32 s74, s20, 2\n"
    "ds_read_b128 v[224:227], v4 offset:6144\n"
    "v_mfma_f32_16x16x32_bf16 v[40:43], a[248:251], v[236:239], v[40:43]\n"
    "s_and_b32 s74, s74, 7\n"
    "ds_read_b64_tr_b16 a[228:229], v2 offset:57344\n"
    "ds_read_b64_tr_b16 a[230:231], v2 offset:59392\n"
    "v_mfma_f32_16x16x32_bf16 v[44:47], a[240:243], a[96:99], v[44:47]\n"
    "s_lshl_b32 s74, s74, 14\n"
    "s_add_u32 s71, s41, s74\n"
    "v_mfma_f32_16x16x32_bf16 v[44:47], a[248:251], v[240:243], v[44:47]\n"
    "buffer_load_dwordx4 v[184:187], v0, s[12:15], s71 offen offset:0 sc1\n"
    "v_mfma_f32_16x16x32_bf16 v[48:51], a[240:243], a[100:103], v[48:51]\n"
    "ds_read_b64_tr_b16 a[232:233], v3 offset:16384\n"
    "ds_read_b64_tr_b16 a[234:235], v3 offset:18432\n"
    "v_mfma_f32_16x16x32_bf16 v[48:51], a[248:251], v[244:247], v[48:51]\n"
    "buffer_load_dwordx4 v1, s[8:11], s70 offen offset:1024 lds\n"
    "v_mfma_f32_16x16x32_bf16 v[52:55], a[240:243], a[104:107], v[52:55]\n"
    "s_add_u32 s70, s70, 8192\n"
    "ds_read_b64_tr_b16 a[236:237], v3 offset:57344\n"
    "ds_read_b64_tr_b16 a[238:239], v3 offset:59392\n"
    "v_mfma_f32_16x16x32_bf16 v[52:55], a[248:251], v[248:251], v[52:55]\n"
    "v_mfma_f32_16x16x32_bf16 v[56:59], a[240:243], a[108:111], v[56:59]\n"
    "s_waitcnt lgkmcnt(15)\n"
    "v_mfma_f32_16x16x32_bf16 v[56:59], a[248:251], v[252:255], v[56:59]\n"
    "v_mfma_f32_16x16x32_bf16 v[60:63], a[244:247], a[84:87], v[60:63]\n"
    "v_mfma_f32_16x16x32_bf16 v[60:63], a[252:255], v[228:231], v[60:63]\n"
    "v_mfma_f32_16x16x32_bf16 v[64:67], a[244:247], a[88:91], v[64:67]\n"
    "buffer_load_dwordx4 v1, s[8:11], s70 offen offset:2048 lds\n"
    "v_mfma_f32_16x16x32_bf16 v[64:67], a[252:255], v[232:235], v[64:67]\n"
    "s_add_u32 s70, s70, 8192\n"
    "ds_read_b128 v[228:231], v4 offset:40960\n"
    "v_mfma_f32_16x16x32_bf16 v[68:71], a[244:247], a[92:95], v[68:71]\n"
    "buffer_store_dwordx2 v[128:129], v6, s[12:15], s83 offen offset:3072 sc1\n"
    "v_mfma_f32_16x16x32_bf16 v[68:71], a[252:255], v[236:239], v[68:71]\n"
    "ds_read_b128 v[232:235], v4 offset:41984\n"
    "v_mfma_f32_16x16x32_bf16 v[72:75], a[244:247], a[96:99], v[72:75]\n"
    "v_mfma_f32_16x16x32_bf16 v[72:75], a[252:255], v[240:243], v[72:75]\n"
    "buffer_load_dwordx4 v[188:191], v0, s[12:15], s71 offen offset:1024 sc1\n"
    "v_mfma_f32_16x16x32_bf16 v[76:79], a[244:247], a[100:103], v[76:79]\n"
    "ds_read_b128 v[236:239], v4 offset:43008\n"
    "v_mfma_f32_16x16x32_bf16 v[76:79], a[252:255], v[244:247], v[76:79]\n"
    "buffer_load_dwordx4 v1, s[8:11], s70 offen offset:3072 lds\n"
    "v_mfma_f32_16x16x32_bf16 v[80:83], a[244:247], a[104:107], v[80:83]\n"
    "s_add_u32 s70, s55, 36864\n"
    "ds_read_b128 v[240:243], v4 offset:44032\n"
    "v_mfma_f32_16x16x32_bf16 v[80:83], a[252:255], v[248:251], v[80:83]\n"
    "s_add_u32 m0, s60, 24576\n"
    "ds_read_b128 v[244:247], v4 offset:45056\n"
    "v_mfma_f32_16x16x32_bf16 v[84:87], a[244:247], a[108:111], v[84:87]\n"
    "v_mfma_f32_16x16x32_bf16 v[84:87], a[252:255], v[252:255], v[84:87]\n"
    "s_waitcnt vmcnt(26) lgkmcnt(5)\n"
    "ds_read_b128 v[248:251], v4 offset:46080\n"
    "v_mfma_f32_16x16x32_bf16 v[32:35], a[224:227], a[112:115], v[32:35]\n"
    "s_barrier\n"
    "v_mfma_f32_16x16x32_bf16 v[32:35], a[232:235], v[200:203], v[32:35]\n"
    "ds_read_b64_tr_b16 a[240:241], v2 offset:20480\n"
    "v_mfma_f32_16x16x32_bf16 v[36:39], a[224:227], a[116:119], v[36:39]\n"
    "buffer_load_dwordx4 v1, s[8:11], s70 offen offset:0 lds\n"
    "v_mfma_f32_16x16x32_bf16 v[36:39], a[232:235], v[204:207], v[36:39]\n"
    "s_add_u32 s70, s70, 8192\n"
    "ds_read_b64_tr_b16 a[242:243], v2 offset:22528\n"
    "v_mfma_f32_16x16x32_bf16 v[40:43], a[224:227], a[120:123], v[40:43]\n"
    "ds_read_b64_tr_b16 a[244:245], v2 offset:61440\n"
    "v_mfma_f32_16x16x32_bf16 v[40:43], a[232:235], v[208:211], v[40:43]\n"
    "ds_read_b64_tr_b16 a[246:247], v2 offset:63488\n"
    "v_mfma_f32_16x16x32_bf16 v[44:47], a[224:227], a[124:127], v[44:47]\n"
    "ds_read_b64_tr_b16 a[248:249], v3 offset:20480\n"
    "v_mfma_f32_16x16x32_bf16 v[44:47], a[232:235], v[212:215], v[44:47]\n"
    "ds_read_b64_tr_b16 a[250:251], v3 offset:22528\n"
    "v_mfma_f32_16x16x32_bf16 v[48:51], a[224:227], a[128:131], v[48:51]\n"
    "ds_read_b64_tr_b16 a[252:253], v3 offset:61440\n"
    "v_mfma_f32_16x16x32_bf16 v[48:51], a[232:235], v[216:219], v[48:51]\n"
    "buffer_load_dwordx4 v1, s[8:11], s70 offen offset:1024 lds\n"
    "v_mfma_f32_16x16x32_bf16 v[52:55], a[224:227], a[132:135], v[52:55]\n"
    "s_add_u32 s70, s70, 8192\n"
    "ds_read_b64_tr_b16 a[254:255], v3 offset:63488\n"
    "v_mfma_f32_16x16x32_bf16 v[52:55], a[232:235], v[220:223], v[52:55]\n"
    "buffer_load_dwordx4 v[192:195], v0, s[12:15], s71 offen offset:2048 sc1\n"
    "v_mfma_f32_16x16x32_bf16 v[56:59], a[224:227], a[136:139], v[56:59]\n"
    "ds_read_b128 v[252:255], v4 offset:47104\n"
    "v_mfma_f32_16x16x32_bf16 v[56:59], a[232:235], v[224:227], v[56:59]\n"
    "v_mfma_f32_16x16x32_bf16 v[60:63], a[228:231], a[112:115], v[60:63]\n"
    "v_mfma_f32_16x16x32_bf16 v[60:63], a[236:239], v[200:203], v[60:63]\n"
    "v_mfma_f32_16x16x32_bf16 v[64:67], a[228:231], a[116:119], v[64:67]\n"
    "buffer_load_dwordx4 v1, s[8:11], s70 offen offset:2048 lds\n"
    "v_mfma_f32_16x16x32_bf16 v[64:67], a[236:239], v[204:207], v[64:67]\n"
    "s_add_u32 s70, s70, 8192\n"
    "ds_read_b128 v[200:203], v10 offset:8192\n"
    "v_mfma_f32_16x16x32_bf16 v[68:71], a[228:231], a[120:123], v[68:71]\n"
    "v_mfma_f32_16x16x32_bf16 v[68:71], a[236:239], v[208:211], v[68:71]\n"
    "ds_read_b128 v[204:207], v10 offset:9216\n"
    "v_mfma_f32_16x16x32_bf16 v[72:75], a[228:231], a[124:127], v[72:75]\n"
    "v_mfma_f32_16x16x32_bf16 v[72:75], a[236:239], v[212:215], v[72:75]\n"
    "ds_read_b128 v[208:211], v10 offset:10240\n"
    "v_mfma_f32_16x16x32_bf16 v[76:79], a[228:231], a[128:131], v[76:79]\n"
    "v_mfma_f32_16x16x32_bf16 v[76:79], a[236:239], v[216:219], v[76:79]\n"
    "buffer_load_dwordx4 v1, s[8:11], s70 offen offset:3072 lds\n"
    "v_mfma_f32_16x16x32_bf16 v[80:83], a[228:231], a[132:135], v[80:83]\n"
    "ds_read_b128 v[212:215], v10 offset:11264\n"
    "v_mfma_f32_16x16x32_bf16 v[80:83], a[236:239], v[220:223], v[80:83]\n"
    "ds_read_b128 v[216:219], v10 offset:12288\n"
    "v_mfma_f32_16x16x32_bf16 v[84:87], a[228:231], a[136:139], v[84:87]\n"
    "v_mfma_f32_16x16x32_bf16 v[84:87], a[236:239], v[224:227], v[84:87]\n"
    "s_waitcnt vmcnt(25) lgkmcnt(6)\n"
    "buffer_load_dwordx2 v[12:13], v6, s[12:15], s71 offen offset:3072 sc1\n"
    "v_mfma_f32_16x16x32_bf16 v[32:35], a[240:243], a[140:143], v[32:35]\n"
    "s_barrier\n"
    "ds_read_b128 v[220:223], v10 offset:13312\n"
    "v_mfma_f32_16x16x32_bf16 v[32:35], a[248:251], v[228:231], v[32:35]\n"
    "s_add_u32 s70, s55, 331776\n"
    "s_add_u32 m0, s60, 28672\n"
    "ds_read_b64_tr_b16 a[224:225], v2 offset:0\n"
    "v_mfma_f32_16x16x32_bf16 v[36:39], a[240:243], a[144:147], v[36:39]\n"
    "buffer_load_dwordx4 v1, s[8:11], s70 offen offset:0 lds\n"
    "v_mfma_f32_16x16x32_bf16 v[36:39], a[248:251], v[232:235], v[36:39]\n"
    "s_add_u32 s70, s70, 8192\n"
    "ds_read_b64_tr_b16 a[226:227], v2 offset:2048\n"
    "v_mfma_f32_16x16x32_bf16 v[40:43], a[240:243], a[148:151], v[40:43]\n"
    "s_sub_u32 s74, s20, 3\n"
    "ds_read_b64_tr_b16 a[228:229], v2 offset:40960\n"
    "v_mfma_f32_16x16x32_bf16 v[40:43], a[248:251], v[236:239], v[40:43]\n"
    "s_and_b32 s74, s74, 7\n"
    "ds_read_b64_tr_b16 a[230:231], v2 offset:43008\n"
    "v_mfma_f32_16x16x32_bf16 v[44:47], a[240:243], a[152:155], v[44:47]\n"
    "s_lshl_b32 s74, s74, 14\n"
    "s_add_u32 s71, s41, s74\n"
    "ds_read_b64_tr_b16 a[232:233], v3 offset:0\n"
    "v_mfma_f32_16x16x32_bf16 v[44:47], a[248:251], v[240:243], v[44:47]\n"
    "buffer_load_dwordx4 v[176:179], v0, s[12:15], s71 offen offset:0 sc1\n"
    "v_mfma_f32_16x16x32_bf16 v[48:51], a[240:243], a[156:159], v[48:51]\n"
    "ds_read_b64_tr_b16 a[234:235], v3 offset:2048\n"
    "v_mfma_f32_16x16x32_bf16 v[48:51], a[248:251], v[244:247], v[48:51]\n"
    "buffer_load_dwordx4 v1, s[8:11], s70 offen offset:1024 lds\n"
    "v_mfma_f32_16x16x32_bf16 v[52:55], a[240:243], a[160:163], v[52:55]\n"
    "s_add_u32 s70, s70, 8192\n"
    "ds_read_b64_tr_b16 a[236:237], v3 offset:40960\n"
    "v_mfma_f32_16x16x32_bf16 v[52:55], a[248:251], v[248:251], v[52:55]\n"
    "ds_read_b64_tr_b16 a[238:239], v3 offset:43008\n"
    "v_mfma_f32_16x16x32_bf16 v[56:59], a[240:243], a[164:167], v[56:59]\n"
    "s_waitcnt lgkmcnt(14)\n"
    "ds_read_b128 v[224:227], v10 offset:14336\n"
    "v_mfma_f32_16x16x32_bf16 v[56:59], a[248:251], v[252:255], v[56:59]\n"
    "v_mfma_f32_16x16x32_bf16 v[60:63], a[244:247], a[140:143], v[60:63]\n"
    "v_mfma_f32_16x16x32_bf16 v[60:63], a[252:255], v[228:231], v[60:63]\n"
    "v_mfma_f32_16x16x32_bf16 v[64:67], a[244:247], a[144:147], v[64:67]\n"
    "buffer_load_dwordx4 v1, s[8:11], s70 offen offset:2048 lds\n"
    "v_mfma_f32_16x16x32_bf16 v[64:67], a[252:255], v[232:235], v[64:67]\n"
    "s_add_u32 s70, s70, 8192\n"
    "ds_read_b128 v[228:231], v10 offset:49152\n"
    "v_mfma_f32_16x16x32_bf16 v[68:71], a[244:247], a[148:151], v[68:71]\n"
    "v_mfma_f32_16x16x32_bf16 v[68:71], a[252:255], v[236:239], v[68:71]\n"
    "ds_read_b128 v[232:235], v10 offset:50176\n"
    "v_mfma_f32_16x16x32_bf16 v[72:75], a[244:247], a[152:155], v[72:75]\n"
    "v_mfma_f32_16x16x32_bf16 v[72:75], a[252:255], v[240:243], v[72:75]\n"
    "ds_read_b128 v[236:239], v10 offset:51200\n"
    "v_mfma_f32_16x16x32_bf16 v[76:79], a[244:247], a[156:159], v[76:79]\n"
    "v_mfma_f32_16x16x32_bf16 v[76:79], a[252:255], v[244:247], v[76:79]\n"
    "s_waitcnt vmcnt(15)\n"
    "buffer_load_dwordx4 v1, s[8:11], s70 offen offset:3072 lds\n"
    "v_mfma_f32_16x16x32_bf16 v[144:147], v[22:25], v[184:187], v[144:147]\n"
    "v_mfma_f32_16x16x32_bf16 v[148:151], v[20:23], v[184:187], v[148:151]\n"
    "v_mfma_f32_16x16x32_bf16 v[80:83], a[244:247], a[160:163], v[80:83]\n"
    "ds_read_b128 v[240:243], v10 offset:52224\n"
    "v_mfma_f32_16x16x32_bf16 v[80:83], a[252:255], v[248:251], v[80:83]\n"
    "buffer_load_dwordx4 v[180:183], v0, s[12:15], s71 offen offset:1024 sc1\n"
    "v_mfma_f32_16x16x32_bf16 v[84:87], a[244:247], a[164:167], v[84:87]\n"
    "ds_read_b128 v[244:247], v10 offset:53248\n"
    "v_mfma_f32_16x16x32_bf16 v[84:87], a[252:255], v[252:255], v[84:87]\n"
    "s_waitcnt vmcnt(25) lgkmcnt(6)\n"
    "ds_read_b128 v[248:251], v10 offset:54272\n"
    "v_mfma_f32_16x16x32_bf16 v[32:35], a[224:227], a[168:171], v[32:35]\n"
    "s_barrier\n"
    "v_mfma_f32_16x16x32_bf16 v[32:35], a[232:235], v[200:203], v[32:35]\n"
    "ds_read_b64_tr_b16 a[240:241], v2 offset:4096\n"
    "v_mfma_f32_16x16x32_bf16 v[36:39], a[224:227], a[172:175], v[36:39]\n"
    "ds_read_b64_tr_b16 a[242:243], v2 offset:6144\n"
    "v_mfma_f32_16x16x32_bf16 v[36:39], a[232:235], v[204:207], v[36:39]\n"
    "ds_read_b64_tr_b16 a[244:245], v2 offset:45056\n"
    "v_mfma_f32_16x16x32_bf16 v[40:43], a[224:227], a[176:179], v[40:43]\n"
    "ds_read_b64_tr_b16 a[246:247], v2 offset:47104\n"
    "v_mfma_f32_16x16x32_bf16 v[40:43], a[232:235], v[208:211], v[40:43]\n"
    "ds_read_b64_tr_b16 a[248:249], v3 offset:4096\n"
    "v_mfma_f32_16x16x32_bf16 v[44:47], a[224:227], a[180:183], v[44:47]\n"
    "ds_read_b64_tr_b16 a[250:251], v3 offset:6144\n"
    "v_mfma_f32_16x16x32_bf16 v[44:47], a[232:235], v[212:215], v[44:47]\n"
    "ds_read_b64_tr_b16 a[252:253], v3 offset:45056\n"
    "v_mfma_f32_16x16x32_bf16 v[48:51], a[224:227], a[184:187], v[48:51]\n"
    "ds_read_b64_tr_b16 a[254:255], v3 offset:47104\n"
    "v_mfma_f32_16x16x32_bf16 v[48:51], a[232:235], v[216:219], v[48:51]\n"
    "s_waitcnt vmcnt(13)\n"
    "ds_read_b128 v[252:255], v10 offset:55296\n"
    "v_mfma_f32_16x16x32_bf16 v[152:155], v[22:25], v[188:191], v[152:155]\n"
    "v_mfma_f32_16x16x32_bf16 v[156:159], v[20:23], v[188:191], v[156:159]\n"
    "v_mfma_f32_16x16x32_bf16 v[52:55], a[224:227], a[188:191], v[52:55]\n"
    "v_mfma_f32_16x16x32_bf16 v[52:55], a[232:235], v[220:223], v[52:55]\n"
    "v_mfma_f32_16x16x32_bf16 v[56:59], a[224:227], a[192:195], v[56:59]\n"
    "s_waitcnt lgkmcnt(15)\n"
    "v_mfma_f32_16x16x32_bf16 v[56:59], a[232:235], v[224:227], v[56:59]\n"
    "buffer_load_dwordx4 v[184:187], v0, s[12:15], s71 offen offset:2048 sc1\n"
    "v_mfma_f32_16x16x32_bf16 v[60:63], a[228:231], a[168:171], v[60:63]\n"
    "v_mfma_f32_16x16x32_bf16 v[60:63], a[236:239], v[200:203], v[60:63]\n"
    "v_mfma_f32_16x16x32_bf16 v[64:67], a[228:231], a[172:175], v[64:67]\n"
    "v_mfma_f32_16x16x32_bf16 v[64:67], a[236:239], v[204:207], v[64:67]\n"
    "ds_read_b128 v[200:203], v4 offset:32768\n"
    "v_mfma_f32_16x16x32_bf16 v[68:71], a[228:231], a[176:179], v[68:71]\n"
    "v_mfma_f32_16x16x32_bf16 v[68:71], a[236:239], v[208:211], v[68:71]\n"
    "ds_read_b128 v[204:207], v4 offset:33792\n"
    "v_mfma_f32_16x16x32_bf16 v[72:75], a[228:231], a[180:183], v[72:75]\n"
    "v_mfma_f32_16x16x32_bf16 v[72:75], a[236:239], v[212:215], v[72:75]\n"
    "ds_read_b128 v[208:211], v4 offset:34816\n"
    "v_mfma_f32_16x16x32_bf16 v[76:79], a[228:231], a[184:187], v[76:79]\n"
    "v_mfma_f32_16x16x32_bf16 v[76:79], a[236:239], v[216:219], v[76:79]\n"
    "ds_read_b128 v[212:215], v4 offset:35840\n"
    "v_mfma_f32_16x16x32_bf16 v[80:83], a[228:231], a[188:191], v[80:83]\n"
    "v_mfma_f32_16x16x32_bf16 v[80:83], a[236:239], v[220:223], v[80:83]\n"
    "ds_read_b128 v[216:219], v4 offset:36864\n"
    "v_mfma_f32_16x16x32_bf16 v[84:87], a[228:231], a[192:195], v[84:87]\n"
    "v_mfma_f32_16x16x32_bf16 v[84:87], a[236:239], v[224:227], v[84:87]\n"
    "s_waitcnt vmcnt(15) lgkmcnt(6)\n"
    "ds_read_b128 v[220:223], v4 offset:37888\n"
    "v_mfma_f32_16x16x32_bf16 v[32:35], a[240:243], a[196:199], v[32:35]\n"
    "s_barrier\n"
    "v_mfma_f32_16x16x32_bf16 v[32:35], a[248:251], v[228:231], v[32:35]\n"
    "buffer_store_dwordx2 v[30:31], v5, s[12:15], s22 offen offset:128 sc1\n"
    "v_mfma_f32_16x16x32_bf16 v[36:39], a[240:243], a[200:203], v[36:39]\n"
    "ds_read_b64_tr_b16 a[224:225], v2 offset:8192\n"
    "v_mfma_f32_16x16x32_bf16 v[36:39], a[248:251], v[232:235], v[36:39]\n"
    "buffer_load_dwordx2 v[14:15], v6, s[12:15], s71 offen offset:3072 sc1\n"
    "v_cvt_pk_bf16_f32 v32, v32, v33\n"
    "v_mfma_f32_16x16x32_bf16 v[40:43], a[240:243], a[204:207], v[40:43]\n"
    "s_add_u32 s74, s20, 6\n"
    "ds_read_b64_tr_b16 a[226:227], v2 offset:10240\n"
    "v_cvt_pk_bf16_f32 v33, v34, v35\n"
    "v_mfma_f32_16x16x32_bf16 v[40:43], a[248:251], v[236:239], v[40:43]\n"
    "s_and_b32 s74, s74, 7\n"
    "ds_read_b64_tr_b16 a[228:229], v2 offset:49152\n"
    "v_cvt_pk_bf16_f32 v34, v36, v37\n"
    "v_mfma_f32_16x16x32_bf16 v[44:47], a[240:243], a[208:211], v[44:47]\n"
    "s_lshl_b32 s74, s74, 17\n"
    "ds_read_b64_tr_b16 a[230:231], v2 offset:51200\n"
    "v_cvt_pk_bf16_f32 v35, v38, v39\n"
    "v_mfma_f32_16x16x32_bf16 v[44:47], a[248:251], v[240:243], v[44:47]\n"
    "s_add_u32 s83, s40, s74\n"
    "ds_read_b64_tr_b16 a[232:233], v3 offset:8192\n"
    "v_cvt_pk_bf16_f32 v36, v40, v41\n"
    "v_mfma_f32_16x16x32_bf16 v[48:51], a[240:243], a[212:215], v[48:51]\n"
    "buffer_store_dwordx4 v[32:35], v0, s[12:15], s83 offen offset:0 sc1\n"
    "v_cvt_pk_bf16_f32 v37, v42, v43\n"
    "v_mfma_f32_16x16x32_bf16 v[48:51], a[248:251], v[244:247], v[48:51]\n"
    "ds_read_b64_tr_b16 a[234:235], v3 offset:10240\n"
    "v_cvt_pk_bf16_f32 v38, v44, v45\n"
    "v_mfma_f32_16x16x32_bf16 v[52:55], a[240:243], a[216:219], v[52:55]\n"
    "ds_read_b64_tr_b16 a[236:237], v3 offset:49152\n"
    "v_cvt_pk_bf16_f32 v39, v46, v47\n"
    "v_mfma_f32_16x16x32_bf16 v[52:55], a[248:251], v[248:251], v[52:55]\n"
    "buffer_store_dwordx4 v[36:39], v0, s[12:15], s83 offen offset:1024 sc1\n"
    "v_cvt_pk_bf16_f32 v40, v48, v49\n"
    "v_mfma_f32_16x16x32_bf16 v[56:59], a[240:243], a[220:223], v[56:59]\n"
    "s_waitcnt lgkmcnt(13)\n"
    "ds_read_b64_tr_b16 a[238:239], v3 offset:51200\n"
    "v_cvt_pk_bf16_f32 v41, v50, v51\n"
    "v_mov_b32 v46, 0\n"
    "v_mfma_f32_16x16x32_bf16 v[56:59], a[248:251], v[252:255], v[56:59]\n"
    "ds_read_b128 v[224:227], v4 offset:38912\n"
    "v_cvt_pk_bf16_f32 v42, v52, v53\n"
    "v_mfma_f32_16x16x32_bf16 v[60:63], a[244:247], a[196:199], v[60:63]\n"
    "v_cvt_pk_bf16_f32 v43, v54, v55\n"
    "v_mov_b32 v47, 0\n"
    "v_mfma_f32_16x16x32_bf16 v[60:63], a[252:255], v[228:231], v[60:63]\n"
    "s_waitcnt vmcnt(11)\n"
    "buffer_store_dwordx4 v[40:43], v0, s[12:15], s83 offen offset:2048 sc1\n"
    "v_cvt_pk_bf16_f32 v44, v56, v57\n"
    "v_mfma_f32_16x16x32_bf16 v[160:163], v[22:25], v[192:195], v[160:163]\n"
    "v_mfma_f32_16x16x32_bf16 v[164:167], v[20:23], v[192:195], v[164:167]\n"
    "v_mfma_f32_16x16x32_bf16 v[64:67], a[244:247], a[200:203], v[64:67]\n"
    "ds_read_b128 v[228:231], v10 offset:0\n"
    "v_cvt_pk_bf16_f32 v45, v58, v59\n"
    "v_mfma_f32_16x16x32_bf16 v[64:67], a[252:255], v[232:235], v[64:67]\n"
    "buffer_store_dwordx4 v[44:47], v0, s[12:15], s83 offen offset:3072 sc1\n"
    "v_cvt_pk_bf16_f32 v60, v60, v61\n"
    "v_mfma_f32_16x16x32_bf16 v[68:71], a[244:247], a[204:207], v[68:71]\n"
    "s_add_u32 s74, s20, 7\n"
    "ds_read_b128 v[232:235], v10 offset:1024\n"
    "v_cvt_pk_bf16_f32 v61, v62, v63\n"
    "v_mfma_f32_16x16x32_bf16 v[68:71], a[252:255], v[236:239], v[68:71]\n"
    "s_and_b32 s74, s74, 7\n"
    "ds_read_b128 v[236:239], v10 offset:2048\n"
    "v_cvt_pk_bf16_f32 v62, v64, v65\n"
    "v_mfma_f32_16x16x32_bf16 v[72:75], a[244:247], a[208:211], v[72:75]\n"
    "s_lshl_b32 s74, s74, 17\n"
    "v_cvt_pk_bf16_f32 v63, v66, v67\n"
    "v_mfma_f32_16x16x32_bf16 v[72:75], a[252:255], v[240:243], v[72:75]\n"
    "s_add_u32 s83, s40, s74\n"
    "ds_read_b128 v[240:243], v10 offset:3072\n"
    "v_cvt_pk_bf16_f32 v64, v68, v69\n"
    "v_mfma_f32_16x16x32_bf16 v[76:79], a[244:247], a[212:215], v[76:79]\n"
    "buffer_store_dwordx4 v[60:63], v0, s[12:15], s83 offen offset:0 sc1\n"
    "v_cvt_pk_bf16_f32 v65, v70, v71\n"
    "v_mfma_f32_16x16x32_bf16 v[76:79], a[252:255], v[244:247], v[76:79]\n"
    "s_waitcnt vmcnt(12)\n"
    "ds_read_b128 v[244:247], v10 offset:4096\n"
    "v_cvt_pk_bf16_f32 v66, v72, v73\n"
    "v_mfma_f32_16x16x32_bf16 v[144:147], v[22:25], v[176:179], v[144:147]\n"
    "v_mfma_f32_16x16x32_bf16 v[148:151], v[20:23], v[176:179], v[148:151]\n"
    "v_mfma_f32_16x16x32_bf16 v[80:83], a[244:247], a[216:219], v[80:83]\n"
    "v_cvt_pk_bf16_f32 v67, v74, v75\n"
    "v_mfma_f32_16x16x32_bf16 v[80:83], a[252:255], v[248:251], v[80:83]\n"
    "buffer_store_dwordx4 v[64:67], v0, s[12:15], s83 offen offset:1024 sc1\n"
    "v_cvt_pk_bf16_f32 v68, v76, v77\n"
    "v_mfma_f32_16x16x32_bf16 v[84:87], a[244:247], a[220:223], v[84:87]\n"
    "ds_read_b128 v[248:251], v10 offset:5120\n"
    "v_cvt_pk_bf16_f32 v69, v78, v79\n"
    "v_mov_b32 v74, 0\n"
    "v_mfma_f32_16x16x32_bf16 v[84:87], a[252:255], v[252:255], v[84:87]\n"
    "s_waitcnt lgkmcnt(13)\n"
    "s_waitcnt vmcnt(21)\n"
    "ds_read_b128 v[252:255], v10 offset:6144\n"
    "v_cvt_pk_bf16_f32 v70, v80, v81\n"
    "v_mfma_f32_16x16x32_bf16 v[144:147], a[224:227], a[0:3], v[144:147]\n"
    "s_waitcnt lgkmcnt(12)\n"
    "s_barrier\n"
    "v_cvt_pk_bf16_f32 v71, v82, v83\n"
    "v_mov_b32 v75, 0\n"
    "v_mfma_f32_16x16x32_bf16 v[144:147], a[228:231], v[200:203], v[144:147]\n"
    "buffer_store_dwordx4 v[68:71], v0, s[12:15], s83 offen offset:2048 sc1\n"
    "v_cvt_pk_bf16_f32 v72, v84, v85\n"
    "v_mfma_f32_16x16x32_bf16 v[148:151], a[224:227], a[4:7], v[148:151]\n"
    "ds_read_b128 v[200:203], v10 offset:40960\n"
    "v_cvt_pk_bf16_f32 v73, v86, v87\n"
    "v_mfma_f32_16x16x32_bf16 v[148:151], a[228:231], v[204:207], v[148:151]\n"
    "buffer_store_dwordx4 v[72:75], v0, s[12:15], s83 offen offset:3072 sc1\n"
    "v_mfma_f32_16x16x32_bf16 v[152:155], a[224:227], a[8:11], v[152:155]\n"
    "ds_read_b64_tr_b16 a[240:241], v2 offset:12288\n"
    "ds_read_b64_tr_b16 a[242:243], v2 offset:14336\n"
    "v_mfma_f32_16x16x32_bf16 v[152:155], a[228:231], v[208:211], v[152:155]\n"
    "ds_read_b128 v[204:207], v10 offset:41984\n"
    "v_mfma_f32_16x16x32_bf16 v[156:159], a[224:227], a[12:15], v[156:159]\n"
    "ds_read_b64_tr_b16 a[244:245], v2 offset:53248\n"
    "ds_read_b64_tr_b16 a[246:247], v2 offset:55296\n"
    "v_mfma_f32_16x16x32_bf16 v[156:159], a[228:231], v[212:215], v[156:159]\n"
    "ds_read_b128 v[208:211], v10 offset:43008\n"
    "v_mfma_f32_16x16x32_bf16 v[160:163], a[224:227], a[16:19], v[160:163]\n"
    "ds_read_b64_tr_b16 a[248:249], v3 offset:12288\n"
    "ds_read_b64_tr_b16 a[250:251], v3 offset:14336\n"
    "v_mfma_f32_16x16x32_bf16 v[160:163], a[228:231], v[216:219], v[160:163]\n"
    "ds_read_b128 v[212:215], v10 offset:44032\n"
    "v_mfma_f32_16x16x32_bf16 v[164:167], a[224:227], a[20:23], v[164:167]\n"
    "ds_read_b64_tr_b16 a[252:253], v3 offset:53248\n"
    "ds_read_b64_tr_b16 a[254:255], v3 offset:55296\n"
    "v_mfma_f32_16x16x32_bf16 v[164:167], a[228:231], v[220:223], v[164:167]\n"
    "ds_read_b128 v[216:219], v10 offset:45056\n"
    "v_mfma_f32_16x16x32_bf16 v[168:171], a[224:227], a[24:27], v[168:171]\n"
    "s_waitcnt lgkmcnt(15)\n"
    "ds_read_b128 v[220:223], v10 offset:46080\n"
    "v_mfma_f32_16x16x32_bf16 v[168:171], a[228:231], v[224:227], v[168:171]\n"
    "s_waitcnt vmcnt(11)\n"
    "ds_read_b128 v[224:227], v10 offset:47104\n"
    "v_mfma_f32_16x16x32_bf16 v[152:155], v[22:25], v[180:183], v[152:155]\n"
    "v_mfma_f32_16x16x32_bf16 v[156:159], v[20:23], v[180:183], v[156:159]\n"
    "v_mfma_f32_16x16x32_bf16 v[144:147], a[232:235], a[28:31], v[144:147]\n"
    "s_waitcnt lgkmcnt(15)\n"
    "v_mfma_f32_16x16x32_bf16 v[144:147], a[236:239], v[228:231], v[144:147]\n"
    "ds_read_b128 v[228:231], v18 offset:8192\n"
    "v_mfma_f32_16x16x32_bf16 v[148:151], a[232:235], a[32:35], v[148:151]\n"
    "s_waitcnt lgkmcnt(15)\n"
    "v_mfma_f32_16x16x32_bf16 v[148:151], a[236:239], v[232:235], v[148:151]\n"
    "ds_read_b128 v[232:235], v18 offset:9216\n"
    "v_mfma_f32_16x16x32_bf16 v[152:155], a[232:235], a[36:39], v[152:155]\n"
    "s_waitcnt lgkmcnt(15)\n"
    "v_mfma_f32_16x16x32_bf16 v[152:155], a[236:239], v[236:239], v[152:155]\n"
    "ds_read_b128 v[236:239], v18 offset:10240\n"
    "v_mfma_f32_16x16x32_bf16 v[156:159], a[232:235], a[40:43], v[156:159]\n"
    "s_waitcnt lgkmcnt(15)\n"
    "v_mfma_f32_16x16x32_bf16 v[156:159], a[236:239], v[240:243], v[156:159]\n"
    "ds_read_b128 v[240:243], v18 offset:11264\n"
    "v_mfma_f32_16x16x32_bf16 v[160:163], a[232:235], a[44:47], v[160:163]\n"
    "s_waitcnt lgkmcnt(15)\n"
    "v_mfma_f32_16x16x32_bf16 v[160:163], a[236:239], v[244:247], v[160:163]\n"
    "ds_read_b128 v[244:247], v18 offset:12288\n"
    "v_mfma_f32_16x16x32_bf16 v[164:167], a[232:235], a[48:51], v[164:167]\n"
    "s_waitcnt lgkmcnt(15)\n"
    "v_mfma_f32_16x16x32_bf16 v[164:167], a[236:239], v[248:251], v[164:167]\n"
    "ds_read_b128 v[248:251], v18 offset:13312\n"
    "v_mfma_f32_16x16x32_bf16 v[168:171], a[232:235], a[52:55], v[168:171]\n"
    "s_waitcnt lgkmcnt(15)\n"
    "v_mfma_f32_16x16x32_bf16 v[168:171], a[236:239], v[252:255], v[168:171]\n"
    "s_waitcnt lgkmcnt(15)\n"
    "s_waitcnt vmcnt(18)\n"
    "ds_read_b128 v[252:255], v18 offset:14336\n"
    "v_mfma_f32_16x16x32_bf16 v[144:147], a[240:243], a[56:59], v[144:147]\n"
    "s_waitcnt lgkmcnt(15)\n"
    "s_barrier\n"
    "v_mfma_f32_16x16x32_bf16 v[144:147], a[244:247], v[200:203], v[144:147]\n"
    "ds_read_b64_tr_b16 a[224:225], v2 offset:16384\n"
    "ds_read_b64_tr_b16 a[226:227], v2 offset:18432\n"
    "v_mfma_f32_16x16x32_bf16 v[148:151], a[240:243], a[60:63], v[148:151]\n"
    "ds_read_b128 v[200:203], v4 offset:0\n"
    "v_mfma_f32_16x16x32_bf16 v[148:151], a[244:247], v[204:207], v[148:151]\n"
    "ds_read_b64_tr_b16 a[228:229], v2 offset:57344\n"
    "ds_read_b64_tr_b16 a[230:231], v2 offset:59392\n"
    "v_mfma_f32_16x16x32_bf16 v[152:155], a[240:243], a[64:67], v[152:155]\n"
    "s_waitcnt lgkmcnt(15)\n"
    "ds_read_b128 v[204:207], v4 offset:1024\n"
    "v_mfma_f32_16x16x32_bf16 v[152:155], a[244:247], v[208:211], v[152:155]\n"
    "ds_read_b64_tr_b16 a[232:233], v3 offset:16384\n"
    "ds_read_b64_tr_b16 a[234:235], v3 offset:18432\n"
    "v_mfma_f32_16x16x32_bf16 v[156:159], a[240:243], a[68:71], v[156:159]\n"
    "s_waitcnt lgkmcnt(15)\n"
    "ds_read_b128 v[208:211], v4 offset:2048\n"
    "v_mfma_f32_16x16x32_bf16 v[156:159], a[244:247], v[212:215], v[156:159]\n"
    "ds_read_b64_tr_b16 a[236:237], v3 offset:57344\n"
    "ds_read_b64_tr_b16 a[238:239], v3 offset:59392\n"
    "v_mfma_f32_16x16x32_bf16 v[160:163], a[240:243], a[72:75], v[160:163]\n"
    "s_waitcnt lgkmcnt(15)\n"
    "ds_read_b128 v[212:215], v4 offset:3072\n"
    "v_mfma_f32_16x16x32_bf16 v[160:163], a[244:247], v[216:219], v[160:163]\n"
    "ds_read_b128 v[216:219], v4 offset:4096\n"
    "v_mfma_f32_16x16x32_bf16 v[164:167], a[240:243], a[76:79], v[164:167]\n"
    "s_waitcnt lgkmcnt(15)\n"
    "v_mfma_f32_16x16x32_bf16 v[164:167], a[244:247], v[220:223], v[164:167]\n"
    "ds_read_b128 v[220:223], v4 offset:5120\n"
    "v_mfma_f32_16x16x32_bf16 v[168:171], a[240:243], a[80:83], v[168:171]\n"
    "s_waitcnt lgkmcnt(15)\n"
    "v_mfma_f32_16x16x32_bf16 v[168:171], a[244:247], v[224:227], v[168:171]\n"
    "ds_read_b128 v[224:227], v4 offset:6144\n"
    "v_mfma_f32_16x16x32_bf16 v[144:147], a[248:251], a[84:87], v[144:147]\n"
    "s_waitcnt lgkmcnt(15)\n"
    "v_mfma_f32_16x16x32_bf16 v[144:147], a[252:255], v[228:231], v[144:147]\n"
    "buffer_load_dwordx2 v[198:199], v19, s[12:15], 0 offen offset:128 sc1\n"
    "v_mfma_f32_16x16x32_bf16 v[148:151], a[248:251], a[88:91], v[148:151]\n"
    "s_waitcnt lgkmcnt(15)\n"
    "ds_read_b128 v[228:231], v4 offset:40960\n"
    "v_mfma_f32_16x16x32_bf16 v[148:151], a[252:255], v[232:235], v[148:151]\n"
    "ds_read_b128 v[232:235], v4 offset:41984\n"
    "v_mfma_f32_16x16x32_bf16 v[152:155], a[248:251], a[92:95], v[152:155]\n"
    "s_waitcnt lgkmcnt(15)\n"
    "v_mfma_f32_16x16x32_bf16 v[152:155], a[252:255], v[236:239], v[152:155]\n"
    "s_waitcnt vmcnt(9)\n"
    "ds_read_b128 v[236:239], v4 offset:43008\n"
    "v_mfma_f32_16x16x32_bf16 v[160:163], v[22:25], v[184:187], v[160:163]\n"
    "v_mfma_f32_16x16x32_bf16 v[164:167], v[20:23], v[184:187], v[164:167]\n"
    "v_mfma_f32_16x16x32_bf16 v[168:171], v[26:29], v[12:15], v[168:171]\n"
    "v_mfma_f32_16x16x32_bf16 v[156:159], a[248:251], a[96:99], v[156:159]\n"
    "s_waitcnt lgkmcnt(15)\n"
    "v_mfma_f32_16x16x32_bf16 v[156:159], a[252:255], v[240:243], v[156:159]\n"
    "ds_read_b128 v[240:243], v4 offset:44032\n"
    "v_mfma_f32_16x16x32_bf16 v[160:163], a[248:251], a[100:103], v[160:163]\n"
    "s_waitcnt lgkmcnt(15)\n"
    "v_mfma_f32_16x16x32_bf16 v[160:163], a[252:255], v[244:247], v[160:163]\n"
    "ds_read_b128 v[244:247], v4 offset:45056\n"
    "v_mfma_f32_16x16x32_bf16 v[164:167], a[248:251], a[104:107], v[164:167]\n"
    "s_waitcnt lgkmcnt(15)\n"
    "v_mfma_f32_16x16x32_bf16 v[164:167], a[252:255], v[248:251], v[164:167]\n"
    "ds_read_b128 v[248:251], v4 offset:46080\n"
    "v_mfma_f32_16x16x32_bf16 v[168:171], a[248:251], a[108:111], v[168:171]\n"
    "s_waitcnt lgkmcnt(15)\n"
    "v_mfma_f32_16x16x32_bf16 v[168:171], a[252:255], v[252:255], v[168:171]\n"
    "s_waitcnt lgkmcnt(15)\n"
    "s_waitcnt vmcnt(13)\n"
    "ds_read_b128 v[252:255], v4 offset:47104\n"
    "v_mfma_f32_16x16x32_bf16 v[144:147], a[224:227], a[112:115], v[144:147]\n"
    "s_waitcnt lgkmcnt(15)\n"
    "s_barrier\n"
    "v_mfma_f32_16x16x32_bf16 v[144:147], a[228:231], v[200:203], v[144:147]\n"
    "ds_read_b64_tr_b16 a[240:241], v2 offset:20480\n"
    "ds_read_b64_tr_b16 a[242:243], v2 offset:22528\n"
    "v_mfma_f32_16x16x32_bf16 v[148:151], a[224:227], a[116:119], v[148:151]\n"
    "s_waitcnt lgkmcnt(15)\n"
    "ds_read_b128 v[200:203], v10 offset:8192\n"
    "v_mfma_f32_16x16x32_bf16 v[148:151], a[228:231], v[204:207], v[148:151]\n"
    "ds_read_b64_tr_b16 a[244:245], v2 offset:61440\n"
    "ds_read_b64_tr_b16 a[246:247], v2 offset:63488\n"
    "v_mfma_f32_16x16x32_bf16 v[152:155], a[224:227], a[120:123], v[152:155]\n"
    "s_waitcnt lgkmcnt(15)\n"
    "ds_read_b128 v[204:207], v10 offset:9216\n"
    "v_mfma_f32_16x16x32_bf16 v[152:155], a[228:231], v[208:211], v[152:155]\n"
    "ds_read_b64_tr_b16 a[248:249], v3 offset:20480\n"
    "ds_read_b64_tr_b16 a[250:251], v3 offset:22528\n"
    "v_mfma_f32_16x16x32_bf16 v[156:159], a[224:227], a[124:127], v[156:159]\n"
    "s_waitcnt lgkmcnt(15)\n"
    "ds_read_b128 v[208:211], v10 offset:10240\n"
    "v_mfma_f32_16x16x32_bf16 v[156:159], a[228:231], v[212:215], v[156:159]\n"
    "ds_read_b64_tr_b16 a[252:253], v3 offset:61440\n"
    "ds_read_b64_tr_b16 a[254:255], v3 offset:63488\n"
    "v_mfma_f32_16x16x32_bf16 v[160:163], a[224:227], a[128:131], v[160:163]\n"
    "s_waitcnt lgkmcnt(15)\n"
    "ds_read_b128 v[212:215], v10 offset:11264\n"
    "v_mfma_f32_16x16x32_bf16 v[160:163], a[228:231], v[216:219], v[160:163]\n"
    "ds_read_b128 v[216:219], v10 offset:12288\n"
    "v_mfma_f32_16x16x32_bf16 v[164:167], a[224:227], a[132:135], v[164:167]\n"
    "s_waitcnt lgkmcnt(15)\n"
    "v_mfma_f32_16x16x32_bf16 v[164:167], a[228:231], v[220:223], v[164:167]\n"
    "ds_read_b128 v[220:223], v10 offset:13312\n"
    "v_mfma_f32_16x16x32_bf16 v[168:171], a[224:227], a[136:139], v[168:171]\n"
    "s_waitcnt lgkmcnt(15)\n"
    "v_mfma_f32_16x16x32_bf16 v[168:171], a[228:231], v[224:227], v[168:171]\n"
    "ds_read_b128 v[224:227], v10 offset:14336\n"
    "v_mfma_f32_16x16x32_bf16 v[144:147], a[232:235], a[140:143], v[144:147]\n"
    "s_waitcnt lgkmcnt(15)\n"
    "v_mfma_f32_16x16x32_bf16 v[144:147], a[236:239], v[228:231], v[144:147]\n"
    "ds_read_b128 v[228:231], v10 offset:49152\n"
    "v_mfma_f32_16x16x32_bf16 v[148:151], a[232:235], a[144:147], v[148:151]\n"
    "s_waitcnt lgkmcnt(15)\n"
    "v_mfma_f32_16x16x32_bf16 v[148:151], a[236:239], v[232:235], v[148:151]\n"
    "ds_read_b128 v[232:235], v10 offset:50176\n"
    "v_mfma_f32_16x16x32_bf16 v[152:155], a[232:235], a[148:151], v[152:155]\n"
    "s_waitcnt lgkmcnt(15)\n"
    "v_mfma_f32_16x16x32_bf16 v[152:155], a[236:239], v[236:239], v[152:155]\n"
    "ds_read_b128 v[236:239], v10 offset:51200\n"
    "v_mfma_f32_16x16x32_bf16 v[156:159], a[232:235], a[152:155], v[156:159]\n"
    "s_waitcnt lgkmcnt(15)\n"
    "v_mfma_f32_16x16x32_bf16 v[156:159], a[236:239], v[240:243], v[156:159]\n"
    "ds_read_b128 v[240:243], v10 offset:52224\n"
    "v_mfma_f32_16x16x32_bf16 v[160:163], a[232:235], a[156:159], v[160:163]\n"
    "s_waitcnt lgkmcnt(15)\n"
    "v_mfma_f32_16x16x32_bf16 v[160:163], a[236:239], v[244:247], v[160:163]\n"
    "ds_read_b128 v[244:247], v10 offset:53248\n"
    "v_mfma_f32_16x16x32_bf16 v[164:167], a[232:235], a[160:163], v[164:167]\n"
    "s_waitcnt lgkmcnt(15)\n"
    "v_mfma_f32_16x16x32_bf16 v[164:167], a[236:239], v[248:251], v[164:167]\n"
    "ds_read_b128 v[248:251], v10 offset:54272\n"
    "v_mfma_f32_16x16x32_bf16 v[168:171], a[232:235], a[164:167], v[168:171]\n"
    "s_waitcnt lgkmcnt(15)\n"
    "v_mfma_f32_16x16x32_bf16 v[168:171], a[236:239], v[252:255], v[168:171]\n"
    "s_waitcnt lgkmcnt(15)\n"
    "s_waitcnt vmcnt(1)\n"
    "ds_read_b128 v[252:255], v10 offset:55296\n"
    "v_mfma_f32_16x16x32_bf16 v[144:147], a[240:243], a[168:171], v[144:147]\n"
    "s_waitcnt lgkmcnt(15)\n"
    "s_barrier\n"
    "v_mfma_f32_16x16x32_bf16 v[144:147], a[244:247], v[200:203], v[144:147]\n"
    "buffer_store_dwordx2 v[30:31], v5, s[12:15], s22 offen offset:192 sc1\n"
    "v_mfma_f32_16x16x32_bf16 v[148:151], a[240:243], a[172:175], v[148:151]\n"
    "s_waitcnt lgkmcnt(15)\n"
    "s_waitcnt vmcnt(1)\n"
    "v_cmp_ne_u64 vcc, s[68:69], v[198:199]\n"
    "v_mfma_f32_16x16x32_bf16 v[148:151], a[244:247], v[204:207], v[148:151]\n"
    "s_cbranch_vccnz LSP2_%=\n"
    "LRT2_%=:\n"
    "v_mfma_f32_16x16x32_bf16 v[152:155], a[240:243], a[176:179], v[152:155]\n"
    "s_waitcnt lgkmcnt(13)\n"
    "s_sub_u32 s74, s20, 4\n"
    "v_mfma_f32_16x16x32_bf16 v[152:155], a[244:247], v[208:211], v[152:155]\n"
    "s_and_b32 s74, s74, 7\n"
    "s_lshl_b32 s74, s74, 14\n"
    "s_add_u32 s71, s41, s74\n"
    "v_mfma_f32_16x16x32_bf16 v[156:159], a[240:243], a[180:183], v[156:159]\n"
    "s_waitcnt lgkmcnt(10)\n"
    "buffer_load_dwordx4 v[116:119], v0, s[12:15], s71 offen offset:0 sc1\n"
    "v_mfma_f32_16x16x32_bf16 v[156:159], a[244:247], v[212:215], v[156:159]\n"
    "v_mfma_f32_16x16x32_bf16 v[160:163], a[240:243], a[184:187], v[160:163]\n"
    "s_waitcnt lgkmcnt(9)\n"
    "buffer_load_dwordx4 v[120:123], v0, s[12:15], s71 offen offset:1024 sc1\n"
    "v_mfma_f32_16x16x32_bf16 v[160:163], a[244:247], v[216:219], v[160:163]\n"
    "v_mfma_f32_16x16x32_bf16 v[164:167], a[240:243], a[188:191], v[164:167]\n"
    "s_waitcnt lgkmcnt(8)\n"
    "buffer_load_dwordx4 v[124:127], v0, s[12:15], s71 offen offset:2048 sc1\n"
    "v_mfma_f32_16x16x32_bf16 v[164:167], a[244:247], v[220:223], v[164:167]\n"
    "v_mfma_f32_16x16x32_bf16 v[168:171], a[240:243], a[192:195], v[168:171]\n"
    "s_waitcnt lgkmcnt(7)\n"
    "buffer_load_dwordx2 v[140:141], v6, s[12:15], s71 offen offset:3072 sc1\n"
    "v_mfma_f32_16x16x32_bf16 v[168:171], a[244:247], v[224:227], v[168:171]\n"
    "s_sub_u32 s74, s20, 5\n"
    "v_mfma_f32_16x16x32_bf16 v[144:147], a[248:251], a[196:199], v[144:147]\n"
    "s_waitcnt lgkmcnt(6)\n"
    "s_and_b32 s74, s74, 7\n"
    "v_mfma_f32_16x16x32_bf16 v[144:147], a[252:255], v[228:231], v[144:147]\n"
    "s_lshl_b32 s74, s74, 14\n"
    "v_mfma_f32_16x16x32_bf16 v[148:151], a[248:251], a[200:203], v[148:151]\n"
    "s_waitcnt lgkmcnt(5)\n"
    "s_add_u32 s71, s41, s74\n"
    "v_mfma_f32_16x16x32_bf16 v[148:151], a[252:255], v[232:235], v[148:151]\n"
    "buffer_load_dwordx4 v[128:131], v0, s[12:15], s71 offen offset:0 sc1\n"
    "v_mfma_f32_16x16x32_bf16 v[152:155], a[248:251], a[204:207], v[152:155]\n"
    "s_waitcnt lgkmcnt(4)\n"
    "v_mfma_f32_16x16x32_bf16 v[152:155], a[252:255], v[236:239], v[152:155]\n"
    "buffer_load_dwordx4 v[132:135], v0, s[12:15], s71 offen offset:1024 sc1\n"
    "v_mfma_f32_16x16x32_bf16 v[156:159], a[248:251], a[208:211], v[156:159]\n"
    "s_waitcnt lgkmcnt(3)\n"
    "v_mfma_f32_16x16x32_bf16 v[156:159], a[252:255], v[240:243], v[156:159]\n"
    "buffer_load_dwordx4 v[136:139], v0, s[12:15], s71 offen offset:2048 sc1\n"
    "v_mfma_f32_16x16x32_bf16 v[160:163], a[248:251], a[212:215], v[160:163]\n"
    "s_waitcnt lgkmcnt(2)\n"
    "v_mfma_f32_16x16x32_bf16 v[160:163], a[252:255], v[244:247], v[160:163]\n"
    "buffer_load_dwordx2 v[142:143], v6, s[12:15], s71 offen offset:3072 sc1\n"
    "v_mfma_f32_16x16x32_bf16 v[164:167], a[248:251], a[216:219], v[164:167]\n"
    "s_waitcnt lgkmcnt(1)\n"
    "v_mfma_f32_16x16x32_bf16 v[164:167], a[252:255], v[248:251], v[164:167]\n"
    "buffer_load_dwordx2 v[198:199], v196, s[12:15], 0 offen offset:192 sc1\n"
    "v_mfma_f32_16x16x32_bf16 v[168:171], a[248:251], a[220:223], v[168:171]\n"
    "s_waitcnt lgkmcnt(0)\n"
    "v_mfma_f32_16x16x32_bf16 v[168:171], a[252:255], v[252:255], v[168:171]\n"
    "buffer_store_dwordx2 v[30:31], v5, s[12:15], s23 offen offset:256 sc1\n"
    "s_lshr_b32 s74, s47, 1\n"
    "s_sub_u32 s41, s41, s74\n"
    "s_sub_u32 s41, s41, s47\n"
    "s_sub_u32 s74, s20, 6\n"
    "s_and_b32 s74, s74, 7\n"
    "s_lshl_b32 s74, s74, 14\n"
    "s_add_u32 s71, s41, s74\n"
    "s_mov_b32 s83, s71\n"
    "s_sub_u32 s74, s20, 7\n"
    "s_and_b32 s74, s74, 7\n"
    "s_lshl_b32 s74, s74, 14\n"
    "s_add_u32 s71, s41, s74\n"
    "s_waitcnt vmcnt(1)\n"
    "v_cmp_ne_u64 vcc, s[68:69], v[198:199]\n"
    "v_add_u32 v200, s47, v4\n"
    "v_add_u32 v201, 81920, v200\n"
    "s_cbranch_vccnz LSK3_%=\n"
    "buffer_load_dwordx4 a[0:3], v0, s[12:15], s83 offen offset:0 sc1\n"
    "s_add_u32 s83, s83, 4096\n"
    "buffer_load_dwordx4 a[4:7], v0, s[12:15], s71 offen offset:0 sc1\n"
    "s_add_u32 s71, s71, 4096\n"
    "buffer_load_dwordx4 a[8:11], v0, s[12:15], s83 offen offset:0 sc1\n"
    "s_add_u32 s83, s83, 4096\n"
    "buffer_load_dwordx4 a[12:15], v0, s[12:15], s71 offen offset:0 sc1\n"
    "s_add_u32 s71, s71, 4096\n"
    "buffer_load_dwordx4 a[16:19], v0, s[12:15], s83 offen offset:0 sc1\n"
    "s_add_u32 s83, s83, 4096\n"
    "buffer_load_dwordx4 a[20:23], v0, s[12:15], s71 offen offset:0 sc1\n"
    "s_add_u32 s71, s71, 4096\n"
    "buffer_load_dwordx4 a[24:27], v0, s[12:15], s83 offen offset:0 sc1\n"
    "buffer_load_dwordx4 a[28:31], v0, s[12:15], s71 offen offset:0 sc1\n"
    "LSK3_%=:\n"
    "v_mfma_f32_16x16x32_bf16 v[144:147], v[22:25], v[116:119], v[144:147]\n"
    "v_mfma_f32_16x16x32_bf16 v[148:151], v[20:23], v[116:119], v[148:151]\n"
    "v_mfma_f32_16x16x32_bf16 v[152:155], v[22:25], v[120:123], v[152:155]\n"
    "v_mfma_f32_16x16x32_bf16 v[156:159], v[20:23], v[120:123], v[156:159]\n"
    "v_mfma_f32_16x16x32_bf16 v[160:163], v[22:25], v[124:127], v[160:163]\n"
    "v_mfma_f32_16x16x32_bf16 v[164:167], v[20:23], v[124:127], v[164:167]\n"
    "v_mfma_f32_16x16x32_bf16 v[144:147], v[22:25], v[128:131], v[144:147]\n"
    "v_mfma_f32_16x16x32_bf16 v[148:151], v[20:23], v[128:131], v[148:151]\n"
    "v_mfma_f32_16x16x32_bf16 v[152:155], v[22:25], v[132:135], v[152:155]\n"
    "v_mfma_f32_16x16x32_bf16 v[156:159], v[20:23], v[132:135], v[156:159]\n"
    "v_mfma_f32_16x16x32_bf16 v[160:163], v[22:25], v[136:139], v[160:163]\n"
    "v_mfma_f32_16x16x32_bf16 v[164:167], v[20:23], v[136:139], v[164:167]\n"
    "v_mfma_f32_16x16x32_bf16 v[168:171], v[26:29], v[140:143], v[168:171]\n"
    "s_nop 3\n"
    "ds_write_b128 v9, v[172:175] offset:15360\n"
    "ds_write_b128 v9, v[144:147] offset:8192\n"
    "ds_write_b128 v9, v[148:151] offset:9216\n"
    "ds_write_b128 v9, v[152:155] offset:10240\n"
    "ds_write_b128 v9, v[156:159] offset:11264\n"
    "ds_write_b128 v9, v[160:163] offset:12288\n"
    "ds_write_b128 v9, v[164:167] offset:13312\n"
    "ds_write_b128 v9, v[168:171] offset:14336\n"
    "s_waitcnt lgkmcnt(0)\n"
    "s_barrier\n"
    "ds_read_b128 v[32:35], v200 offset:8192\n"
    "ds_read_b128 v[36:39], v200 offset:49152\n"
    "ds_read_b128 v[40:43], v201 offset:8192\n"
    "ds_read_b128 v[44:47], v201 offset:49152\n"
    "ds_read_b128 v[48:51], v200 offset:9216\n"
    "ds_read_b128 v[52:55], v200 offset:50176\n"
    "ds_read_b128 v[56:59], v201 offset:9216\n"
    "ds_read_b128 v[60:63], v201 offset:50176\n"
    "s_cbranch_vccz LDN3_%=\n"
    "s_branch LSP3_%=\n"
    "LRT3_%=:\n"
    "buffer_load_dwordx4 a[0:3], v0, s[12:15], s83 offen offset:0 sc1\n"
    "s_add_u32 s83, s83, 4096\n"
    "buffer_load_dwordx4 a[4:7], v0, s[12:15], s71 offen offset:0 sc1\n"
    "s_add_u32 s71, s71, 4096\n"
    "buffer_load_dwordx4 a[8:11], v0, s[12:15], s83 offen offset:0 sc1\n"
    "s_add_u32 s83, s83, 4096\n"
    "buffer_load_dwordx4 a[12:15], v0, s[12:15], s71 offen offset:0 sc1\n"
    "s_add_u32 s71, s71, 4096\n"
    "buffer_load_dwordx4 a[16:19], v0, s[12:15], s83 offen offset:0 sc1\n"
    "s_add_u32 s83, s83, 4096\n"
    "buffer_load_dwordx4 a[20:23], v0, s[12:15], s71 offen offset:0 sc1\n"
    "s_add_u32 s71, s71, 4096\n"
    "buffer_load_dwordx4 a[24:27], v0, s[12:15], s83 offen offset:0 sc1\n"
    "buffer_load_dwordx4 a[28:31], v0, s[12:15], s71 offen offset:0 sc1\n"
    "LDN3_%=:\n"
    "s_waitcnt lgkmcnt(0)\n"
    "s_waitcnt vmcnt(7)\n"
    "v_mfma_f32_16x16x32_bf16 v[32:35], v[22:25], a[0:3], v[32:35]\n"
    "v_mfma_f32_16x16x32_bf16 v[48:51], v[20:23], a[0:3], v[48:51]\n"
    "s_waitcnt vmcnt(6)\n"
    "v_mfma_f32_16x16x32_bf16 v[32:35], v[22:25], a[4:7], v[32:35]\n"
    "v_mfma_f32_16x16x32_bf16 v[48:51], v[20:23], a[4:7], v[48:51]\n"
    "s_waitcnt vmcnt(5)\n"
    "v_mfma_f32_16x16x32_bf16 v[36:39], v[22:25], a[8:11], v[36:39]\n"
    "v_mfma_f32_16x16x32_bf16 v[52:55], v[20:23], a[8:11], v[52:55]\n"
    "s_waitcnt vmcnt(4)\n"
    "v_mfma_f32_16x16x32_bf16 v[36:39], v[22:25], a[12:15], v[36:39]\n"
    "v_mfma_f32_16x16x32_bf16 v[52:55], v[20:23], a[12:15], v[52:55]\n"
    "s_waitcnt vmcnt(3)\n"
    "v_mfma_f32_16x16x32_bf16 v[40:43], v[22:25], a[16:19], v[40:43]\n"
    "v_mfma_f32_16x16x32_bf16 v[56:59], v[20:23], a[16:19], v[56:59]\n"
    "s_waitcnt vmcnt(2)\n"
    "v_mfma_f32_16x16x32_bf16 v[40:43], v[22:25], a[20:23], v[40:43]\n"
    "v_mfma_f32_16x16x32_bf16 v[56:59], v[20:23], a[20:23], v[56:59]\n"
    "s_waitcnt vmcnt(1)\n"
    "v_mfma_f32_16x16x32_bf16 v[44:47], v[22:25], a[24:27], v[44:47]\n"
    "v_mfma_f32_16x16x32_bf16 v[60:63], v[20:23], a[24:27], v[60:63]\n"
    "s_waitcnt vmcnt(0)\n"
    "v_mfma_f32_16x16x32_bf16 v[44:47], v[22:25], a[28:31], v[44:47]\n"
    "v_mfma_f32_16x16x32_bf16 v[60:63], v[20:23], a[28:31], v[60:63]\n"
    "v_cvt_pk_bf16_f32 v204, v32, v33\n"
    "v_cvt_pk_bf16_f32 v205, v34, v35\n"
    "v_cvt_pk_bf16_f32 v206, v36, v37\n"
    "v_cvt_pk_bf16_f32 v207, v38, v39\n"
    "v_cvt_pk_bf16_f32 v208, v48, v49\n"
    "v_cvt_pk_bf16_f32 v209, v50, v51\n"
    "v_cvt_pk_bf16_f32 v210, v52, v53\n"
    "v_cvt_pk_bf16_f32 v211, v54, v55\n"
    "s_nop 0\n"
    "v_permlane16_swap_b32 v204, v206\n"
    "v_permlane16_swap_b32 v205, v207\n"
    "buffer_store_dwordx4 v[204:207], v11, s[16:19], s44 offen offset:0 sc0 sc1\n"
    "v_permlane16_swap_b32 v208, v210\n"
    "v_permlane16_swap_b32 v209, v211\n"
    "buffer_store_dwordx4 v[208:211], v11, s[16:19], s43 offen offset:0 sc0 sc1\n"
    "v_cvt_pk_bf16_f32 v212, v40, v41\n"
    "v_cvt_pk_bf16_f32 v213, v42, v43\n"
    "v_cvt_pk_bf16_f32 v214, v44, v45\n"
    "v_cvt_pk_bf16_f32 v215, v46, v47\n"
    "v_cvt_pk_bf16_f32 v216, v56, v57\n"
    "v_cvt_pk_bf16_f32 v217, v58, v59\n"
    "v_cvt_pk_bf16_f32 v218, v60, v61\n"
    "v_cvt_pk_bf16_f32 v219, v62, v63\n"
    "s_nop 0\n"
    "v_permlane16_swap_b32 v212, v214\n"
    "v_permlane16_swap_b32 v213, v215\n"
    "buffer_store_dwordx4 v[212:215], v11, s[16:19], s44 offen offset:64 sc0 sc1\n"
    "v_permlane16_swap_b32 v216, v218\n"
    "v_permlane16_swap_b32 v217, v219\n"
    "buffer_store_dwordx4 v[216:219], v11, s[16:19], s43 offen offset:64 sc0 sc1\n"
    "s_branch LEND_%=\n"
    "LSP0_%=:\n"
    "s_mov_b32 s76, 524288\n"
    "LSQ0_%=:\n"
    "s_sleep 1\n"
    "buffer_load_dwordx2 v[198:199], v7, s[12:15], 0 offen offset:0 sc1\n"
    "s_waitcnt vmcnt(0)\n"
    "v_cmp_ne_u64 vcc, s[68:69], v[198:199]\n"
    "s_nop 1\n"
    "s_cbranch_vccz LRT0_%=\n"
    "s_sub_u32 s76, s76, 1\n"
    "s_cbranch_scc0 LSQ0_%=\n"
    "s_branch LRT0_%=\n"
    "LSP1_%=:\n"
    "s_mov_b32 s76, 524288\n"
    "LSQ1_%=:\n"
    "s_sleep 1\n"
    "buffer_load_dwordx2 v[198:199], v8, s[12:15], 0 offen offset:64 sc1\n"
    "s_waitcnt vmcnt(0)\n"
    "v_cmp_ne_u64 vcc, s[68:69], v[198:199]\n"
    "s_nop 1\n"
    "s_cbranch_vccz LRT1_%=\n"
    "s_sub_u32 s76, s76, 1\n"
    "s_cbranch_scc0 LSQ1_%=\n"
    "s_branch LRT1_%=\n"
    "LSP2_%=:\n"
    "s_mov_b32 s76, 524288\n"
    "LSQ2_%=:\n"
    "s_sleep 1\n"
    "buffer_load_dwordx2 v[198:199], v19, s[12:15], 0 offen offset:128 sc1\n"
    "s_waitcnt vmcnt(0)\n"
    "v_cmp_ne_u64 vcc, s[68:69], v[198:199]\n"
    "s_nop 1\n"
    "s_cbranch_vccz LRT2_%=\n"
    "s_sub_u32 s76, s76, 1\n"
    "s_cbranch_scc0 LSQ2_%=\n"
    "s_branch LRT2_%=\n"
    "LSP3_%=:\n"
    "s_mov_b32 s76, 524288\n"
    "LSQ3_%=:\n"
    "s_sleep 1\n"
    "buffer_load_dwordx2 v[198:199], v196, s[12:15], 0 offen offset:192 sc1\n"
    "s_waitcnt vmcnt(0)\n"
    "v_cmp_ne_u64 vcc, s[68:69], v[198:199]\n"
    "s_nop 1\n"
    "s_cbranch_vccz LRT3_%=\n"
    "s_sub_u32 s76, s76, 1\n"
    "s_cbranch_scc0 LSQ3_%=\n"
    "s_branch LRT3_%=\n"
    "LPW0_%=:\n"
    "s_memrealtime s[36:37]\n"
    "s_waitcnt lgkmcnt(0)\n"
    "s_sub_u32 s36, s36, s77\n"
    "s_cmp_lt_i32 s36, 0\n"
    "s_cbranch_scc1 LPW0_%=\n"
    "s_branch LPR0_%=\n"
    "LPW1_%=:\n"
    "s_memrealtime s[36:37]\n"
    "s_waitcnt lgkmcnt(0)\n"
    "s_sub_u32 s36, s36, s77\n"
    "s_cmp_lt_i32 s36, 0\n"
    "s_cbranch_scc1 LPW1_%=\n"
    "s_branch LPR1_%=\n"
    "LPW2_%=:\n"
    "s_memrealtime s[36:37]\n"
    "s_waitcnt lgkmcnt(0)\n"
    "s_sub_u32 s36, s36, s77\n"
    "s_cmp_lt_i32 s36, 0\n"
    "s_cbranch_scc1 LPW2_%=\n"
    "s_branch LPR2_%=\n"
    "LPW3_%=:\n"
    "s_memrealtime s[36:37]\n"
    "s_waitcnt lgkmcnt(0)\n"
    "s_sub_u32 s36, s36, s77\n"
    "s_cmp_lt_i32 s36, 0\n"
    "s_cbranch_scc1 LPW3_%=\n"
    "s_branch LPR3_%=\n"
    "LPW4_%=:\n"
    "s_memrealtime s[36:37]\n"
    "s_waitcnt lgkmcnt(0)\n"
    "s_sub_u32 s36, s36, s77\n"
    "s_cmp_lt_i32 s36, 0\n"
    "s_cbranch_scc1 LPW4_%=\n"
    "s_branch LPR4_%=\n"
    "LFB_%=:\n"
    "s_waitcnt lgkmcnt(0)\n"
    "s_mov_b32 s81, -1\n"
    "s_mov_b32 s50, 131072\n"
    "s_mov_b32 s72, s63\n"
    "s_mov_b32 s73, 0\n"
    "s_waitcnt vmcnt(0)\n"
    "s_barrier\n"
    "s_add_u32 m0, s60, s73\n"
    "s_nop 0\n"
    "buffer_load_dwordx4 v5, s[48:51], s72 offen offset:0 nt lds\n"
    "buffer_load_dwordx4 v6, s[48:51], s72 offen offset:1024 nt lds\n"
    "buffer_load_dwordx4 v7, s[48:51], s72 offen offset:2048 nt lds\n"
    "buffer_load_dwordx4 v8, s[48:51], s72 offen offset:3072 lds\n"
    "s_add_u32 s72, s72, 4096\n"
    "s_and_b32 s72, s72, 131071\n"
    "s_add_u32 s73, s73, 4096\n"
    "s_sub_u32 s74, s73, 40960\n"
    "s_min_u32 s73, s73, s74\n"
    "s_add_u32 m0, s60, s73\n"
    "s_nop 0\n"
    "buffer_load_dwordx4 v5, s[48:51], s72 offen offset:0 nt lds\n"
    "buffer_load_dwordx4 v6, s[48:51], s72 offen offset:1024 nt lds\n"
    "buffer_load_dwordx4 v7, s[48:51], s72 offen offset:2048 nt lds\n"
    "buffer_load_dwordx4 v8, s[48:51], s72 offen offset:3072 lds\n"
    "s_add_u32 s72, s72, 4096\n"
    "s_and_b32 s72, s72, 131071\n"
    "s_add_u32 s73, s73, 4096\n"
    "s_sub_u32 s74, s73, 40960\n"
    "s_min_u32 s73, s73, s74\n"
    "s_add_u32 m0, s60, s73\n"
    "s_nop 0\n"
    "buffer_load_dwordx4 v5, s[48:51], s72 offen offset:0 nt lds\n"
    "buffer_load_dwordx4 v6, s[48:51], s72 offen offset:1024 nt lds\n"
    "buffer_load_dwordx4 v7, s[48:51], s72 offen offset:2048 nt lds\n"
    "buffer_load_dwordx4 v8, s[48:51], s72 offen offset:3072 lds\n"
    "s_add_u32 s72, s72, 4096\n"
    "s_and_b32 s72, s72, 131071\n"
    "s_add_u32 s73, s73, 4096\n"
    "s_sub_u32 s74, s73, 40960\n"
    "s_min_u32 s73, s73, s74\n"
    "s_add_u32 m0, s60, s73\n"
    "s_nop 0\n"
    "buffer_load_dwordx4 v5, s[48:51], s72 offen offset:0 nt lds\n"
    "buffer_load_dwordx4 v6, s[48:51], s72 offen offset:1024 nt lds\n"
    "buffer_load_dwordx4 v7, s[48:51], s72 offen offset:2048 nt lds\n"
    "buffer_load_dwordx4 v8, s[48:51], s72 offen offset:3072 lds\n"
    "s_add_u32 s72, s72, 4096\n"
    "s_and_b32 s72, s72, 131071\n"
    "s_add_u32 s73, s73, 4096\n"
    "s_sub_u32 s74, s73, 40960\n"
    "s_min_u32 s73, s73, s74\n"
    "s_add_u32 m0, s60, s73\n"
    "s_nop 0\n"
    "buffer_load_dwordx4 v5, s[48:51], s72 offen offset:0 nt lds\n"
    "buffer_load_dwordx4 v6, s[48:51], s72 offen offset:1024 nt lds\n"
    "buffer_load_dwordx4 v7, s[48:51], s72 offen offset:2048 nt lds\n"
    "buffer_load_dwordx4 v8, s[48:51], s72 offen offset:3072 lds\n"
    "s_add_u32 s72, s72, 4096\n"
    "s_and_b32 s72, s72, 131071\n"
    "s_add_u32 s73, s73, 4096\n"
    "s_sub_u32 s74, s73, 40960\n"
    "s_min_u32 s73, s73, s74\n"
    "s_add_u32 m0, s60, s73\n"
    "s_nop 0\n"
    "buffer_load_dwordx4 v5, s[48:51], s72 offen offset:0 nt lds\n"
    "buffer_load_dwordx4 v6, s[48:51], s72 offen offset:1024 nt lds\n"
    "buffer_load_dwordx4 v7, s[48:51], s72 offen offset:2048 nt lds\n"
    "buffer_load_dwordx4 v8, s[48:51], s72 offen offset:3072 lds\n"
    "s_add_u32 s72, s72, 4096\n"
    "s_and_b32 s72, s72, 131071\n"
    "s_add_u32 s73, s73, 4096\n"
    "s_sub_u32 s74, s73, 40960\n"
    "s_min_u32 s73, s73, s74\n"
    "s_add_u32 m0, s60, s73\n"
    "s_nop 0\n"
    "buffer_load_dwordx4 v5, s[48:51], s72 offen offset:0 nt lds\n"
    "buffer_load_dwordx4 v6, s[48:51], s72 offen offset:1024 nt lds\n"
    "buffer_load_dwordx4 v7, s[48:51], s72 offen offset:2048 nt lds\n"
    "buffer_load_dwordx4 v8, s[48:51], s72 offen offset:3072 lds\n"
    "s_add_u32 s72, s72, 4096\n"
    "s_and_b32 s72, s72, 131071\n"
    "s_add_u32 s73, s73, 4096\n"
    "s_sub_u32 s74, s73, 40960\n"
    "s_min_u32 s73, s73, s74\n"
    "s_add_u32 m0, s60, s73\n"
    "s_nop 0\n"
    "buffer_load_dwordx4 v5, s[48:51], s72 offen offset:0 nt lds\n"
    "buffer_load_dwordx4 v6, s[48:51], s72 offen offset:1024 nt lds\n"
    "buffer_load_dwordx4 v7, s[48:51], s72 offen offset:2048 nt lds\n"
    "buffer_load_dwordx4 v8, s[48:51], s72 offen offset:3072 lds\n"
    "s_add_u32 s72, s72, 4096\n"
    "s_and_b32 s72, s72, 131071\n"
    "s_add_u32 s73, s73, 4096\n"
    "s_sub_u32 s74, s73, 40960\n"
    "s_min_u32 s73, s73, s74\n"
    "s_add_u32 m0, s60, s73\n"
    "s_nop 0\n"
    "buffer_load_dwordx4 v5, s[48:51], s72 offen offset:0 nt lds\n"
    "buffer_load_dwordx4 v6, s[48:51], s72 offen offset:1024 nt lds\n"
    "buffer_load_dwordx4 v7, s[48:51], s72 offen offset:2048 nt lds\n"
    "buffer_load_dwordx4 v8, s[48:51], s72 offen offset:3072 lds\n"
    "s_add_u32 s72, s72, 4096\n"
    "s_and_b32 s72, s72, 131071\n"
    "s_add_u32 s73, s73, 4096\n"
    "s_sub_u32 s74, s73, 40960\n"
    "s_min_u32 s73, s73, s74\n"
    "s_add_u32 m0, s60, s73\n"
    "s_nop 0\n"
    "buffer_load_dwordx4 v5, s[48:51], s72 offen offset:0 nt lds\n"
    "buffer_load_dwordx4 v6, s[48:51], s72 offen offset:1024 nt lds\n"
    "buffer_load_dwordx4 v7, s[48:51], s72 offen offset:2048 nt lds\n"
    "buffer_load_dwordx4 v8, s[48:51], s72 offen offset:3072 lds\n"
    "s_add_u32 s72, s72, 4096\n"
    "s_and_b32 s72, s72, 131071\n"
    "s_add_u32 s73, s73, 4096\n"
    "s_sub_u32 s74, s73, 40960\n"
    "s_min_u32 s73, s73, s74\n"
    "v_and_b32 v244, 240, v0\n"
    "v_lshlrev_b32 v244, 4, v244\n"
    "v_lshrrev_b32 v245, 4, v0\n"
    "v_and_b32 v245, 32, v245\n"
    "v_or_b32 v244, v244, v245\n"
    "v_lshrrev_b32 v245, 4, v0\n"
    "v_and_b32 v245, 16, v245\n"
    "v_or_b32 v244, v244, v245\n"
    "v_mov_b32 v228, 4286578688\n"
    "v_mov_b32 v229, 4286578688\n"
    "v_mov_b32 v230, 4286578688\n"
    "v_mov_b32 v231, 4286578688\n"
    "v_mov_b32 v232, 4286578688\n"
    "v_mov_b32 v233, 4286578688\n"
    "v_mov_b32 v234, 4286578688\n"
    "s_mov_b32 s83, 0\n"
    "LF_%=:\n"
    "buffer_load_dwordx4 v[172:175], v244, s[56:59], s83 offen offset:0\n"
    "buffer_load_dwordx4 v[176:179], v244, s[56:59], s83 offen offset:64\n"
    "buffer_load_dwordx4 v[180:183], v244, s[56:59], s83 offen offset:128\n"
    "buffer_load_dwordx4 v[184:187], v244, s[56:59], s83 offen offset:192\n"
    "s_waitcnt vmcnt(0)\n"
    "v_mfma_f32_16x16x32_bf16 v[204:207], v[172:175], v[32:35], 0\n"
    "v_mfma_f32_16x16x32_bf16 v[204:207], v[176:179], v[36:39], v[204:207]\n"
    "v_mfma_f32_16x16x32_bf16 v[204:207], v[180:183], v[40:43], v[204:207]\n"
    "v_mfma_f32_16x16x32_bf16 v[204:207], v[184:187], v[44:47], v[204:207]\n"
    "s_nop 7\n"
    "v_max3_f32 v228, v228, v204, v205\n"
    "v_max3_f32 v228, v228, v206, v207\n"
    "v_mfma_f32_16x16x32_bf16 v[204:207], v[172:175], v[48:51], 0\n"
    "v_mfma_f32_16x16x32_bf16 v[204:207], v[176:179], v[52:55], v[204:207]\n"
    "v_mfma_f32_16x16x32_bf16 v[204:207], v[180:183], v[56:59], v[204:207]\n"
    "v_mfma_f32_16x16x32_bf16 v[204:207], v[184:187], v[60:63], v[204:207]\n"
    "s_nop 7\n"
    "v_max3_f32 v229, v229, v204, v205\n"
    "v_max3_f32 v229, v229, v206, v207\n"
    "v_mfma_f32_16x16x32_bf16 v[204:207], v[172:175], v[64:67], 0\n"
    "v_mfma_f32_16x16x32_bf16 v[204:207], v[176:179], v[68:71], v[204:207]\n"
    "v_mfma_f32_16x16x32_bf16 v[204:207], v[180:183], v[72:75], v[204:207]\n"
    "v_mfma_f32_16x16x32_bf16 v[204:207], v[184:187], v[76:79], v[204:207]\n"
    "s_nop 7\n"
    "v_max3_f32 v230, v230, v204, v205\n"
    "v_max3_f32 v230, v230, v206, v207\n"
    "v_mfma_f32_16x16x32_bf16 v[204:207], v[172:175], v[80:83], 0\n"
    "v_mfma_f32_16x16x32_bf16 v[204:207], v[176:179], v[84:87], v[204:207]\n"
    "v_mfma_f32_16x16x32_bf16 v[204:207], v[180:183], v[88:91], v[204:207]\n"
    "v_mfma_f32_16x16x32_bf16 v[204:207], v[184:187], v[92:95], v[204:207]\n"
    "s_nop 7\n"
    "v_max3_f32 v231, v231, v204, v205\n"
    "v_max3_f32 v231, v231, v206, v207\n"
    "v_mfma_f32_16x16x32_bf16 v[204:207], v[172:175], v[96:99], 0\n"
    "v_mfma_f32_16x16x32_bf16 v[204:207], v[176:179], v[100:103], v[204:207]\n"
    "v_mfma_f32_16x16x32_bf16 v[204:207], v[180:183], v[104:107], v[204:207]\n"
    "v_mfma_f32_16x16x32_bf16 v[204:207], v[184:187], v[108:111], v[204:207]\n"
    "s_nop 7\n"
    "v_max3_f32 v232, v232, v204, v205\n"
    "v_max3_f32 v232, v232, v206, v207\n"
    "v_mfma_f32_16x16x32_bf16 v[204:207], v[172:175], v[112:115], 0\n"
    "v_mfma_f32_16x16x32_bf16 v[204:207], v[176:179], v[116:119], v[204:207]\n"
    "v_mfma_f32_16x16x32_bf16 v[204:207], v[180:183], v[120:123], v[204:207]\n"
    "v_mfma_f32_16x16x32_bf16 v[204:207], v[184:187], v[124:127], v[204:207]\n"
    "s_nop 7\n"
    "v_max3_f32 v233, v233, v204, v205\n"
    "v_max3_f32 v233, v233, v206, v207\n"
    "v_mfma_f32_16x16x32_bf16 v[204:207], v[172:175], v[128:131], 0\n"
    "v_mfma_f32_16x16x32_bf16 v[204:207], v[176:179], v[132:135], v[204:207]\n"
    "v_mfma_f32_16x16x32_bf16 v[204:207], v[180:183], v[136:139], v[204:207]\n"
    "v_mfma_f32_16x16x32_bf16 v[204:207], v[184:187], v[140:143], v[204:207]\n"
    "s_nop 7\n"
    "v_max3_f32 v234, v234, v204, v205\n"
    "v_max3_f32 v234, v234, v206, v207\n"
    "s_add_u32 s83, s83, 4096\n"
    "s_cmp_lt_u32 s83, 131072\n"
    "s_cbranch_scc1 LF_%=\n"
    "v_mov_b32 v245, v228\n"
    "s_nop 1\n"
    "v_permlane32_swap_b32 v228, v245\n"
    "v_max_f32 v228, v228, v245\n"
    "v_mov_b32 v245, v228\n"
    "s_nop 1\n"
    "v_permlane16_swap_b32 v228, v245\n"
    "v_max_f32 v228, v228, v245\n"
    "v_sub_f32 v144, 0, v228\n"
    "v_sub_f32 v145, 0, v228\n"
    "v_sub_f32 v146, 0, v228\n"
    "v_sub_f32 v147, 0, v228\n"
    "v_mov_b32 v245, v229\n"
    "s_nop 1\n"
    "v_permlane32_swap_b32 v229, v245\n"
    "v_max_f32 v229, v229, v245\n"
    "v_mov_b32 v245, v229\n"
    "s_nop 1\n"
    "v_permlane16_swap_b32 v229, v245\n"
    "v_max_f32 v229, v229, v245\n"
    "v_sub_f32 v148, 0, v229\n"
    "v_sub_f32 v149, 0, v229\n"
    "v_sub_f32 v150, 0, v229\n"
    "v_sub_f32 v151, 0, v229\n"
    "v_mov_b32 v245, v230\n"
    "s_nop 1\n"
    "v_permlane32_swap_b32 v230, v245\n"
    "v_max_f32 v230, v230, v245\n"
    "v_mov_b32 v245, v230\n"
    "s_nop 1\n"
    "v_permlane16_swap_b32 v230, v245\n"
    "v_max_f32 v230, v230, v245\n"
    "v_sub_f32 v152, 0, v230\n"
    "v_sub_f32 v153, 0, v230\n"
    "v_sub_f32 v154, 0, v230\n"
    "v_sub_f32 v155, 0, v230\n"
    "v_mov_b32 v245, v231\n"
    "s_nop 1\n"
    "v_permlane32_swap_b32 v231, v245\n"
    "v_max_f32 v231, v231, v245\n"
    "v_mov_b32 v245, v231\n"
    "s_nop 1\n"
    "v_permlane16_swap_b32 v231, v245\n"
    "v_max_f32 v231, v231, v245\n"
    "v_sub_f32 v156, 0, v231\n"
    "v_sub_f32 v157, 0, v231\n"
    "v_sub_f32 v158, 0, v231\n"
    "v_sub_f32 v159, 0, v231\n"
    "v_mov_b32 v245, v232\n"
    "s_nop 1\n"
    "v_permlane32_swap_b32 v232, v245\n"
    "v_max_f32 v232, v232, v245\n"
    "v_mov_b32 v245, v232\n"
    "s_nop 1\n"
    "v_permlane16_swap_b32 v232, v245\n"
    "v_max_f32 v232, v232, v245\n"
    "v_sub_f32 v160, 0, v232\n"
    "v_sub_f32 v161, 0, v232\n"
    "v_sub_f32 v162, 0, v232\n"
    "v_sub_f32 v163, 0, v232\n"
    "v_mov_b32 v245, v233\n"
    "s_nop 1\n"
    "v_permlane32_swap_b32 v233, v245\n"
    "v_max_f32 v233, v233, v245\n"
    "v_mov_b32 v245, v233\n"
    "s_nop 1\n"
    "v_permlane16_swap_b32 v233, v245\n"
    "v_max_f32 v233, v233, v245\n"
    "v_sub_f32 v164, 0, v233\n"
    "v_sub_f32 v165, 0, v233\n"
    "v_sub_f32 v166, 0, v233\n"
    "v_sub_f32 v167, 0, v233\n"
    "v_mov_b32 v245, v234\n"
    "s_nop 1\n"
    "v_permlane32_swap_b32 v234, v245\n"
    "v_max_f32 v234, v234, v245\n"
    "v_mov_b32 v245, v234\n"
    "s_nop 1\n"
    "v_permlane16_swap_b32 v234, v245\n"
    "v_max_f32 v234, v234, v245\n"
    "v_sub_f32 v168, 0, v234\n"
    "v_sub_f32 v169, 0, v234\n"
    "v_sub_f32 v170, 0, v234\n"
    "v_sub_f32 v171, 0, v234\n"
    "s_barrier\n"
    "ds_read_b128 a[224:227], v20 offset:0\n"
    "ds_read_b128 a[228:231], v21 offset:0\n"
    "ds_read_b128 a[232:235], v22 offset:0\n"
    "ds_read_b128 a[236:239], v23 offset:0\n"
    "ds_read_b128 a[240:243], v20 offset:4096\n"
    "ds_read_b128 a[244:247], v21 offset:4096\n"
    "ds_read_b128 a[248:251], v22 offset:4096\n"
    "ds_read_b128 a[252:255], v23 offset:4096\n"
    "s_waitcnt lgkmcnt(0)\n"
    "s_branch LATT_%=\n"
    "LEND_%=:\n"

    : "+{v[0:15]}"(va), "+{v[16:31]}"(vb), "+{a[120:135]}"(pk)
    : "{s[40:43]}"(pW), "{s[44:47]}"(pX), "{s[48:49]}"(pKV), "{s[56:57]}"(pK), "{s[8:9]}"(pWO), "{s[12:13]}"(pPB), "{s[16:17]}"(pOUT), "{s[66:67]}"(pD),
      "{s[50:51]}"(qKV), "{s[58:59]}"(qK), "{s[10:11]}"(qWO), "{s[14:15]}"(qPB), "{s[18:19]}"(qOUT),
      "{s[52:55]}"(lw), "{s[20:23]}"(sj), "{s[60:63]}"(sm), "{s[64:65]}"(sn)
    : "v32", "v33", "v34", "v35", "v36", "v37", "v38", "v39", "v40", "v41", "v42", "v43", "v44", "v45", "v46", "v47", "v48", "v49", "v50", "v51", "v52", "v53", "v54", "v55", "v56", "v57", "v58", "v59", "v60", "v61", "v62", "v63", "v64", "v65", "v66", "v67", "v68", "v69", "v70", "v71", "v72", "v73", "v74", "v75", "v76", "v77", "v78", "v79", "v80", "v81", "v82", "v83", "v84", "v85", "v86", "v87", "v88", "v89", "v90", "v91", "v92", "v93", "v94", "v95", "v96", "v97", "v98", "v99", "v100", "v101", "v102", "v103", "v104", "v105", "v106", "v107", "v108", "v109", "v110", "v111", "v112", "v113", "v114", "v115", "v116", "v117", "v118", "v119", "v120", "v121", "v122", "v123", "v124", "v125", "v126", "v127", "v128", "v129", "v130", "v131", "v132", "v133", "v134", "v135", "v136", "v137", "v138", "v139", "v140", "v141", "v142", "v143", "v144", "v145", "v146", "v147", "v148", "v149", "v150", "v151", "v152", "v153", "v154", "v155", "v156", "v157", "v158", "v159", "v160", "v161", "v162", "v163", "v164", "v165", "v166", "v167", "v168", "v169", "v170", "v171", "v172", "v173", "v174", "v175", "v176", "v177", "v178", "v179", "v180", "v181", "v182", "v183", "v184", "v185", "v186", "v187", "v188", "v189", "v190", "v191", "v192", "v193", "v194", "v195", "v196", "v197", "v198", "v199", "v200", "v201", "v202", "v203", "v204", "v205", "v206", "v207", "v208", "v209", "v210", "v211", "v212", "v213", "v214", "v215", "v216", "v217", "v218", "v219", "v220", "v221", "v222", "v223", "v224", "v225", "v226", "v227", "v228", "v229", "v230", "v231", "v232", "v233", "v234", "v235", "v236", "v237", "v238", "v239", "v240", "v241", "v242", "v243", "v244", "v245", "v246", "v247", "v248", "v249", "v250", "v251", "v252", "v253", "v254", "v255", "a0", "a1", "a2", "a3", "a4", "a5", "a6", "a7", "a8", "a9", "a10", "a11", "a12", "a13", "a14", "a15", "a16", "a17", "a18", "a19", "a20", "a21", "a22", "a23", "a24", "a25", "a26", "a27", "a28", "a29", "a30", "a31", "a32", "a33", "a34", "a35", "a36", "a37", "a38", "a39", "a40", "a41", "a42", "a43", "a44", "a45", "a46", "a47", "a48", "a49", "a50", "a51", "a52", "a53", "a54", "a55", "a56", "a57", "a58", "a59", "a60", "a61", "a62", "a63", "a64", "a65", "a66", "a67", "a68", "a69", "a70", "a71", "a72", "a73", "a74", "a75", "a76", "a77", "a78", "a79", "a80", "a81", "a82", "a83", "a84", "a85", "a86", "a87", "a88", "a89", "a90", "a91", "a92", "a93", "a94", "a95", "a96", "a97", "a98", "a99", "a100", "a101", "a102", "a103", "a104", "a105", "a106", "a107", "a108", "a109", "a110", "a111", "a112", "a113", "a114", "a115", "a116", "a117", "a118", "a119", "a136", "a137", "a138", "a139", "a140", "a141", "a142", "a143", "a144", "a145", "a146", "a147", "a148", "a149", "a150", "a151", "a152", "a153", "a154", "a155", "a156", "a157", "a158", "a159", "a160", "a161", "a162", "a163", "a164", "a165", "a166", "a167", "a168", "a169", "a170", "a171", "a172", "a173", "a174", "a175", "a176", "a177", "a178", "a179", "a180", "a181", "a182", "a183", "a184", "a185", "a186", "a187", "a188", "a189", "a190", "a191", "a192", "a193", "a194", "a195", "a196", "a197", "a198", "a199", "a200", "a201", "a202", "a203", "a204", "a205", "a206", "a207", "a208", "a209", "a210", "a211", "a212", "a213", "a214", "a215", "a216", "a217", "a218", "a219", "a220", "a221", "a222", "a223", "a224", "a225", "a226", "a227", "a228", "a229", "a230", "a231", "a232", "a233", "a234", "a235", "a236", "a237", "a238", "a239", "a240", "a241", "a242", "a243", "a244", "a245", "a246", "a247", "a248", "a249", "a250", "a251", "a252", "a253", "a254", "a255", "s24", "s25", "s26", "s27", "s28", "s29", "s30", "s31", "s36", "s37", "s38", "s39", "s68", "s69", "s70", "s71", "s72", "s73", "s74", "s75", "s76", "s77", "s78", "s79", "s80", "s81", "s82", "s83", "s84", "s85", "s86", "s87", "s88", "s89", "s90", "s91", "s92", "s93", "s94", "s95", "s96", "s97", "s98", "s99", "vcc", "scc", "memory");
}

extern "C" void kernel_launch(void* const* d_in, const int* in_sizes, int n_in, void* d_out, int out_size, void* d_ws, size_t ws_size, hipStream_t stream) {
  Args A;
  for (int p = 0; p < 2; ++p) {
    A.a[p][0] = d_in[5 * p + 0]; A.a[p][1] = d_in[5 * p + 3]; A.a[p][2] = d_in[5 * p + 1]; A.a[p][3] = d_in[5 * p + 2];
    A.a[p][4] = d_in[5 * p + 4]; A.a[p][5] = d_ws; A.a[p][6] = d_out; A.a[p][7] = d_ws;
  }
  (void)hipFuncSetAttribute((const void*)fused_kernel, hipFuncAttributeMaxDynamicSharedMemorySize, 163840);
  fused_kernel<<<256, 256, 163840, stream>>>(A);
}
```
